```python
import torch

def _softmax(x, axis=-1):
    return torch.softmax(x, dim=axis)

def _gelu(x):
    return torch.nn.functional.gelu(x, approximate='tanh')

def _silu(x):
    return torch.nn.functional.silu(x)

def _sigmoid(x):
    return torch.sigmoid(x)

def _layernorm(x, axis=-1, eps=1e-05):
    m = x.mean(dim=axis, keepdim=True)
    v = x.var(dim=axis, keepdim=True, unbiased=False)
    return (x - m) / torch.sqrt(v + eps)

def _scatter_add(tgt, idx, src, axis=0):
    return tgt.clone().index_add_(axis, idx, src)

def _segment_sum(src, seg, n):
    out = torch.zeros((n,) + src.shape[1:], dtype=src.dtype)
    return out.index_add_(0, seg, src)

def _pad(x, pw):
    flat = tuple((v for pair in reversed(pw) for v in pair))
    return torch.nn.functional.pad(x, flat)
kernel_name = 'parallel_sum__linear_attn_block__mixer_gmlp__prompt'

def setup_inputs():

    _w = lambda a: (lambda t: t.to(torch.bfloat16) if t.is_floating_point() else t)(torch.as_tensor(a))

    def _A_setup():
        B, S, D = (1, 2048, 640)
        return {'x': _w((lambda *s: torch.randn(*s, dtype=torch.bfloat16) * 0.05)(B, S, D)), 'wqkv': _w((lambda *s: torch.randn(*s, dtype=torch.bfloat16) * 0.8)(D, 3 * D)), 'wo': _w((lambda *s: torch.randn(*s, dtype=torch.bfloat16) * 0.2)(D, D))}
    _A_in = _A_setup()

    def _B_setup():
        B, S, D, F = (1, 2048, 640, 1536)
        Fh = F // 2
        return {'x': _w((lambda *s: torch.randn(*s, dtype=torch.bfloat16) * 0.05)(B, S, D)), 'w_in': _w((lambda *s: torch.randn(*s, dtype=torch.bfloat16) * 0.4)(D, 2 * Fh)), 'w_sp': _w(0.01 * (lambda *s: torch.randn(*s, dtype=torch.bfloat16) * 0.4)(S, S)), 'b_sp': _w(1.0 + 0.01 * (lambda *s: torch.randn(*s, dtype=torch.bfloat16) * 0.05)(S)), 'w_out': _w((lambda *s: torch.randn(*s, dtype=torch.bfloat16) * 0.05)(Fh, D))}
    _B_in = _B_setup()
    return {'A_x': _A_in['x'], 'A_wqkv': _A_in['wqkv'], 'A_wo': _A_in['wo'], 'B_x': _B_in['x'], 'B_w_in': _B_in['w_in'], 'B_w_sp': _B_in['w_sp'], 'B_b_sp': _B_in['b_sp'], 'B_w_out': _B_in['w_out']}

def reference(A_x, A_wqkv, A_wo, B_x, B_w_in, B_w_sp, B_b_sp, B_w_out):

    def _A_ref(x, wqkv, wo):
        B, S, D = x.shape
        H = 32
        Dh = D // H
        qkv = (lambda x, *d: x.permute(*d))((x @ wqkv).reshape(B, S, 3, H, Dh), 2, 0, 3, 1, 4)
        q, k, v = (qkv[0], qkv[1], qkv[2])
        phi = lambda t: torch.where(t > 0, t + 1, torch.exp(t)) + 1e-06
        pq, pk = (phi(q), phi(k))
        kv = torch.einsum('bhsd,bhse->bhde', pk, v)
        z = torch.einsum('bhsd->bhd', pk)
        num = torch.einsum('bhqd,bhde->bhqe', pq, kv)
        den = torch.einsum('bhqd,bhd->bhq', pq, z)[..., None] + 1e-06
        o = num / den
        return (lambda x, *d: x.permute(*d))(o, 0, 2, 1, 3).reshape(B, S, D) @ wo

    def _B_ref(x, w_in, w_sp, b_sp, w_out):
        B, S, D = x.shape
        F = w_in.shape[-1]
        Fh = F // 2
        z = _gelu(torch.einsum('bsd,df->bsf', x, w_in))
        z1 = z[..., :Fh].clone()
        z2 = _layernorm(z[..., Fh:], axis=-1)
        g = torch.einsum('ts,bsf->btf', w_sp, z2) + b_sp[None, :, None]
        return torch.einsum('bsf,fd->bsd', z1 * g, w_out)
    x = A_x
    xn = _layernorm(x, axis=-1)
    return x + _A_ref(xn, A_wqkv, A_wo) + _B_ref(xn, B_w_in, B_w_sp, B_b_sp, B_w_out)
```

```cpp
#include <hip/hip_runtime.h>
#include <hip/hip_bf16.h>
#include <stdint.h>

typedef __attribute__((ext_vector_type(8))) short bf16x8;
typedef __attribute__((ext_vector_type(4))) short bf16x4;
typedef __attribute__((ext_vector_type(4))) float f32x4;
typedef __attribute__((ext_vector_type(2))) __bf16 bf2;
typedef __attribute__((ext_vector_type(4))) unsigned u32x4;
typedef __attribute__((ext_vector_type(2))) unsigned u32x2;
typedef unsigned short u16;
typedef unsigned long long u64;
#define LAS __attribute__((address_space(3)))
#define GAS __attribute__((address_space(1)))
#define DI __device__ __forceinline__
#define RLX_AGENT __ATOMIC_RELAXED, __HIP_MEMORY_SCOPE_AGENT

constexpr int S_ = 2048, D_ = 640, NQKV = 1920, FIN = 1536, FH = 768;

constexpr unsigned WS_PQ  = 0;
constexpr unsigned WS_Z1  = WS_PQ + 16 * 8 * 8 * 3 * 1024;
constexpr unsigned WS_Z2N = WS_Z1 + 16 * 8 * 24 * 1024;
constexpr unsigned WS_OH  = WS_Z2N + 8 * 64 * 6 * 1024;
constexpr unsigned WS_KVT = WS_OH + 32 * 44 * 4 * 1024;
constexpr unsigned WS_KVP = WS_KVT + 8 * 18 * 1024;
constexpr unsigned WS_GR  = WS_KVP + 16 * 32 * 420 * 4;
constexpr unsigned WS_F1  = WS_GR + 16 * 8 * 256 * 8;
constexpr unsigned WS_FO  = WS_F1 + 16 * 24 * 64;
constexpr unsigned WS_F2  = WS_FO + 256 * 64;
constexpr unsigned WS_FK  = WS_F2 + 256 * 64;
constexpr unsigned WS_EP  = WS_FK + 32 * 64;
constexpr unsigned WS_END = WS_EP + 64;

#define STAMP(i)
#define STAMPC(i)

DI float bf2f(u16 v) { return __uint_as_float(((unsigned)v) << 16); }
DI unsigned cvt_pk(float lo, float hi) {
  bf2 r; r[0] = (__bf16)lo; r[1] = (__bf16)hi; return __builtin_bit_cast(unsigned, r);
}
DI float rbf(float x) { return (float)((__bf16)x); }
DI float lo16(unsigned u) { return __uint_as_float(u << 16); }
DI float hi16(unsigned u) { return __uint_as_float(u & 0xffff0000u); }

DI void glds16(const void* gbase, unsigned voff, unsigned lds_dst) {
  unsigned keep;
  asm volatile("s_mov_b32 %0, m0\n\ts_mov_b32 m0, %3\n\ts_nop 0\n\tglobal_load_lds_dwordx4 %1, %2\n\ts_mov_b32 m0, %0"
               : "=&s"(keep) : "v"(voff), "s"(gbase), "s"(lds_dst) : "memory");
}
DI void glds16c(const void* gbase, unsigned voff, unsigned lds_dst) {
  unsigned keep;
  asm volatile("s_mov_b32 %0, m0\n\ts_mov_b32 m0, %3\n\ts_nop 0\n\tglobal_load_lds_dwordx4 %1, %2 sc1\n\ts_mov_b32 m0, %0"
               : "=&s"(keep) : "v"(voff), "s"(gbase), "s"(lds_dst) : "memory");
}
template <int N> DI void wait_vm() { asm volatile("s_waitcnt vmcnt(%0)" :: "n"(N) : "memory"); }
DI void bar() { asm volatile("s_waitcnt lgkmcnt(0)" ::: "memory"); __builtin_amdgcn_s_barrier(); asm volatile("" ::: "memory"); }

DI bf16x8 lds_b128(unsigned addr) { return *(LAS bf16x8*)(uintptr_t)addr; }
DI bf16x8 lds_2b64(unsigned addr0, unsigned addr1) {
  bf16x4 a = *(LAS bf16x4*)(uintptr_t)addr0, b = *(LAS bf16x4*)(uintptr_t)addr1;
  return __builtin_shufflevector(a, b, 0, 1, 2, 3, 4, 5, 6, 7);
}
DI bf16x8 lds_tr(unsigned addr0, unsigned addr1) {
  bf16x4 a = __builtin_amdgcn_ds_read_tr16_b64_v4i16((LAS bf16x4*)(uintptr_t)addr0);
  bf16x4 b = __builtin_amdgcn_ds_read_tr16_b64_v4i16((LAS bf16x4*)(uintptr_t)addr1);
  return __builtin_shufflevector(a, b, 0, 1, 2, 3, 4, 5, 6, 7);
}
DI float ldsf(unsigned a) { return *(LAS float*)(uintptr_t)a; }
DI void stsf(unsigned a, float v) { *(LAS float*)(uintptr_t)a = v; }
DI f32x4 ldsf4(unsigned a) { return *(LAS f32x4*)(uintptr_t)a; }
DI void stsf4(unsigned a, f32x4 v) { *(LAS f32x4*)(uintptr_t)a = v; }
#define MFMA(a, b, c) __builtin_amdgcn_mfma_f32_16x16x32_bf16((a), (b), (c), 0, 0, 0)

DI float gelu_t(float x) {
  float u = x * (0.7978845608f + 0.0356774081f * x * x);
  float e = __builtin_amdgcn_exp2f(-2.885390082f * u);
  return x * __builtin_amdgcn_rcpf(1.0f + e);
}
DI float phi_f(float t) {
  float r = t > 0.f ? rbf(t + 1.0f) : rbf(__builtin_amdgcn_exp2f(t * 1.4426950409f));
  return r + 1e-6f;
}

typedef __amdgpu_buffer_rsrc_t rsrc_t;
DI void st16(rsrc_t r, unsigned off, u32x4 v) { __builtin_amdgcn_raw_buffer_store_b128(v, r, off, 0, 16); }
DI u32x4 ld16(rsrc_t r, unsigned off) { return __builtin_amdgcn_raw_buffer_load_b128(r, off, 0, 16); }
DI void st8(char* ws, unsigned off, u32x2 v) { __hip_atomic_store((GAS u64*)(ws + off), ((u64)v[1] << 32) | v[0], RLX_AGENT); }
DI void st8u(char* ws, unsigned off, u64 v) { __hip_atomic_store((GAS u64*)(ws + off), v, RLX_AGENT); }
DI void st4(char* ws, unsigned off, float v) { __hip_atomic_store((GAS unsigned*)(ws + off), __float_as_uint(v), RLX_AGENT); }
DI u64 ld8(const char* ws, unsigned off) { return __hip_atomic_load((GAS u64*)(ws + off), RLX_AGENT); }
DI float ld4(const char* ws, unsigned off) { return __uint_as_float(__hip_atomic_load((GAS unsigned*)(ws + off), RLX_AGENT)); }
constexpr unsigned SPIN_CAP = 1u << 20;
DI void wait_words(const char* ws, unsigned off, bool act, u64 tag) {
#pragma unroll 1
  for (unsigned sp = 0; sp < SPIN_CAP; ++sp) {
    u64 v = act ? ld8(ws, off) : tag;
    if (__all(v == tag)) break;
    __builtin_amdgcn_s_sleep(1);
  }
  __builtin_amdgcn_fence(__ATOMIC_ACQUIRE, "wavefront");
}

struct Params { const u16 *x, *wqkv, *wo, *win, *wsp, *bsp, *wout; u16* out; char* ws; };

constexpr unsigned K1_STAGE = 47104;
constexpr unsigned L_MISC = 153600;
constexpr int LDS_TOTAL = 163840;
DI unsigned la2(int j) { return 47104u + j * 8192u; }
DI unsigned lb2(int j) { return j < 3 ? j * 12288u : 96256u + (j - 3) * 12288u; }
constexpr unsigned L_KVT = 133120;
constexpr unsigned P3_SLOT = 36864;

DI void row_stats(unsigned sb, int tid, float& s0, float& q0, float& s1, float& q1) {
  const bf2 ones2 = __builtin_bit_cast(bf2, 0x3F803F80u);
  u32x4 r0 = *(LAS u32x4*)(uintptr_t)(sb + tid * 16);
  u32x4 r1 = *(LAS u32x4*)(uintptr_t)(sb + 8192 + tid * 16);
#pragma unroll
  for (int j = 0; j < 4; ++j) {
    unsigned ua = r0[j], ub = r1[j];
    bf2 a = __builtin_bit_cast(bf2, ua), b = __builtin_bit_cast(bf2, ub);
    s0 = __builtin_amdgcn_fdot2_f32_bf16(a, ones2, s0, false);
    q0 = __builtin_amdgcn_fdot2_f32_bf16(a, a, q0, false);
    s1 = __builtin_amdgcn_fdot2_f32_bf16(b, ones2, s1, false);
    q1 = __builtin_amdgcn_fdot2_f32_bf16(b, b, q1, false);
  }
}
DI void row_stats_finish(unsigned L_ST, int tid, float s0, float q0, float s1, float q1) {
#pragma unroll
  for (int o = 1; o < 8; o <<= 1) {
    s0 += __shfl_xor(s0, o); q0 += __shfl_xor(q0, o); s1 += __shfl_xor(s1, o); q1 += __shfl_xor(q1, o);
  }
  if ((tid & 7) == 0) {
    float m0 = s0 * (1.f / D_), m1 = s1 * (1.f / D_);
    float v0 = rbf(fmaxf(q0 * (1.f / D_) - m0 * m0, 0.f)), v1 = rbf(fmaxf(q1 * (1.f / D_) - m1 * m1, 0.f));
    float d0 = rbf(sqrtf(rbf(v0 + 1e-5f))), d1 = rbf(sqrtf(rbf(v1 + 1e-5f)));
    int r = tid >> 3;
    stsf(L_ST + r * 4, rbf(m0)); stsf(L_ST + 512 + r * 4, 1.f / d0);
    stsf(L_ST + (64 + r) * 4, rbf(m1)); stsf(L_ST + 512 + (64 + r) * 4, 1.f / d1);
  }
}

DI void p1q_consumer(const Params& P, rsrc_t rws, int panel, int cbi, unsigned lds0, u64 tag) {
  char* ws = P.ws;
  const int tid = threadIdx.x, l = tid & 63;
  const int w = __builtin_amdgcn_readfirstlane(tid >> 6);
  const int wm = w >> 2, wn = w & 3;
  const int g = l >> 4, fr = l & 15, q = (l & 15) >> 2, p = l & 3, g1 = g & 1;
  const unsigned aoff0 = (unsigned)((wm * 64 + fr) * 128 + ((g ^ (l & 7)) << 4));
  unsigned bo[4][2];
#pragma unroll
  for (int n = 0; n < 4; ++n)
#pragma unroll
    for (int t = 0; t < 2; ++t) {
      const int na = n ^ (2 * wm), e = n & 1;
      bo[n][t] = 16384u + g * 3840 + p * 8 +
                 (wn < 3 ? wn * 1024 + (na >> 1) * 512 + (4 * t + q) * 64 + ((e ^ g1) * 32)
                         : 3072 + (na == 3 ? 2 : na) * 256 + (4 * (t ^ g1) + q) * 32);
    }
  f32x4 acc[4][4];
#pragma unroll
  for (int m = 0; m < 4; ++m)
#pragma unroll
    for (int n = 0; n < 4; ++n) acc[m][n] = (f32x4){0.f, 0.f, 0.f, 0.f};
  f32x4 cs[2] = {{0.f, 0.f, 0.f, 0.f}, {0.f, 0.f, 0.f, 0.f}};
  float s0 = 0.f, q0 = 0.f, s1 = 0.f, q1 = 0.f;
  const short one = 0x3F80;
  const bf16x8 ones = {one, one, one, one, one, one, one, one};
  const bf16x8 zeros = {0, 0, 0, 0, 0, 0, 0, 0};

  bf16x8 af[4], bfr[2][4];
#pragma unroll
  for (int m = 0; m < 4; ++m) { af[m] = zeros; bfr[1][m] = zeros; }
  auto step = [&](unsigned sb, int ks) {
#pragma unroll
    for (int n = 0; n < 4; ++n) bfr[ks][n] = lds_tr(sb + ks * 4 * 3840 + bo[n][0], sb + ks * 4 * 3840 + bo[n][1]);
    cs[0] = MFMA(bfr[ks ^ 1][0], ones, cs[0]); cs[1] = MFMA(bfr[ks ^ 1][1], ones, cs[1]);
#pragma unroll
    for (int m = 0; m < 4; ++m) {
#pragma unroll
      for (int n = 0; n < 4; ++n) acc[m][n] = MFMA(bfr[ks ^ 1][n], af[m], acc[m][n]);
      af[m] = lds_b128(sb + ((aoff0 ^ (ks * 64)) + m * 2048));
    }
  };
  unsigned sb = lds0;
#pragma unroll 1
  for (int kt = 0; kt < 10; ++kt) {
    bar();
    step(sb, 0);
    row_stats(sb, tid, s0, q0, s1, q1);
    step(sb, 1);
    sb += K1_STAGE; if (sb == lds0 + 3 * K1_STAGE) sb = lds0;
  }
  cs[0] = MFMA(bfr[1][0], ones, cs[0]); cs[1] = MFMA(bfr[1][1], ones, cs[1]);
#pragma unroll
  for (int m = 0; m < 4; ++m)
#pragma unroll
    for (int n = 0; n < 4; ++n) acc[m][n] = MFMA(bfr[1][n], af[m], acc[m][n]);
  STAMP(1); STAMPC(9);
  const unsigned L_ST = lds0 + L_MISC, L_CS = lds0 + L_MISC + 1024;
  row_stats_finish(L_ST, tid, s0, q0, s1, q1);
  if (fr == 0) {
    stsf4(L_CS + ((wn * 4 + 2 * wm) * 16 + 4 * g) * 4, cs[0]);
    stsf4(L_CS + ((wn * 4 + 2 * wm + 1) * 16 + 4 * g) * 4, cs[1]);
  }
  bar();
  f32x4 csn[4];
#pragma unroll
  for (int n = 0; n < 4; ++n) csn[n] = ldsf4(L_CS + ((wn * 4 + (n ^ (2 * wm))) * 16 + 4 * g) * 4);
  const unsigned L_K = lds0, L_V = L_K + 20480;
  const unsigned pqb = WS_PQ + (unsigned)(((panel * 8 + cbi) * 8 + wm * 4) * 3) * 1024 + l * 16;
  u32x4 qs[4][2];
#pragma unroll
  for (int m = 0; m < 4; ++m) {
    const int rl = wm * 64 + m * 16 + fr;
    const float mean = ldsf(L_ST + rl * 4), rinv = ldsf(L_ST + 512 + rl * 4);
    const int kb = rl >> 3, pos = (rl & 7) ^ ((kb & 1) << 2);
#pragma unroll
    for (int n = 0; n < 4; ++n) {
      const int na = n ^ (2 * wm);
      if (na < 3 || wn < 3) {
        const int seg = wn < 3 ? wn : na, t = wn < 3 ? na : 4;
        float v[4];
#pragma unroll
        for (int j = 0; j < 4; ++j) v[j] = rbf((acc[m][n][j] - mean * csn[n][j]) * rinv);
        if (seg < 2) {
#pragma unroll
          for (int j = 0; j < 4; ++j) v[j] = phi_f(v[j]);
        }
        u32x2 o = {cvt_pk(v[0], v[1]), cvt_pk(v[2], v[3])};
        qs[m][n >> 1][(n & 1) * 2] = o[0]; qs[m][n >> 1][(n & 1) * 2 + 1] = o[1];
        if (seg > 0) *(LAS u32x2*)(uintptr_t)((seg == 1 ? L_K : L_V) + (kb * 5 + t) * 256 + pos * 32 + g * 8) = o;
      }
    }
  }
  bar();
  {
    const unsigned kvp = WS_KVP + (unsigned)(panel * 32 + cbi * 4) * 420 * 4;
    const unsigned troff = (unsigned)(g * 5 * 256 + ((4 * g1 + q) * 32) + p * 8);
#pragma unroll 1
    for (int job = w; job < 18; job += 8) {
      int i, j;
      if (job < 13) { i = (job + 1) / 3; j = job + 1 - 3 * i + i - 1; }
      else { i = job - 13; j = -1; }
      f32x4 d = {0.f, 0.f, 0.f, 0.f};
#pragma unroll
      for (int ks = 0; ks < 4; ++ks) {
        unsigned a0 = L_K + troff + (ks * 20 + i) * 256;
        bf16x8 ka = lds_tr(a0, a0 ^ 128);
        bf16x8 vb = ones;
        if (j >= 0) { unsigned b0 = L_V + troff + (ks * 20 + j) * 256; vb = lds_tr(b0, b0 ^ 128); }
        d = MFMA(ka, vb, d);
      }
#pragma unroll
      for (int r = 0; r < 4; ++r) {
        int dd = 16 * i + 4 * g + r, h = dd / 20, dl = dd - h * 20;
        if (j >= 0) {
          int e = 16 * j + fr, he = e / 20;
          if (he == h) st4(ws, kvp + (h * 420 + dl * 20 + (e - he * 20)) * 4, d[r]);
        } else if (fr == 0) st4(ws, kvp + (h * 420 + 400 + dl) * 4, d[r]);
      }
    }
  }
  if (wn == 0) {
#pragma unroll
    for (int m = 0; m < 4; ++m) { st16(rws, pqb + (m * 3 + wm) * 1024, qs[m][0]); st16(rws, pqb + (m * 3 + (wm ^ 1)) * 1024, qs[m][1]); }
  } else if (wn == 3) {
#pragma unroll
    for (int m = 0; m < 4; ++m) {
      const u32x4 t4 = wm == 0 ? qs[m][0] : qs[m][1];
      st16(rws, pqb + (m * 3 + 2) * 1024, (u32x4){t4[0], t4[1], 0u, 0u});
    }
  }
  wait_vm<0>();
  bar();
  if (tid == 0) st8u(ws, WS_F1 + (panel * 24 + cbi) * 64, tag);
}

DI void p1z_consumer(const Params& P, rsrc_t rws, int panel, int cbj, unsigned lds0, u64 tag) {
  char* ws = P.ws;
  const int tid = threadIdx.x, l = tid & 63;
  const int w = __builtin_amdgcn_readfirstlane(tid >> 6);
  const int wm = w & 3, wn = w >> 2;
  const int g = l >> 4, fr = l & 15, q = (l & 15) >> 2, p = l & 3, g1 = g & 1;
  const unsigned aoff0 = (unsigned)((wm * 32 + fr) * 128 + ((g ^ (l & 7)) << 4));
  unsigned bo[3];
  int ca[3];
#pragma unroll
  for (int n = 0; n < 3; ++n) {
    int c = n + wm; c = c >= 6 ? c - 6 : c >= 3 ? c - 3 : c; c += wn * 3;
    ca[n] = c;
    bo[n] = 16384u + g * 1536 + (c >> 1) * 512 + q * 64 + (((c & 1) ^ g1) * 32) + p * 8;
  }
  const short one = 0x3F80;
  const bf16x8 ones = {one, one, one, one, one, one, one, one};
  const bf16x8 zeros = {0, 0, 0, 0, 0, 0, 0, 0};
  f32x4 accA[2][3], accB[2][3];
#pragma unroll
  for (int m = 0; m < 2; ++m)
#pragma unroll
    for (int n = 0; n < 3; ++n) { accA[m][n] = (f32x4){0.f, 0.f, 0.f, 0.f}; accB[m][n] = (f32x4){0.f, 0.f, 0.f, 0.f}; }
  f32x4 csA = {0.f, 0.f, 0.f, 0.f}, csB = {0.f, 0.f, 0.f, 0.f};
  float s0 = 0.f, q0 = 0.f, s1 = 0.f, q1 = 0.f;
  bf16x8 af[2][2], bfr[2][3];
  af[1][0] = af[1][1] = zeros; bfr[1][0] = bfr[1][1] = bfr[1][2] = zeros;
  auto rd = [&](unsigned sb, int ks) {
#pragma unroll
    for (int m = 0; m < 2; ++m) af[ks][m] = lds_b128(sb + ((aoff0 ^ (ks * 64)) + m * 2048));
#pragma unroll
    for (int n = 0; n < 3; ++n) { const unsigned o = sb + ks * 4 * 1536 + bo[n]; bfr[ks][n] = lds_tr(o, o + 256); }
  };
  auto mmA = [&](int ks) {
#pragma unroll
    for (int n = 0; n < 3; ++n)
#pragma unroll
      for (int m = 0; m < 2; ++m) accA[m][n] = MFMA(af[ks][m], bfr[ks][n], accA[m][n]);
    csA = MFMA(ones, bfr[ks][0], csA);
  };
  auto mmB = [&](int ks) {
#pragma unroll
    for (int n = 0; n < 3; ++n)
#pragma unroll
      for (int m = 0; m < 2; ++m) accB[m][n] = MFMA(bfr[ks][n], af[ks][m], accB[m][n]);
    csB = MFMA(bfr[ks][0], ones, csB);
  };
  const unsigned L_ST = lds0 + L_MISC, L_CS = lds0 + L_MISC + 1024;
  const unsigned L_RS = lds0 + L_MISC + 2048;
  const unsigned L_FS = lds0 + L_MISC + 4096;
  unsigned sb = lds0;
#pragma unroll 1
  for (int kt = 0; kt < 10; ++kt) {
    bar();
    rd(sb, 0);
    mmA(1);
    rd(sb, 1);
    row_stats(sb, tid, s0, q0, s1, q1);
    mmA(0);
    sb += K1_STAGE; if (sb == lds0 + 3 * K1_STAGE) sb = lds0;
  }
  u32x4 zv[3];
  float part = 0.f;
  const unsigned tl = (unsigned)tag, th = tl * 0x9E3779B1u ^ (unsigned)(tag >> 32);
  const unsigned tg = (tid & 1) ? th : tl;
  const unsigned gbase = WS_GR + (unsigned)(panel * 8) * 2048 + tid * 8;
#pragma unroll
  for (int kt = 10; kt < 20; ++kt) {
    bar();
    rd(sb, 0);
    if (kt == 10) mmA(1); else mmB(1);
    rd(sb, 1);
    if (kt == 10) {
      row_stats_finish(L_ST, tid, s0, q0, s1, q1);
      if (wm < 3 && g == 0) stsf(L_CS + ((wn * 3 + wm) * 16 + fr) * 4, csA[0]);
    }
    if (kt == 11) {
      float cn[3];
#pragma unroll
      for (int n = 0; n < 3; ++n) cn[n] = ldsf(L_CS + (ca[n] * 16 + fr) * 4);
#pragma unroll
      for (int m = 0; m < 2; ++m) {
        const int rl = wm * 32 + m * 16 + 4 * g;
        const f32x4 mean = ldsf4(L_ST + rl * 4), rinv = ldsf4(L_ST + 512 + rl * 4);
        float rs[4] = {0.f, 0.f, 0.f, 0.f}, rq[4] = {0.f, 0.f, 0.f, 0.f};
#pragma unroll
        for (int n = 0; n < 3; ++n) {
          float v[4];
#pragma unroll
          for (int j = 0; j < 4; ++j) {
            v[j] = rbf(gelu_t(rbf((accA[m][n][j] - mean[j] * cn[n]) * rinv[j])));
            rs[j] += v[j]; rq[j] += v[j] * v[j];
          }
          zv[n][2 * m] = cvt_pk(v[0], v[1]); zv[n][2 * m + 1] = cvt_pk(v[2], v[3]);
        }
#pragma unroll
        for (int j = 0; j < 4; ++j) {
#pragma unroll
          for (int o = 1; o < 16; o <<= 1) { rs[j] += __shfl_xor(rs[j], o); rq[j] += __shfl_xor(rq[j], o); }
        }
        if (fr == 0) {
          stsf4(L_RS + ((wn * 128 + rl) * 2) * 4, (f32x4){rs[0], rq[0], rs[1], rq[1]});
          stsf4(L_RS + ((wn * 128 + rl) * 2 + 4) * 4, (f32x4){rs[2], rq[2], rs[3], rq[3]});
        }
      }
    }
    if (kt == 12 && tid < 256) {
      part = ldsf(L_RS + tid * 4) + ldsf(L_RS + 1024 + tid * 4);
      st8u(ws, gbase + cbj * 2048, ((u64)tg << 32) | __float_as_uint(part));
    }
    if (kt == 15 && tid < 256) {
      float tot = part;
#pragma unroll 1
      for (unsigned sp = 0; sp < SPIN_CAP; ++sp) {
        bool ok = true; tot = part;
#pragma unroll
        for (int i = 1; i < 8; ++i) {
          u64 v = ld8(ws, gbase + ((cbj + i) & 7) * 2048);
          ok &= (unsigned)(v >> 32) == tg; tot += __uint_as_float((unsigned)v);
        }
        if (__all(ok)) break;
        __builtin_amdgcn_s_sleep(1);
      }
      float other = __shfl_xor(tot, 1);
      if ((tid & 1) == 0) {
        float m = tot * (1.f / FH);
        float v = rbf(fmaxf(other * (1.f / FH) - m * m, 0.f));
        stsf(L_FS + (tid >> 1) * 4, rbf(m));
        stsf(L_FS + 512 + (tid >> 1) * 4, 1.f / rbf(sqrtf(rbf(v + 1e-5f))));
      }
    }
    if (kt == 16) {
      f32x4 mean[2], ri[2];
#pragma unroll
      for (int m = 0; m < 2; ++m) { const int rl = wm * 32 + m * 16 + 4 * g; mean[m] = ldsf4(L_FS + rl * 4); ri[m] = ldsf4(L_FS + 512 + rl * 4); }
#pragma unroll
      for (int n = 0; n < 3; ++n) {
        u32x4 o;
#pragma unroll
        for (int m = 0; m < 2; ++m) {
          o[2 * m] = cvt_pk(rbf(lo16(zv[n][2 * m]) - mean[m][0]) * ri[m][0], rbf(hi16(zv[n][2 * m]) - mean[m][1]) * ri[m][1]);
          o[2 * m + 1] = cvt_pk(rbf(lo16(zv[n][2 * m + 1]) - mean[m][2]) * ri[m][2], rbf(hi16(zv[n][2 * m + 1]) - mean[m][3]) * ri[m][3]);
        }
        st16(rws, WS_Z2N + (unsigned)(((cbj * 64 + panel * 4 + wm) * 6 + ca[n]) * 1024) + l * 16, o);
      }
    }
    if (kt == 18) wait_vm<0>();
    if (kt == 19 && tid == 0) st8u(ws, WS_F1 + (panel * 24 + 8 + cbj) * 64, tag);
    mmB(0);
    sb += K1_STAGE; if (sb == lds0 + 3 * K1_STAGE) sb = lds0;
  }
  mmB(1);
  STAMP(1);
  if (wm < 3 && fr == 0) stsf4(L_CS + 384 + ((wn * 3 + wm) * 16 + 4 * g) * 4, csB);
  bar();
  {
    f32x4 cn[3];
#pragma unroll
    for (int n = 0; n < 3; ++n) cn[n] = ldsf4(L_CS + 384 + (ca[n] * 16 + 4 * g) * 4);
    float mean[2], rinv[2];
#pragma unroll
    for (int m = 0; m < 2; ++m) { const int rl = wm * 32 + m * 16 + fr; mean[m] = ldsf(L_ST + rl * 4); rinv[m] = ldsf(L_ST + 512 + rl * 4); }
#pragma unroll
    for (int n = 0; n < 3; ++n) {
      u32x4 o;
#pragma unroll
      for (int m = 0; m < 2; ++m) {
        float v[4];
#pragma unroll
        for (int j = 0; j < 4; ++j) v[j] = gelu_t(rbf((accB[m][n][j] - mean[m] * cn[n][j]) * rinv[m]));
        o[2 * m] = cvt_pk(v[0], v[1]); o[2 * m + 1] = cvt_pk(v[2], v[3]);
      }
      st16(rws, WS_Z1 + (unsigned)(((panel * 8 + cbj) * 4 + wm) * 6 + ca[n]) * 1024 + l * 16, o);
    }
  }
  wait_vm<0>();
  bar();
  if (tid == 0) st8u(ws, WS_F1 + (panel * 24 + 16 + cbj) * 64, tag);
}

template <int MODE>
DI void p1_loader(const Params& P, int panel, int cbi, unsigned lds0, int lw, const char* gA2, unsigned voff2) {
  constexpr int NBW = MODE == 0 ? 8 : 3;
  constexpr int NPER = 4 + NBW;
  constexpr int NEPI = MODE == 0 ? 3 : 2;
  constexpr int NK = MODE == 0 ? 10 : 20;
  const int l = threadIdx.x & 63;
  constexpr int ldw = MODE == 0 ? NQKV : FIN;
  const unsigned voffA = (unsigned)(((l >> 3) * D_ + (((l & 7) ^ (l >> 3)) << 3)) * 2);
  const char* gA = (const char*)(P.x + (size_t)(panel * 128 + lw * 8) * D_);
  unsigned voffB[NBW];
#pragma unroll
  for (int i = 0; i < NBW; ++i) {
    int row, col;
    if (MODE == 0) {
      int ins = lw + 4 * i; if (ins >= 30) ins = 29;
      int pos = ins * 1024 + l * 16, kb = pos / 3840, r = pos - kb * 3840;
      if (r < 3072) {
        int pr = r >> 9, cp = (r >> 4) & 3;
        row = kb * 8 + ((r >> 6) & 7);
        col = (pr >> 1) * 640 + cbi * 80 + (pr & 1) * 32 + ((cp ^ (2 * (kb & 1))) << 3);
      } else {
        int r2 = r - 3072;
        row = kb * 8 + (((r2 >> 5) & 7) ^ ((kb & 1) << 2));
        col = (r2 >> 8) * 640 + cbi * 80 + 64 + (((r2 >> 4) & 1) << 3);
      }
    } else {
      int u = (lw + 4 * i) * 2 + (l >> 5), kb = u / 3, pr = u - kb * 3;
      row = kb * 8 + ((l >> 2) & 7);
      col = cbi * 96 + pr * 32 + (((l & 3) ^ (2 * (kb & 1))) << 3);
    }
    voffB[i] = (unsigned)((row * ldw + col) * 2);
  }
  auto stage = [&](int kt, unsigned sb) {
    const int k = (MODE == 1 && kt >= 10) ? kt - 10 : kt;
    const char* a = gA + k * 128;
#pragma unroll
    for (int i = 0; i < 4; ++i) glds16(a + (size_t)i * 32 * D_ * 2, voffA, sb + i * 4096 + lw * 1024);
    const char* b = (MODE == 0 ? (const char*)P.wqkv : (const char*)P.win + (kt < 10 ? FH * 2 : 0)) + (size_t)k * 64 * ldw * 2;
#pragma unroll
    for (int i = 0; i < NBW; ++i) {
      int ins = lw + 4 * i; if (MODE == 0 && ins >= 30) ins = 29;
      glds16(b, voffB[i], sb + 16384 + ins * 1024);
    }
  };
  auto pre2 = [&](int j) {
#pragma unroll
    for (int i = 0; i < 2; ++i) glds16(gA2 + j * 128 + (size_t)i * 32 * S_ * 2, voff2, lds0 + la2(j) + i * 4096 + lw * 1024);
  };
  stage(0, lds0);
  stage(1, lds0 + K1_STAGE);
  unsigned sb = lds0 + 2 * K1_STAGE;
#pragma unroll 1
  for (int kt = 0; kt < NK - 2; ++kt) {
    wait_vm<NPER>();
    bar();
    stage(kt + 2, sb);
    sb += K1_STAGE; if (sb == lds0 + 3 * K1_STAGE) sb = lds0;
  }
  if (MODE == 0) {
    wait_vm<NPER>(); bar();
#pragma unroll
    for (int j = 0; j < 5; ++j) pre2(j);
    wait_vm<10>(); bar();
    pre2(5);
  } else {
    wait_vm<NPER>(); bar();
    pre2(4); pre2(5);
    wait_vm<4>(); bar();
    bar();
#pragma unroll
    for (int j = 0; j < 4; ++j) pre2(j);
  }
#pragma unroll
  for (int i = 0; i < NEPI - (MODE == 0 ? 0 : 1); ++i) bar();
}

__global__ __launch_bounds__(768, 3) void mega_kernel(Params P) {
  extern __shared__ __attribute__((aligned(16))) char smem[];
  const unsigned lds0 = (unsigned)(uintptr_t)(LAS char*)smem;
  char* ws = P.ws;
  const int tid = threadIdx.x, l = tid & 63;
  const int w = __builtin_amdgcn_readfirstlane(tid >> 6);
  const int g = l >> 4, fr = l & 15;
  const int b = blockIdx.x, xcd = b & 7, bi = b >> 3;
  const int panel1 = (xcd & 1) * 8 + (bi & 7), ci = bi >> 3, cb1 = (xcd >> 1) * 2 + (ci & 1);
  const bool isq = ci < 2;
  const int panel2 = (xcd >> 1) * 8 + (bi & 7), cb2 = (xcd & 1) * 4 + (bi >> 3);
  const int panel3 = xcd * 4 + (bi & 3), cb3 = bi >> 2;
  STAMP(0); STAMPC(8);
  const u64 tag = ld8(ws, WS_EP) + 1;

  if (w >= 8) {
    const int lw = w - 8;
    const unsigned voff2 = (unsigned)(((l >> 3) * S_ + (((l & 7) ^ ((l >> 4) | ((lw & 1) << 2))) << 3)) * 2);
    const char* gA2 = (const char*)(P.wsp + (size_t)(panel2 * 64 + lw * 8) * S_);
    if (isq) p1_loader<0>(P, panel1, cb1, lds0, lw, gA2, voff2);
    else p1_loader<1>(P, panel1, cb1, lds0, lw, gA2, voff2);
    STAMP(2);
    wait_words(ws, WS_F1 + ((l & 15) * 24 + 8 + cb2) * 64, l < 16, tag);
    STAMP(3);
    const unsigned l16 = l * 16;
    const char* gB2 = ws + WS_Z2N + (size_t)cb2 * 64 * 6144 + lw * 1024;
    auto ldB = [&](int kt, int j) {
#pragma unroll
      for (int i = 0; i < 3; ++i) glds16c(gB2 + kt * 12288 + i * 4096, l16, lds0 + lb2(j) + i * 4096 + lw * 1024);
    };
    auto ldA = [&](int kt, int j) {
#pragma unroll
      for (int i = 0; i < 2; ++i) glds16(gA2 + kt * 128 + (size_t)i * 32 * S_ * 2, voff2, lds0 + la2(j) + i * 4096 + lw * 1024);
    };
#pragma unroll
    for (int j = 0; j < 6; ++j) ldB(j, j);
    int slot = 0;
#pragma unroll 1
    for (int kt = 0; kt < 32; ++kt) {
      if (kt < 5) wait_vm<12>();
      else if (kt < 28) wait_vm<20>();
      else if (kt == 28) wait_vm<15>();
      else if (kt == 29) wait_vm<10>();
      else if (kt == 30) wait_vm<5>();
      else wait_vm<0>();
      bar();
      if (kt == 14) {
        wait_words(ws, WS_FK + (cb2 * 4 + (l & 3)) * 64, l < 4, tag);
#pragma unroll
        for (int i = 0; i < 5; ++i) {
          int ins = lw + 4 * i; if (ins >= 18) ins = 17;
          glds16c(ws + WS_KVT + cb2 * 18432 + ins * 1024, l16, lds0 + L_KVT + ins * 1024);
        }
      }
      if (kt >= 1 && kt + 5 < 32) { ldA(kt + 5, slot); ldB(kt + 5, slot); if (++slot == 6) slot = 0; }
    }
    bar();
    STAMP(4);
    const char* gA3 = ws + WS_OH + (size_t)panel3 * 44 * 4096 + lw * 1024;
    unsigned voffB3[5];
#pragma unroll
    for (int i = 0; i < 5; ++i) {
      int pos = (lw + 4 * i) * 1024 + l * 16, kb = pos / 1280, r = pos - kb * 1280, row, col;
      if (r < 1024) { int rr = (r >> 6) & 7; row = kb * 8 + rr; col = (r >> 9) * 32 + ((((r >> 4) & 3) ^ (2 * (rr >> 2))) << 3); }
      else { int r2 = r - 1024; row = kb * 8 + (r2 >> 5); col = 64 + (((r2 >> 4) & 1) << 3); }
      voffB3[i] = (unsigned)((row * D_ + cb3 * 80 + col) * 2);
    }
    auto ldB3 = [&](int u, int j) {
      const char* bsrc = u < 5 ? (const char*)P.wo + (size_t)u * 128 * D_ * 2 : (const char*)P.wout + (size_t)(u - 5) * 128 * D_ * 2;
#pragma unroll
      for (int i = 0; i < 5; ++i) glds16(bsrc, voffB3[i], lds0 + j * P3_SLOT + 16384 + (lw + 4 * i) * 1024);
    };
    auto ldA3 = [&](int u, int j) {
#pragma unroll
      for (int i = 0; i < 4; ++i) glds16c(gA3 + u * 16384 + i * 4096, l16, lds0 + j * P3_SLOT + i * 4096 + lw * 1024);
    };
#pragma unroll
    for (int j = 0; j < 3; ++j) ldB3(j, j);
    wait_words(ws, WS_FO + (panel3 * 8 + (l & 7)) * 64, l < 8, tag);
#pragma unroll
    for (int j = 0; j < 3; ++j) ldA3(j, j);
    bar();
    bar();
    ldA3(3, 3); ldB3(3, 3);
    bar();
    slot = 0;
#pragma unroll 1
    for (int u = 0; u < 11; ++u) {
      if (u == 0) wait_vm<17>();
      else if (u == 1) wait_vm<13>();
      else if (u < 9) wait_vm<18>();
      else if (u == 9) wait_vm<9>();
      else wait_vm<0>();
      bar();
      if (u == 2) { wait_words(ws, WS_F2 + (panel3 * 8 + (l & 7)) * 64, l < 8, tag); STAMP(5); }
      if (u >= 1 && u + 3 < 11) { ldA3(u + 3, slot); ldB3(u + 3, slot); if (++slot == 4) slot = 0; }
    }
    bar();
    bar();
    if (b == 0 && lw == 0) {
#pragma unroll
      for (int i = 0; i < 4; ++i) wait_words(ws, WS_F2 + (i * 64 + l) * 64, true, tag);
      if (l == 0) st8u(ws, WS_EP, tag);
    }
    STAMP(6);
    return;
  }

  const rsrc_t rws = __builtin_amdgcn_make_buffer_rsrc(ws, 0, (int)WS_END, 0x00020000);
  if (isq) p1q_consumer(P, rws, panel1, cb1, lds0, tag);
  else p1z_consumer(P, rws, panel1, cb1, lds0, tag);
  STAMP(2);

  {
    const int wt = w & 3, kh = w >> 2;
    const int rsw = ((wt * 16 + fr) >> 1) & 7;
    const unsigned ao0 = (unsigned)((wt * 16 + fr) * 128 + (((4 * kh + (g >> 1)) ^ rsw) << 4) + (g & 1) * 8);
    const unsigned ao1 = (unsigned)((wt * 16 + fr) * 128 + (((4 * kh + 2 + (g >> 1)) ^ rsw) << 4) + (g & 1) * 8);
    const unsigned bo = (unsigned)(kh * 6144 + l * 16);
    const bf16x8 zeros = {0, 0, 0, 0, 0, 0, 0, 0};
    f32x4 acc[6];
#pragma unroll
    for (int n = 0; n < 6; ++n) acc[n] = (f32x4){0.f, 0.f, 0.f, 0.f};
    bf16x8 af[2], bfr[2][6];
    af[1] = zeros;
#pragma unroll
    for (int n = 0; n < 6; ++n) bfr[1][n] = zeros;
    const bool reducer = !isq && (bi & 12) == 8;
    const int rh = xcd * 4 + (bi & 3);
    float pv[16];
    const int row = panel2 * 64 + wt * 16 + fr;
    u32x2 zreg[6];
    bf16x8 pf[3];
    const float bs = bf2f(P.bsp[row]);
    int slot = 0;
    auto step = [&](int cur) {
      const unsigned sa = lds0 + la2(slot), sbb = lds0 + lb2(slot) + bo;
      af[cur] = lds_2b64(sa + ao0, sa + ao1);
#pragma unroll
      for (int n = 0; n < 6; ++n) bfr[cur][n] = lds_b128(sbb + n * 1024);
#pragma unroll
      for (int n = 0; n < 6; ++n) acc[n] = MFMA(bfr[cur ^ 1][n], af[cur ^ 1], acc[n]);
      if (++slot == 6) slot = 0;
    };
#pragma unroll 1
    for (int kt = 0; kt < 32; kt += 2) {
      bar();
      step(0);
      if (reducer) {
        if (kt == 2) {
          wait_words(ws, WS_F1 + ((l & 15) * 24 + (rh >> 2)) * 64, l < 16, tag);
          if (tid < 420) {
#pragma unroll
            for (int i = 0; i < 16; ++i)
              pv[i] = __uint_as_float(__builtin_amdgcn_raw_buffer_load_b32(rws, tid * 4, WS_KVP + (i * 32 + rh) * 1680, 16));
          }
        } else if (kt == 6) {
          float s = 0.f;
#pragma unroll
          for (int i = 0; i < 16; ++i) s += pv[i];
          if (tid < 420) stsf(lds0 + L_MISC + tid * 4, s);
        } else if (kt == 8) {
          if (tid < 480) {
            int tz = tid; asm volatile("" : "+v"(tz));
            const int which = tz / 240, r = tz - which * 240, e = r / 12, sg = r - e * 12, st = sg >> 2, gg = sg & 3, hl = rh & 3;
            const int eg = hl * 20 + e, et = eg >> 4;
            const int st0 = et >= 3 ? 1 : 0;
            const bool used = et == 0 ? st == 0 : (st == st0 || st == st0 + 1);
            const int fi = et == 0 ? 0 : 1 + (et - 1) * 2 + (st - st0);
            float v[8];
#pragma unroll
            for (int j = 0; j < 8; ++j) {
              int dl = st * 32 + 16 * (j >> 2) + 4 * gg + (j & 3) - hl * 20;
              bool in = dl >= 0 && dl < 20;
              int idx = in ? (which == 0 ? dl * 20 + e : 400 + dl) : 0;
              float t = ldsf(lds0 + L_MISC + idx * 4);
              v[j] = in ? t : 0.f;
            }
            if (used) st16(rws, WS_KVT + (unsigned)((((rh >> 2) * 2 + which) * 9 + fi) * 1024 + (gg * 16 + (eg & 15)) * 16),
                           (u32x4){cvt_pk(v[0], v[1]), cvt_pk(v[2], v[3]), cvt_pk(v[4], v[5]), cvt_pk(v[6], v[7])});
          }
          wait_vm<0>();
        } else if (kt == 10) {
          if (tid == 0) st8u(ws, WS_FK + rh * 64, tag);
        }
      }
      if (kt == 14 && kh == 1) {
        wait_words(ws, WS_F1 + ((panel2 >> 1) * 24 + cb2) * 64, l == 0, tag);
        const unsigned pqo = WS_PQ + (unsigned)((((panel2 >> 1) * 8 + cb2) * 8 + (panel2 & 1) * 4 + wt) * 3) * 1024 + l * 16;
#pragma unroll
        for (int s = 0; s < 3; ++s) pf[s] = __builtin_bit_cast(bf16x8, ld16(rws, pqo + s * 1024));
      }
      if (kt == 22 && kh == 1) {
        u32x2 ov[5];
#pragma unroll
        for (int et = 0; et < 5; ++et) {
          f32x4 num = {0.f, 0.f, 0.f, 0.f}, den = {0.f, 0.f, 0.f, 0.f};
          const int st0 = et >= 3 ? 1 : 0, nst = et == 0 ? 1 : 2, f0 = et == 0 ? 0 : 1 + (et - 1) * 2;
#pragma unroll
          for (int s = 0; s < nst; ++s) {
            bf16x8 ka = lds_b128(lds0 + L_KVT + (f0 + s) * 1024 + l * 16), za = lds_b128(lds0 + L_KVT + (9 + f0 + s) * 1024 + l * 16);
            num = MFMA(ka, pf[st0 + s], num); den = MFMA(za, pf[st0 + s], den);
          }
          float v[4];
#pragma unroll
          for (int j = 0; j < 4; ++j) v[j] = rbf(num[j]) / rbf(rbf(den[j]) + 1e-6f);
          ov[et] = (u32x2){cvt_pk(v[0], v[1]), cvt_pk(v[2], v[3])};
        }
        const unsigned ob = WS_OH + (unsigned)((panel2 * 44 + ((cb2 * 5) >> 1)) * 4 + wt) * 1024 + l * 16;
        if ((cb2 & 1) == 0) {
          st16(rws, ob, (u32x4){ov[0][0], ov[0][1], ov[1][0], ov[1][1]});
          st16(rws, ob + 4096, (u32x4){ov[2][0], ov[2][1], ov[3][0], ov[3][1]});
          st8(ws, ob + 8192, ov[4]);
        } else {
          st8(ws, ob + 8, ov[0]);
          st16(rws, ob + 4096, (u32x4){ov[1][0], ov[1][1], ov[2][0], ov[2][1]});
          st16(rws, ob + 8192, (u32x4){ov[3][0], ov[3][1], ov[4][0], ov[4][1]});
        }
      }
      if (kt == 26) {
        if (kh == 1) wait_vm<0>();
        else {
          wait_words(ws, WS_F1 + ((panel2 >> 1) * 24 + 16 + cb2) * 64, l == 0, tag);
          const int m128 = (panel2 & 1) * 4 + wt;
          const unsigned zo = WS_Z1 + (unsigned)((((panel2 >> 1) * 8 + cb2) * 4 + (m128 >> 1)) * 6) * 1024 + l * 16 + (m128 & 1) * 8;
#pragma unroll
          for (int n = 0; n < 6; ++n) zreg[n] = __builtin_amdgcn_raw_buffer_load_b64(rws, zo + n * 1024, 0, 16);
        }
      }
      if (kt == 28 && tid == 0) st8u(ws, WS_FO + (panel2 * 8 + cb2) * 64, tag);
      bar();
      step(1);
    }
#pragma unroll
    for (int n = 0; n < 6; ++n) acc[n] = MFMA(bfr[1][n], af[1], acc[n]);
    bar();
    STAMP(4);
    const unsigned L_RED = lds0 + 3 * P3_SLOT + 8192;
    if (kh == 1) {
#pragma unroll
      for (int n = 0; n < 6; ++n) stsf4(L_RED + (wt * 6 + n) * 1024 + l * 16, acc[n]);
    }
    asm volatile("s_waitcnt lgkmcnt(0)" ::: "memory");
    bar();
    if (kh == 0) {
#pragma unroll
      for (int n = 0; n < 6; ++n) acc[n] += ldsf4(L_RED + (wt * 6 + n) * 1024 + l * 16);
    }
    bar();
    if (kh == 0) {
      u32x2 hv[6];
#pragma unroll
      for (int n = 0; n < 6; ++n) {
        float g0 = rbf(rbf(acc[n][0]) + bs), g1 = rbf(rbf(acc[n][1]) + bs);
        float g2 = rbf(rbf(acc[n][2]) + bs), g3 = rbf(rbf(acc[n][3]) + bs);
        hv[n] = (u32x2){cvt_pk(lo16(zreg[n][0]) * g0, hi16(zreg[n][0]) * g1), cvt_pk(lo16(zreg[n][1]) * g2, hi16(zreg[n][1]) * g3)};
      }
      const unsigned hb = WS_OH + (unsigned)((panel2 * 44 + 20 + cb2 * 3) * 4 + wt) * 1024 + l * 16;
#pragma unroll
      for (int s = 0; s < 3; ++s) st16(rws, hb + s * 4096, (u32x4){hv[2 * s][0], hv[2 * s][1], hv[2 * s + 1][0], hv[2 * s + 1][1]});
      wait_vm<0>();
    }
    bar();
    if (tid == 0) st8u(ws, WS_F2 + (panel2 * 8 + cb2) * 64, tag);
    STAMP(5);
  }
  {
    const int wsr = w & 3, kh = w >> 2;
    const int q = (l & 15) >> 2, p = l & 3, g1 = g & 1;
    const unsigned ao = (unsigned)(kh * 8192 + wsr * 1024 + l * 16);
    const unsigned bb = 16384u + (kh * 8 + (g >> 1)) * 1280 + p * 8;
    const unsigned bp0 = bb + (4 * g1 + q) * 64 + g1 * 32, bp1 = bb + (4 * g1 + q) * 64 + (g1 ^ 1) * 32, bs1 = bb + 1024 + (4 * g1 + q) * 32;
    const bf16x8 zeros = {0, 0, 0, 0, 0, 0, 0, 0};
    f32x4 acc[5];
#pragma unroll
    for (int n = 0; n < 5; ++n) acc[n] = (f32x4){0.f, 0.f, 0.f, 0.f};
    bf16x8 af[2], bfr[2][5];
    af[1] = zeros;
#pragma unroll
    for (int n = 0; n < 5; ++n) bfr[1][n] = zeros;
    u32x4 xv[2];
    const int c0 = tid, c1 = tid + 512;
    const size_t xo0 = (size_t)(panel3 * 64 + c0 / 10) * D_ + cb3 * 80 + (c0 % 10) * 8;
    const size_t xo1 = (size_t)(panel3 * 64 + c1 / 10) * D_ + cb3 * 80 + (c1 % 10) * 8;
    xv[0] = *(const u32x4*)(P.x + xo0);
    if (tid < 128) xv[1] = *(const u32x4*)(P.x + xo1);
    unsigned sb = lds0;
    auto step = [&](int s) {
      af[s] = lds_b128(sb + ao + s * 4096);
      const unsigned o = sb + s * 5120;
#pragma unroll
      for (int n = 0; n < 4; ++n) { const unsigned a = o + ((n & 1) ? bp1 : bp0) + (n >> 1) * 512; bfr[s][n] = lds_tr(a, a + 2560); }
      bfr[s][4] = lds_tr(o + bs1, o + bs1 + 2560);
#pragma unroll
      for (int n = 0; n < 5; ++n) acc[n] = MFMA(bfr[s ^ 1][n], af[s ^ 1], acc[n]);
    };
#pragma unroll 1
    for (int u = 0; u < 11; ++u) {
      bar();
      step(0);
      step(1);
      sb += P3_SLOT; if (sb == lds0 + 4 * P3_SLOT) sb = lds0;
    }
#pragma unroll
    for (int n = 0; n < 5; ++n) acc[n] = MFMA(bfr[1][n], af[1], acc[n]);
    STAMP(6);
    bar();
#pragma unroll
    for (int n = 0; n < 5; ++n) stsf4(lds0 + (unsigned)(((kh * 64 + wsr * 16 + fr) * 84 + n * 16 + 4 * g) * 4), acc[n]);
    bar();
#pragma unroll
    for (int i = 0; i < 2; ++i) {
      if (i == 0 || tid < 128) {
        const int c = tid + i * 512, r = c / 10, ch = c - r * 10;
        const unsigned a = lds0 + (unsigned)((r * 84 + ch * 8) * 4);
        f32x4 a0 = ldsf4(a) + ldsf4(a + 64 * 84 * 4), a1 = ldsf4(a + 16) + ldsf4(a + 64 * 84 * 4 + 16);
        u32x4 o = {cvt_pk(lo16(xv[i][0]) + a0[0], hi16(xv[i][0]) + a0[1]), cvt_pk(lo16(xv[i][1]) + a0[2], hi16(xv[i][1]) + a0[3]),
                   cvt_pk(lo16(xv[i][2]) + a1[0], hi16(xv[i][2]) + a1[1]), cvt_pk(lo16(xv[i][3]) + a1[2], hi16(xv[i][3]) + a1[3])};
        *(u32x4*)(P.out + (i == 0 ? xo0 : xo1)) = o;
      }
    }
    STAMP(7);
  }
}

extern "C" void kernel_launch(void* const* d_in, const int* in_sizes, int n_in, void* d_out, int out_size,
                              void* d_ws, size_t ws_size, hipStream_t stream) {
  Params P;
  P.x = (const u16*)d_in[0];
  P.wqkv = (const u16*)d_in[1];
  P.wo = (const u16*)d_in[2];
  P.win = (const u16*)d_in[4];
  P.wsp = (const u16*)d_in[5];
  P.bsp = (const u16*)d_in[6];
  P.wout = (const u16*)d_in[7];
  P.out = (u16*)d_out;
  P.ws = (char*)d_ws;
  (void)hipFuncSetAttribute((const void*)mega_kernel, hipFuncAttributeMaxDynamicSharedMemorySize, LDS_TOTAL);
  mega_kernel<<<256, 768, LDS_TOTAL, stream>>>(P);
}
```

```cpp
#include <hip/hip_runtime.h>
#include <hip/hip_bf16.h>
#include <stdint.h>

typedef __attribute__((ext_vector_type(8))) short bf16x8;
typedef __attribute__((ext_vector_type(4))) short bf16x4;
typedef __attribute__((ext_vector_type(4))) float f32x4;
typedef __attribute__((ext_vector_type(2))) float f32x2;
typedef __attribute__((ext_vector_type(2))) __bf16 bf2;
typedef __attribute__((ext_vector_type(4))) unsigned u32x4;
typedef __attribute__((ext_vector_type(2))) unsigned u32x2;
typedef unsigned short u16;
typedef unsigned long long u64;
#define LAS __attribute__((address_space(3)))
#define GAS __attribute__((address_space(1)))
#define DI __device__ __forceinline__
#define RLX_AGENT __ATOMIC_RELAXED, __HIP_MEMORY_SCOPE_AGENT

constexpr int S_ = 2048, D_ = 640, NQKV = 1920, FIN = 1536, FH = 768;

constexpr unsigned WS_PQ  = 0;
constexpr unsigned WS_Z1  = WS_PQ + 16 * 8 * 8 * 3 * 1024;
constexpr unsigned WS_Z2N = WS_Z1 + 16 * 8 * 24 * 1024;
constexpr unsigned WS_OH  = WS_Z2N + 8 * 64 * 6 * 1024;
constexpr unsigned WS_WF  = WS_OH + 32 * 44 * 4 * 1024;
constexpr unsigned WS_KVT = WS_WF + 8 * 44 * 5 * 1024;
constexpr unsigned WS_KVP = WS_KVT + 8 * 18 * 1024;
constexpr unsigned WS_GR  = WS_KVP + 8 * 18 * 16 * 1024;
constexpr unsigned WS_GX  = WS_GR + 16 * 8 * 256 * 8;
constexpr unsigned WS_GC  = WS_GX + 2048 * 16;
constexpr unsigned WS_F1  = WS_GC + 24 * 10 * 2048;
constexpr unsigned WS_F2  = WS_F1 + 16 * 24 * 64;
constexpr unsigned WS_FK  = WS_F2 + 256 * 64;
constexpr unsigned WS_EP  = WS_FK + 128 * 64;
constexpr unsigned WS_END = WS_EP + 64;

#define STAMP(i)

DI float bf2f(u16 v) { return __uint_as_float(((unsigned)v) << 16); }
DI unsigned cvt_pk(float lo, float hi) {
  bf2 r; r[0] = (__bf16)lo; r[1] = (__bf16)hi; return __builtin_bit_cast(unsigned, r);
}
DI float rbf(float x) { return (float)((__bf16)x); }
DI float lo16(unsigned u) { return __uint_as_float(u << 16); }
DI float hi16(unsigned u) { return __uint_as_float(u & 0xffff0000u); }

DI void glds16(const void* gbase, unsigned voff, unsigned lds_dst) {
  unsigned keep;
  asm volatile("s_mov_b32 %0, m0\n\ts_mov_b32 m0, %3\n\ts_nop 0\n\tglobal_load_lds_dwordx4 %1, %2\n\ts_mov_b32 m0, %0"
               : "=&s"(keep) : "v"(voff), "s"(gbase), "s"(lds_dst) : "memory");
}
DI void glds16c(const void* gbase, unsigned voff, unsigned lds_dst) {
  unsigned keep;
  asm volatile("s_mov_b32 %0, m0\n\ts_mov_b32 m0, %3\n\ts_nop 0\n\tglobal_load_lds_dwordx4 %1, %2 sc1\n\ts_mov_b32 m0, %0"
               : "=&s"(keep) : "v"(voff), "s"(gbase), "s"(lds_dst) : "memory");
}
#define GL_PRE "s_mov_b32 %0, m0\n\ts_mov_b32 m0, %2\n\ts_nop 0\n\t"
#define GL_POST "s_mov_b32 m0, %0"
DI void gl2(const void* gb, unsigned lds, unsigned v0, unsigned v1) {
  unsigned keep;
  asm volatile(GL_PRE "global_load_lds_dwordx4 %3, %1\n\tglobal_load_lds_dwordx4 %4, %1 offset:1024\n\t" GL_POST
               : "=&s"(keep) : "s"(gb), "s"(lds), "v"(v0), "v"(v1) : "memory");
}
DI void gl3(const void* gb, unsigned lds, unsigned v0, unsigned v1, unsigned v2) {
  unsigned keep;
  asm volatile(GL_PRE "global_load_lds_dwordx4 %3, %1\n\tglobal_load_lds_dwordx4 %4, %1 offset:1024\n\tglobal_load_lds_dwordx4 %5, %1 offset:2048\n\t" GL_POST
               : "=&s"(keep) : "s"(gb), "s"(lds), "v"(v0), "v"(v1), "v"(v2) : "memory");
}
DI void gl4(const void* gb, unsigned lds, unsigned v0, unsigned v1, unsigned v2, unsigned v3) {
  unsigned keep;
  asm volatile(GL_PRE "global_load_lds_dwordx4 %3, %1\n\tglobal_load_lds_dwordx4 %4, %1 offset:1024\n\tglobal_load_lds_dwordx4 %5, %1 offset:2048\n\tglobal_load_lds_dwordx4 %6, %1 offset:3072\n\t" GL_POST
               : "=&s"(keep) : "s"(gb), "s"(lds), "v"(v0), "v"(v1), "v"(v2), "v"(v3) : "memory");
}
DI void gl3c(const void* gb, unsigned lds, unsigned v0) {
  unsigned keep;
  asm volatile(GL_PRE "global_load_lds_dwordx4 %3, %1 sc1\n\tglobal_load_lds_dwordx4 %3, %1 offset:1024 sc1\n\tglobal_load_lds_dwordx4 %3, %1 offset:2048 sc1\n\t" GL_POST
               : "=&s"(keep) : "s"(gb), "s"(lds), "v"(v0) : "memory");
}
DI void gl4c(const void* gb, unsigned lds, unsigned v0) {
  unsigned keep;
  asm volatile(GL_PRE "global_load_lds_dwordx4 %3, %1 sc1\n\tglobal_load_lds_dwordx4 %3, %1 offset:1024 sc1\n\tglobal_load_lds_dwordx4 %3, %1 offset:2048 sc1\n\tglobal_load_lds_dwordx4 %3, %1 offset:3072 sc1\n\t" GL_POST
               : "=&s"(keep) : "s"(gb), "s"(lds), "v"(v0) : "memory");
}
template <int N> DI void wait_vm() { asm volatile("s_waitcnt vmcnt(%0)" :: "n"(N) : "memory"); }

DI bf16x8 lds_b128(unsigned addr) { return *(LAS bf16x8*)(uintptr_t)addr; }
DI bf16x8 lds_2b64(unsigned addr0, unsigned addr1) {
  bf16x4 a = *(LAS bf16x4*)(uintptr_t)addr0, b = *(LAS bf16x4*)(uintptr_t)addr1;
  return __builtin_shufflevector(a, b, 0, 1, 2, 3, 4, 5, 6, 7);
}
DI bf16x8 lds_tr(unsigned addr0, unsigned addr1) {
  bf16x4 a = __builtin_amdgcn_ds_read_tr16_b64_v4i16((LAS bf16x4*)(uintptr_t)addr0);
  bf16x4 b = __builtin_amdgcn_ds_read_tr16_b64_v4i16((LAS bf16x4*)(uintptr_t)addr1);
  return __builtin_shufflevector(a, b, 0, 1, 2, 3, 4, 5, 6, 7);
}
DI float ldsf(unsigned a) { return *(LAS float*)(uintptr_t)a; }
DI void stsf(unsigned a, float v) { *(LAS float*)(uintptr_t)a = v; }
DI f32x4 ldsf4(unsigned a) { return *(LAS f32x4*)(uintptr_t)a; }
DI void stsf4(unsigned a, f32x4 v) { *(LAS f32x4*)(uintptr_t)a = v; }
#define SB0 __builtin_amdgcn_sched_barrier(0)
#define MFMA(a, b, c) __builtin_amdgcn_mfma_f32_16x16x32_bf16((a), (b), (c), 0, 0, 0)

template <int CTRL> DI float dpp(float v) {
  return __builtin_bit_cast(float, __builtin_amdgcn_update_dpp(0, __builtin_bit_cast(int, v), CTRL, 0xf, 0xf, true));
}
DI float sum8(float v) { v += dpp<0xB1>(v); v += dpp<0x4E>(v); v += dpp<0x141>(v); return v; }
DI float sum16(float v) { v = sum8(v); v += dpp<0x140>(v); return v; }

DI float gelu_t(float x) {
  float u = x * (0.7978845608f + 0.0356774081f * x * x);
  float e = __builtin_amdgcn_exp2f(-2.885390082f * u);
  return x * __builtin_amdgcn_rcpf(1.0f + e);
}
DI float phi_f(float t) {
  return (fmaxf(t, 0.f) + 1e-6f) + __builtin_amdgcn_exp2f(fminf(t, 0.f) * 1.4426950409f);
}

typedef __amdgpu_buffer_rsrc_t rsrc_t;
DI void st16(rsrc_t r, unsigned off, u32x4 v) { __builtin_amdgcn_raw_buffer_store_b128(v, r, off, 0, 16); }
DI u32x4 ld16(rsrc_t r, unsigned off) { return __builtin_amdgcn_raw_buffer_load_b128(r, off, 0, 16); }
DI void st8(char* ws, unsigned off, u32x2 v) { __hip_atomic_store((GAS u64*)(ws + off), ((u64)v[1] << 32) | v[0], RLX_AGENT); }
DI void st8u(char* ws, unsigned off, u64 v) { __hip_atomic_store((GAS u64*)(ws + off), v, RLX_AGENT); }
DI void st4(char* ws, unsigned off, float v) { __hip_atomic_store((GAS unsigned*)(ws + off), __float_as_uint(v), RLX_AGENT); }
DI u64 ld8(const char* ws, unsigned off) { return __hip_atomic_load((GAS u64*)(ws + off), RLX_AGENT); }
constexpr unsigned SPIN_CAP = 1u << 20;
DI void wait_words(const char* ws, unsigned off, bool act, u64 tag) {
#pragma unroll 1
  for (unsigned sp = 0; sp < SPIN_CAP; ++sp) {
    u64 v = act ? ld8(ws, off) : tag;
    if (__all(v == tag)) break;
    __builtin_amdgcn_s_sleep(1);
  }
  __builtin_amdgcn_fence(__ATOMIC_ACQUIRE, "wavefront");
}

struct Params { const u16 *x, *wqkv, *wo, *win, *wsp, *bsp, *wout; u16* out; char* ws; };

constexpr unsigned KBS = 4224;
constexpr unsigned K1_STAGE = 16384 + 8 * KBS;
constexpr unsigned L_MISC = 153600;
constexpr int LDS_TOTAL = 163840;
constexpr unsigned P2_SLOT = 20480;
constexpr unsigned L_KVT = 135168;
constexpr unsigned P3_SLOT = 36864;

DI void bar() { asm volatile("s_waitcnt lgkmcnt(0)" ::: "memory"); __builtin_amdgcn_s_barrier(); asm volatile("" ::: "memory"); }

struct Side { u32x4 r0, r1; unsigned wv[8]; };
DI void side_issue(const Params& P, Side& S, int row, int w) {
  const int l = threadIdx.x & 63;
  const u32x4* src = (const u32x4*)(P.x + (size_t)row * D_);
  S.r0 = src[l]; S.r1 = src[64 + (l & 15)];
  int item = blockIdx.x; if (item > 219) item = 219;
  const int st = item / 5, T = (item - st * 5) * 8 + w;
  const u16* wsrc = (st < 20 ? P.wo + (size_t)st * 32 * D_ : P.wout + (size_t)(st - 20) * 32 * D_) + (size_t)(l >> 4) * 4 * D_ + T * 16 + (l & 15);
#pragma unroll
  for (int e = 0; e < 8; ++e) S.wv[e] = wsrc[(size_t)(16 * (e >> 2) + (e & 3)) * D_];
}
DI float rdl(float v, int lane) { return __builtin_bit_cast(float, __builtin_amdgcn_readlane(__builtin_bit_cast(int, v), lane)); }
DI void side_finish(const Params& P, rsrc_t rws, const Side& S, int row, int w, unsigned tl, unsigned th) {
  const int l = threadIdx.x & 63;
  float s = 0.f, q = 0.f, s2 = 0.f, q2 = 0.f;
#pragma unroll
  for (int j = 0; j < 4; ++j) {
    float a = lo16(S.r0[j]), b = hi16(S.r0[j]), c = lo16(S.r1[j]), d = hi16(S.r1[j]);
    s += a + b; q += a * a + b * b; s2 += c + d; q2 += c * c + d * d;
  }
  s = sum16(s); q = sum16(q); s2 = sum16(s2); q2 = sum16(q2);
  s = (rdl(s, 0) + rdl(s, 16)) + (rdl(s, 32) + rdl(s, 48)) + s2;
  q = (rdl(q, 0) + rdl(q, 16)) + (rdl(q, 32) + rdl(q, 48)) + q2;
  const float m = s * (1.f / D_);
  const float v = rbf(fmaxf(q * (1.f / D_) - m * m, 0.f));
  const float r = 1.f / rbf(sqrtf(rbf(v + 1e-5f)));
  if (l < 2) st8u(P.ws, WS_GX + row * 16 + l * 8, l == 0 ? (((u64)tl << 32) | __float_as_uint(-rbf(m) * r)) : (((u64)th << 32) | __float_as_uint(r)));
  int item = blockIdx.x; if (item > 219) item = 219;
  const int st = item / 5, T = (item - st * 5) * 8 + w, cb = T / 5, nt = T - cb * 5;
  st16(rws, WS_WF + (unsigned)(((cb * 44 + st) * 5 + nt) * 1024) + l * 16,
       (u32x4){S.wv[0] | (S.wv[1] << 16), S.wv[2] | (S.wv[3] << 16), S.wv[4] | (S.wv[5] << 16), S.wv[6] | (S.wv[7] << 16)});
}
DI void xstat_gather(const char* ws, int panel, unsigned L_ST, int tid, unsigned tg, u64 gv) {
  if (tid < 256) {
#pragma unroll 1
    for (unsigned sp = 0; sp < SPIN_CAP; ++sp) {
      if (__all((unsigned)(gv >> 32) == tg)) break;
      __builtin_amdgcn_s_sleep(1);
      gv = ld8(ws, WS_GX + panel * 2048 + tid * 8);
    }
    stsf(L_ST + (tid & 1) * 512 + (tid >> 1) * 4, __uint_as_float((unsigned)gv));
  }
}

DI void csum_issue(const char* ws, int blk, int t, u64 (&cv)[10]) {
#pragma unroll
  for (int k = 0; k < 10; ++k) cv[k] = ld8(ws, WS_GC + (blk * 10 + k) * 2048 + t * 8);
}
DI void csum_gather(const char* ws, int blk, int t, unsigned tl, u64 (&cv)[10], unsigned dst) {
#pragma unroll 1
  for (unsigned sp = 0; sp < SPIN_CAP; ++sp) {
    bool ok = true;
#pragma unroll
    for (int k = 0; k < 10; ++k) ok &= (unsigned)(cv[k] >> 32) == tl;
    if (__all(ok)) break;
    __builtin_amdgcn_s_sleep(1);
    csum_issue(ws, blk, t, cv);
  }
  float a = 0.f;
#pragma unroll
  for (int k = 0; k < 10; ++k) a += __uint_as_float((unsigned)cv[k]);
  stsf(dst + t * 4, a);
}
DI void csum_store(char* ws, unsigned off, f32x4 d, int fr, int g, unsigned tl) {
  const float v = fr == 0 ? d[0] : fr == 1 ? d[1] : fr == 2 ? d[2] : d[3];
  if (fr < 4) st8u(ws, off + (4 * g + fr) * 8, ((u64)tl << 32) | __float_as_uint(v));
}

DI void p1q_consumer(const Params& P, rsrc_t rws, int w, int panel, int cbi, unsigned lds0, u64 tag) {
  char* ws = P.ws;
  const int tid = threadIdx.x, l = tid & 63;
  const int wm = w >> 2, wc = (w + 2 * wm) & 3;
  const int g = l >> 4, fr = l & 15, q = (l & 15) >> 2, p = l & 3, g1 = g & 1;
  const unsigned aoff0 = (unsigned)((wm * 64 + fr) * 128 + ((g ^ (l & 7)) << 4));
  const unsigned ttab = (unsigned)(0x4498E765DC32BA10ull >> (16 * wc)) & 0xffffu;
  int tile[4];
  unsigned bo[4];
#pragma unroll
  for (int n = 0; n < 4; ++n) {
    tile[n] = (ttab >> (4 * n)) & 15;
    bo[n] = 16384u + g * KBS + q * 480 + p * 8 + tile[n] * 32;
  }
  const unsigned tl = (unsigned)tag, th = tl * 0x9E3779B1u ^ (unsigned)(tag >> 32);
  {
    Side S; side_issue(P, S, panel * 128 + cbi * 8 + w, w);
    side_finish(P, rws, S, panel * 128 + cbi * 8 + w, w, tl, th);
  }
  f32x4 acc[4][4];
#pragma unroll
  for (int m = 0; m < 4; ++m)
#pragma unroll
    for (int n = 0; n < 4; ++n) acc[m][n] = (f32x4){0.f, 0.f, 0.f, 0.f};
  const short one = 0x3F80;
  const bf16x8 ones = {one, one, one, one, one, one, one, one};
  const bf16x8 zeros = {0, 0, 0, 0, 0, 0, 0, 0};

  bf16x8 af[2][4], bfr[2][4];
#pragma unroll
  for (int m = 0; m < 4; ++m) { af[1][m] = zeros; bfr[1][m] = zeros; }
  auto mm = [&](int ks) {
#pragma unroll
    for (int n = 0; n < 3; ++n)
#pragma unroll
      for (int m = 0; m < 4; ++m) acc[m][n] = MFMA(bfr[ks][n], af[ks][m], acc[m][n]);
    if (wc < 3) {
#pragma unroll
      for (int m = 0; m < 4; ++m) acc[m][3] = MFMA(bfr[ks][3], af[ks][m], acc[m][3]);
    }
  };
  auto step = [&](unsigned sb, int ks) {
#pragma unroll
    for (int n = 0; n < 4; ++n) bfr[ks][n] = lds_tr(sb + ks * 4 * KBS + bo[n], sb + ks * 4 * KBS + bo[n] + 1920);
#pragma unroll
    for (int m = 0; m < 4; ++m) af[ks][m] = lds_b128(sb + ((aoff0 ^ (ks * 64)) + m * 2048));
    SB0;
    mm(ks ^ 1);
    SB0;
  };
  unsigned sb = lds0;
  auto run = [&](int n) {
#pragma unroll 1
    for (int kt = 0; kt < n; ++kt) {
      bar();
      step(sb, 0);
      step(sb, 1);
      sb += K1_STAGE; if (sb == lds0 + 3 * K1_STAGE) sb = lds0;
    }
  };
  run(1);
  if (panel < 10) {
#pragma unroll
    for (int h = 0; h < 2; ++h) {
      const bf16x8 b0 = wm == 0 ? bfr[0][h] : bfr[0][2 + h], b1 = wm == 0 ? bfr[1][h] : bfr[1][2 + h];
      const int tt = wm == 0 ? tile[h] : tile[2 + h];
      f32x4 d = MFMA(b0, ones, ((f32x4){0.f, 0.f, 0.f, 0.f}));
      d = MFMA(b1, ones, d);
      csum_store(ws, WS_GC + (cbi * 10 + panel) * 2048 + tt * 128, d, fr, g, tl);
    }
  }
  run(9);
  mm(1);
  STAMP(1);
  bar();
  STAMP(10);
  const unsigned L_ST = lds0 + L_MISC, L_CS = lds0 + L_MISC + 5120;
  const unsigned L_K = lds0, L_V = L_K + 20480, L_Q = lds0 + 122880;
  float mr[4], rinv[4];
#pragma unroll
  for (int m = 0; m < 4; ++m) { const int rl = wm * 64 + m * 16 + fr; mr[m] = ldsf(L_ST + rl * 4); rinv[m] = ldsf(L_ST + 512 + rl * 4); }
#pragma unroll
  for (int n = 0; n < 4; ++n) {
    if (n < 3 || wc < 3) {
      const int t = tile[n];
      const f32x4 cs = ldsf4(L_CS + (t * 16 + 4 * g) * 4);
      unsigned dst, dstep;
      if (t < 5) { dst = L_Q + (wm * 12 + (t >> 1)) * 1024 + l * 16 + (t & 1) * 8; dstep = 3072; }
      else {
        const int kb = wm * 8 + (fr >> 3), pos = (fr & 7) ^ ((fr >> 3) << 2);
        dst = (t < 10 ? L_K + (t - 5) * 256 : L_V + (t - 10) * 256) + kb * 1280 + pos * 32 + g * 8; dstep = 2560;
      }
      if (t < 10) {
#pragma unroll
        for (int m = 0; m < 4; ++m) {
          f32x4 v = acc[m][n] * rinv[m] + cs * mr[m];
#pragma unroll
          for (int j = 0; j < 4; ++j) v[j] = phi_f(v[j]);
          *(LAS u32x2*)(uintptr_t)(dst + m * dstep) = (u32x2){cvt_pk(v[0], v[1]), cvt_pk(v[2], v[3])};
        }
      } else {
#pragma unroll
        for (int m = 0; m < 4; ++m) {
          f32x4 v = acc[m][n] * rinv[m] + cs * mr[m];
          *(LAS u32x2*)(uintptr_t)(dst + m * dstep) = (u32x2){cvt_pk(v[0], v[1]), cvt_pk(v[2], v[3])};
        }
      }
    }
  }
  STAMP(8);
  bar();
  {
    const unsigned troff = (unsigned)(g * 5 * 256 + ((4 * g1 + q) * 32) + p * 8);
#pragma unroll 1
    for (int job = w; job < 18; job += 8) {
      int i, j;
      if (job < 13) { i = (job + 1) / 3; j = job + 1 - 3 * i + i - 1; }
      else { i = job - 13; j = -1; }
      f32x4 d = {0.f, 0.f, 0.f, 0.f};
#pragma unroll
      for (int ks = 0; ks < 4; ++ks) {
        unsigned a0 = L_K + troff + (ks * 20 + i) * 256;
        bf16x8 ka = lds_tr(a0, a0 ^ 128);
        bf16x8 vb = ones;
        if (j >= 0) { unsigned b0 = L_V + troff + (ks * 20 + j) * 256; vb = lds_tr(b0, b0 ^ 128); }
        d = MFMA(ka, vb, d);
      }
      st16(rws, WS_KVP + (unsigned)(((cbi * 18 + job) * 16 + panel) * 1024) + l * 16, __builtin_bit_cast(u32x4, d));
    }
    const unsigned pqb = WS_PQ + (unsigned)(((panel * 8 + cbi) * 8 + w) * 3) * 1024 + l * 16;
    const u32x4 q0 = *(LAS u32x4*)(uintptr_t)(L_Q + w * 3072 + l * 16), q1 = *(LAS u32x4*)(uintptr_t)(L_Q + w * 3072 + 1024 + l * 16);
    const u32x2 q2 = *(LAS u32x2*)(uintptr_t)(L_Q + w * 3072 + 2048 + l * 16);
    st16(rws, pqb, q0); st16(rws, pqb + 1024, q1); st16(rws, pqb + 2048, (u32x4){q2[0], q2[1], 0u, 0u});
  }
  STAMP(9);
  wait_vm<0>();
  bar();
  if (tid == 0) st8u(ws, WS_F1 + (panel * 24 + cbi) * 64, tag);
}

DI void p1z_consumer(const Params& P, rsrc_t rws, int w, int panel, int cbj, unsigned lds0, u64 tag) {
  char* ws = P.ws;
  const int tid = threadIdx.x, l = tid & 63;
  const int wm = w & 3, wn = w >> 2;
  const int g = l >> 4, fr = l & 15, q = (l & 15) >> 2, p = l & 3, g1 = g & 1;
  const unsigned aoff0 = (unsigned)((wm * 32 + fr) * 128 + ((g ^ (l & 7)) << 4));
  unsigned bo[3];
  int ca[3];
#pragma unroll
  for (int n = 0; n < 3; ++n) {
    const int c = wn * 3 + n;
    ca[n] = c;
    int pc = c + g1; if (pc == 6) pc = 0;
    bo[n] = 16384u + g * 1536 + q * 192 + pc * 32 + p * 8;
  }
  const short one = 0x3F80;
  const bf16x8 ones = {one, one, one, one, one, one, one, one};
  const bf16x8 zeros = {0, 0, 0, 0, 0, 0, 0, 0};
  f32x4 accA[2][3], accB[2][3];
#pragma unroll
  for (int m = 0; m < 2; ++m)
#pragma unroll
    for (int n = 0; n < 3; ++n) { accA[m][n] = (f32x4){0.f, 0.f, 0.f, 0.f}; accB[m][n] = (f32x4){0.f, 0.f, 0.f, 0.f}; }
  const unsigned tl = (unsigned)tag, th = tl * 0x9E3779B1u ^ (unsigned)(tag >> 32), tg = (tid & 1) ? th : tl;
  bf16x8 af[2][2], bfr[2][3];
  af[1][0] = af[1][1] = zeros; bfr[1][0] = bfr[1][1] = bfr[1][2] = zeros;
  auto rd = [&](unsigned sb, int ks) {
#pragma unroll
    for (int m = 0; m < 2; ++m) af[ks][m] = lds_b128(sb + ((aoff0 ^ (ks * 64)) + m * 2048));
#pragma unroll
    for (int n = 0; n < 3; ++n) { const unsigned o = sb + ks * 4 * 1536 + bo[n]; bfr[ks][n] = lds_tr(o, o + 768); }
    SB0;
  };
  auto mmA = [&](int ks) {
#pragma unroll
    for (int n = 0; n < 3; ++n)
#pragma unroll
      for (int m = 0; m < 2; ++m) accA[m][n] = MFMA(af[ks][m], bfr[ks][n], accA[m][n]);
    SB0;
  };
  auto mmB = [&](int ks) {
#pragma unroll
    for (int n = 0; n < 3; ++n)
#pragma unroll
      for (int m = 0; m < 2; ++m) accB[m][n] = MFMA(bfr[ks][n], af[ks][m], accB[m][n]);
    SB0;
  };
  const unsigned L_ST = lds0 + L_MISC, L_CS = lds0 + L_MISC + 5120;
  const unsigned L_RS = lds0 + L_MISC + 2048;
  const unsigned L_FS = lds0 + L_MISC + 4096;
  unsigned sb = lds0;
  auto nxt = [&]() { sb += K1_STAGE; if (sb == lds0 + 3 * K1_STAGE) sb = lds0; };
  auto runA = [&](int n) {
#pragma unroll 1
    for (int kt = 0; kt < n; ++kt) { bar(); rd(sb, 0); mmA(1); rd(sb, 1); mmA(0); nxt(); }
  };
  auto runB = [&](int n) {
#pragma unroll 1
    for (int kt = 0; kt < n; ++kt) { bar(); rd(sb, 0); mmB(1); rd(sb, 1); mmB(0); nxt(); }
  };
  auto cpub = [&](int blk) {
    if (panel < 10 && wm == 0) {
#pragma unroll
      for (int n = 0; n < 3; ++n) {
        f32x4 d = MFMA(bfr[0][n], ones, ((f32x4){0.f, 0.f, 0.f, 0.f}));
        d = MFMA(bfr[1][n], ones, d);
        csum_store(ws, WS_GC + (blk * 10 + panel) * 2048 + ca[n] * 128, d, fr, g, tl);
      }
    }
  };
  {
    Side S; side_issue(P, S, panel * 128 + 64 + cbj * 8 + w, w);
    side_finish(P, rws, S, panel * 128 + 64 + cbj * 8 + w, w, tl, th);
  }
  runA(1);
  cpub(8 + cbj);
  runA(6);
  const int tc = tid - 256;
  {
    u64 cv[10];
    if (tid < 256) cv[0] = ld8(ws, WS_GX + panel * 2048 + tid * 8);
    else if (tc < 96) csum_issue(ws, 8 + cbj, tc, cv);
    runA(3);
    if (tid < 256) xstat_gather(ws, panel, L_ST, tid, tg, cv[0]);
    else if (tc < 96) csum_gather(ws, 8 + cbj, tc, tl, cv, L_CS);
  }
  bar(); rd(sb, 0); mmA(1); rd(sb, 1); mmB(0); nxt();
  cpub(16 + cbj);
  STAMP(8);
  {
    float cn[3];
#pragma unroll
    for (int n = 0; n < 3; ++n) cn[n] = ldsf(L_CS + (ca[n] * 16 + fr) * 4);
#pragma unroll
    for (int m = 0; m < 2; ++m) {
      const int rl = wm * 32 + m * 16 + 4 * g;
      const f32x4 mr = ldsf4(L_ST + rl * 4), rinv = ldsf4(L_ST + 512 + rl * 4);
      f32x4 rs = {0.f, 0.f, 0.f, 0.f}, rq = {0.f, 0.f, 0.f, 0.f};
#pragma unroll
      for (int n = 0; n < 3; ++n) {
        f32x4 v = accA[m][n] * rinv + mr * cn[n];
#pragma unroll
        for (int j = 0; j < 4; ++j) v[j] = gelu_t(v[j]);
        rs += v; rq += v * v;
        accA[m][n] = v;
      }
#pragma unroll
      for (int j = 0; j < 4; ++j) { rs[j] = sum16(rs[j]); rq[j] = sum16(rq[j]); }
      if (fr == 0) {
        stsf4(L_RS + ((wn * 128 + rl) * 2) * 4, (f32x4){rs[0], rq[0], rs[1], rq[1]});
        stsf4(L_RS + ((wn * 128 + rl) * 2 + 4) * 4, (f32x4){rs[2], rq[2], rs[3], rq[3]});
      }
    }
  }
  bar(); rd(sb, 0);
  float part = 0.f;
  const unsigned gbase = WS_GR + (unsigned)(panel * 8) * 2048 + tid * 8;
  if (tid < 256) {
    part = ldsf(L_RS + tid * 4) + ldsf(L_RS + 1024 + tid * 4);
    st8u(ws, gbase + cbj * 2048, ((u64)tg << 32) | __float_as_uint(part));
  }
  mmB(1); rd(sb, 1); mmB(0); nxt();
  STAMP(9);
  runB(2);
  {
    u64 cv[10];
    if (tid < 256) {
#pragma unroll
      for (int i = 1; i < 8; ++i) cv[i] = ld8(ws, gbase + ((cbj + i) & 7) * 2048);
    } else if (tc < 96) csum_issue(ws, 16 + cbj, tc, cv);
    runB(2);
    if (tid < 256) {
      float tot;
#pragma unroll 1
      for (unsigned sp = 0; sp < SPIN_CAP; ++sp) {
        bool ok = true; tot = part;
#pragma unroll
        for (int i = 1; i < 8; ++i) { ok &= (unsigned)(cv[i] >> 32) == tg; tot += __uint_as_float((unsigned)cv[i]); }
        if (__all(ok)) break;
#pragma unroll
        for (int i = 1; i < 8; ++i) cv[i] = ld8(ws, gbase + ((cbj + i) & 7) * 2048);
      }
      float other = dpp<0xB1>(tot);
      if ((tid & 1) == 0) {
        float m = tot * (1.f / FH);
        float v = rbf(fmaxf(other * (1.f / FH) - m * m, 0.f));
        float r = 1.f / rbf(sqrtf(rbf(v + 1e-5f)));
        stsf(L_FS + (tid >> 1) * 4, -rbf(m) * r);
        stsf(L_FS + 512 + (tid >> 1) * 4, r);
      }
    } else if (tc < 96) csum_gather(ws, 16 + cbj, tc, tl, cv, L_CS + 512);
  }
  bar(); rd(sb, 0);
  {
    f32x4 mr[2], ri[2];
#pragma unroll
    for (int m = 0; m < 2; ++m) { const int rl = wm * 32 + m * 16 + 4 * g; mr[m] = ldsf4(L_FS + rl * 4); ri[m] = ldsf4(L_FS + 512 + rl * 4); }
#pragma unroll
    for (int n = 0; n < 3; ++n) {
      f32x4 a = accA[0][n] * ri[0] + mr[0], b = accA[1][n] * ri[1] + mr[1];
      st16(rws, WS_Z2N + (unsigned)(((cbj * 64 + panel * 4 + wm) * 6 + ca[n]) * 1024) + l * 16,
           (u32x4){cvt_pk(a[0], a[1]), cvt_pk(a[2], a[3]), cvt_pk(b[0], b[1]), cvt_pk(b[2], b[3])});
    }
  }
  mmB(1); rd(sb, 1); mmB(0); nxt();
  STAMP(10);
  runB(2);
  wait_vm<0>();
  bar();
  if (tid == 0) st8u(ws, WS_F1 + (panel * 24 + 8 + cbj) * 64, tag);
  STAMP(11);
  rd(sb, 0); mmB(1); rd(sb, 1); mmB(0);
  mmB(1);
  STAMP(1);
  bar();
  {
    f32x4 cn[3];
#pragma unroll
    for (int n = 0; n < 3; ++n) { const unsigned a = L_CS + 512 + (ca[n] * 16 + 4 * g) * 4; cn[n] = ldsf4(a); }
    float mr[2], rinv[2];
#pragma unroll
    for (int m = 0; m < 2; ++m) { const int rl = wm * 32 + m * 16 + fr; mr[m] = ldsf(L_ST + rl * 4); rinv[m] = ldsf(L_ST + 512 + rl * 4); }
#pragma unroll
    for (int n = 0; n < 3; ++n) {
      u32x4 o;
#pragma unroll
      for (int m = 0; m < 2; ++m) {
        f32x4 v = accB[m][n] * rinv[m] + cn[n] * mr[m];
#pragma unroll
        for (int j = 0; j < 4; ++j) v[j] = gelu_t(v[j]);
        o[2 * m] = cvt_pk(v[0], v[1]); o[2 * m + 1] = cvt_pk(v[2], v[3]);
      }
      st16(rws, WS_Z1 + (unsigned)(((panel * 8 + cbj) * 4 + wm) * 6 + ca[n]) * 1024 + l * 16, o);
    }
  }
  wait_vm<0>();
  bar();
  if (tid == 0) st8u(ws, WS_F1 + (panel * 24 + 16 + cbj) * 64, tag);
}

__global__ __launch_bounds__(768, 3) void mega_kernel(Params P) {
  extern __shared__ __attribute__((aligned(16))) char smem[];
  const unsigned lds0 = (unsigned)(uintptr_t)(LAS char*)smem;
  char* ws = P.ws;
  const int tid = threadIdx.x, l = tid & 63;
  const int w = __builtin_amdgcn_readfirstlane(tid >> 6);
  const int g = l >> 4, fr = l & 15;
  const int b = blockIdx.x, xcd = b & 7, bi = b >> 3;
  const int panel1 = (xcd & 1) * 8 + (bi & 7), ci = bi >> 3, cb1 = (xcd >> 1) * 2 + (ci & 1);
  const bool isq = ci < 2;
  const int panel2 = (xcd >> 1) * 8 + (bi & 7), cb2 = (xcd & 1) * 4 + (bi >> 3);
  const int panel3 = xcd * 4 + (bi & 3), cb3 = bi >> 2;
  STAMP(0);
  const u64 tag = ld8(ws, WS_EP) + 1;

  if (w >= 8) {
    const int lw = w - 8;
    const unsigned vA0 = (unsigned)(((l >> 3) * D_ + (((l & 7) ^ (l >> 3)) << 3)) * 2);
    const unsigned vA1 = vA0 + 8 * D_ * 2 - 1024, vA2 = vA0 + 16 * D_ * 2 - 2048, vA3 = vA0 + 24 * D_ * 2 - 3072;
    const char* gA = (const char*)(P.x + (size_t)(panel1 * 128 + lw * 32) * D_);
    const unsigned v20 = (unsigned)(((l >> 3) * S_ + (((l & 7) ^ (l >> 4)) << 3)) * 2);
    const unsigned v21 = (unsigned)((((l >> 3) + 8) * S_ + (((l & 7) ^ ((l >> 4) | 4)) << 3)) * 2) - 1024;
    const char* gA2 = (const char*)(P.wsp + (size_t)(panel2 * 64 + lw * 16) * S_);
    auto ldA = [&](int kt, unsigned sl) { gl2(gA2 + kt * 128, sl + lw * 2048, v20, v21); };
    auto sl2 = [&](int kt) { return lds0 + ((kt + 4) % 6) * P2_SLOT; };
    const unsigned tl = (unsigned)tag, th = tl * 0x9E3779B1u ^ (unsigned)(tag >> 32);
    const int lt = tid - 512;
    auto gather = [&](int blk, int ncol, unsigned dst, bool xs) {
      u64 cv[10]; u64 gv = 0;
      const int tc = lt < ncol ? lt : 0;
      if (xs) gv = ld8(ws, WS_GX + panel1 * 2048 + lt * 8);
      csum_issue(ws, blk, tc, cv);
      if (xs) xstat_gather(ws, panel1, lds0 + L_MISC, lt, (lt & 1) ? th : tl, gv);
      csum_gather(ws, blk, tc, tl, cv, dst);
    };
    const unsigned l16 = l * 16;
    const char* gB2 = ws + WS_Z2N + (size_t)cb2 * 64 * 6144 + lw * 3072;
    auto ldB = [&](int kt, unsigned sl) { gl3c(gB2 + kt * 12288, sl + 8192 + lw * 3072, l16); };
    const unsigned zfl = WS_F1 + ((l & 15) * 24 + 8 + cb2) * 64;
    auto try_z = [&]() {
      const u64 v = l < 16 ? ld8(ws, zfl) : tag;
      return (bool)__builtin_amdgcn_readfirstlane((int)__all(v == tag));
    };
    bool early = false;
    const int rot = panel1 < 10 ? panel1 : panel1 - 10;
    auto krot = [&](int kt) { int k = kt + rot; if (k >= 20) k -= 20; else if (k >= 10) k -= 10; return k; };
    if (isq) {
      gl4(gA + rot * 128, lds0 + lw * 4096, vA0, vA1, vA2, vA3);
      unsigned vB[4];
#pragma unroll
      for (int i = 0; i < 4; ++i) {
        int c = i * 64 + l; if (c > 239) c = 239;
        const int row = c / 30, cc = c - row * 30, seg = cc / 10, ch = cc - seg * 10;
        vB[i] = (unsigned)((row * NQKV + seg * 640 + cb1 * 80 + ch * 8) * 2) + 4096 - i * 1024;
      }
      const char* gB = (const char*)P.wqkv + (size_t)lw * 16 * NQKV * 2 - 4096;
      auto stageB = [&](int kt, unsigned sb) {
        const char* bp = gB + (size_t)krot(kt) * 64 * NQKV * 2;
        gl4(bp, sb + 16384 + lw * 2 * KBS, vB[0], vB[1], vB[2], vB[3]);
        gl4(bp + 8 * NQKV * 2, sb + 16384 + (lw * 2 + 1) * KBS, vB[0], vB[1], vB[2], vB[3]);
      };
      auto stage = [&](int kt, unsigned sb) { gl4(gA + krot(kt) * 128, sb + lw * 4096, vA0, vA1, vA2, vA3); stageB(kt, sb); };
      stageB(0, lds0); stage(1, lds0 + K1_STAGE);
      wait_vm<12>(); bar();
      stage(2, lds0 + 2 * K1_STAGE);
      unsigned sb = lds0, sc = lds0 + K1_STAGE;
#pragma unroll 1
      for (int kt = 1; kt < 8; ++kt) {
        wait_vm<12>(); bar();
        if (kt == 2) STAMP(10);
        if (kt == 7) STAMP(11);
        stage(kt + 2, sb);
        sb = sc; sc += K1_STAGE; if (sc == lds0 + 3 * K1_STAGE) sc = lds0;
      }
      wait_vm<12>(); bar();
      ldA(5, sl2(5)); ldA(0, sl2(0));
      wait_vm<4>(); bar();
      ldA(1, sl2(1));
      gather(cb1, 240, lds0 + L_MISC + 5120, true);
      bar();
      ldA(4, sl2(4));
#pragma unroll 1
      for (int t = 0; t < 3 && !early; ++t) early = try_z();
      if (early) { ldB(0, sl2(0)); ldB(1, sl2(1)); ldB(4, sl2(4)); ldB(5, sl2(5)); }
      bar();
      if (!early) { early = try_z(); if (early) { ldB(0, sl2(0)); ldB(1, sl2(1)); ldB(4, sl2(4)); ldB(5, sl2(5)); } }
      bar();
    } else {
      gl4(gA + rot * 128, lds0 + lw * 4096, vA0, vA1, vA2, vA3);
      unsigned vB[3];
#pragma unroll
      for (int i = 0; i < 3; ++i) {
        const int c = i * 64 + l, row = c / 12, cc = c - row * 12;
        int pc = (cc >> 1) - (row >> 3); if (pc < 0) pc = 5;
        vB[i] = (unsigned)((row * FIN + cb1 * 96 + pc * 16 + (cc & 1) * 8) * 2) + 4096 - i * 1024;
      }
      const char* gB = (const char*)P.win + (size_t)lw * 16 * FIN * 2 - 4096;
      auto stageB = [&](int kt, unsigned sb) {
        const int k = krot(kt);
        gl3(gB + (kt < 10 ? FH * 2 : 0) + (size_t)k * 64 * FIN * 2, sb + 16384 + lw * 3072, vB[0], vB[1], vB[2]);
      };
      auto stage = [&](int kt, unsigned sb) {
        const int k = krot(kt);
        gl4(gA + k * 128, sb + lw * 4096, vA0, vA1, vA2, vA3); stageB(kt, sb);
      };
      stageB(0, lds0); stage(1, lds0 + K1_STAGE);
      wait_vm<7>(); bar();
      stage(2, lds0 + 2 * K1_STAGE);
      unsigned sb = lds0, sc = lds0 + K1_STAGE;
      auto runL = [&](int k0, int k1) {
#pragma unroll 1
        for (int kt = k0; kt < k1; ++kt) {
          wait_vm<7>(); bar();
          stage(kt + 2, sb);
          sb = sc; sc += K1_STAGE; if (sc == lds0 + 3 * K1_STAGE) sc = lds0;
        }
      };
      runL(1, 18);
      ldA(0, sl2(0)); ldA(4, sl2(4));
      wait_vm<11>(); bar();
      ldA(1, sl2(1));
      wait_vm<6>(); bar();
      bar();
      ldA(5, sl2(5));
#pragma unroll 1
      for (int t = 0; t < 2 && !early; ++t) early = try_z();
      if (early) { ldB(0, sl2(0)); ldB(1, sl2(1)); ldB(4, sl2(4)); ldB(5, sl2(5)); }
      bar();
    }
    STAMP(2);
    if (!early) {
      wait_words(ws, zfl, l < 16, tag);
      ldB(0, sl2(0)); ldB(1, sl2(1)); ldB(4, sl2(4)); ldB(5, sl2(5));
    }
    STAMP(3);
    ldA(2, sl2(2)); ldA(3, sl2(3)); ldB(2, sl2(2)); ldB(3, sl2(3));
    wait_vm<16>(); bar();
    {
      unsigned sl = sl2(0);
      auto ldU = [&](int u) {
        ldA(2 * u, sl); ldB(2 * u, sl); ldA(2 * u + 1, sl + P2_SLOT); ldB(2 * u + 1, sl + P2_SLOT);
        sl += 2 * P2_SLOT; if (sl == lds0 + 6 * P2_SLOT) sl = lds0;
      };
      wait_vm<0>(); bar(); ldU(3);
#pragma unroll 1
      for (int u = 2; u < 14; ++u) {
        wait_vm<10>(); bar();
        if (u == 4) STAMP(8);
        if (u == 10) STAMP(9);
        ldU(u + 2);
      }
    }
    wait_vm<10>(); bar();
    wait_words(ws, WS_F1 + (((lw * 64 + l) >> 4) * 24 + ((l & 8) ? 8 : 0) + (l & 15)) * 64, true, tag);
    wait_vm<0>(); bar();
    STAMP(4);
    const char* gA3 = ws + WS_OH + (size_t)panel3 * 44 * 4096 + lw * 4096;
    const char* gB3 = ws + WS_WF + (size_t)cb3 * 44 * 5120 + lw * 5120;
    auto ldB3 = [&](int u, unsigned sl) {
      gl4c(gB3 + u * 20480, sl + 16384 + lw * 5120, l16);
      glds16c(gB3 + u * 20480 + 4096, l16, sl + 16384 + lw * 5120 + 4096);
    };
    auto ldA3 = [&](int u, unsigned sl) { gl4c(gA3 + u * 16384, sl + lw * 4096, l16); };
    bar();
    ldB3(0, lds0); ldB3(1, lds0 + P3_SLOT); ldB3(2, lds0 + 2 * P3_SLOT);
    bar(); bar();
    ldB3(3, lds0 + 3 * P3_SLOT);
    wait_words(ws, WS_F2 + (panel3 * 8 + (l & 7)) * 64, l < 8, tag);
    STAMP(5);
#pragma unroll
    for (int j = 0; j < 4; ++j) ldA3(j, lds0 + j * P3_SLOT);
    wait_vm<12>(); bar();
    wait_vm<8>(); bar(); ldA3(4, lds0); ldB3(4, lds0);
    wait_vm<13>(); bar(); ldA3(5, lds0 + P3_SLOT); ldB3(5, lds0 + P3_SLOT);
    {
      unsigned sl = lds0 + 2 * P3_SLOT;
#pragma unroll 1
      for (int u = 3; u < 8; ++u) {
        wait_vm<18>(); bar();
        ldA3(u + 3, sl); ldB3(u + 3, sl);
        sl += P3_SLOT; if (sl == lds0 + 4 * P3_SLOT) sl = lds0;
      }
    }
    wait_vm<18>(); bar(); wait_vm<9>(); bar(); wait_vm<0>(); bar();
    bar(); bar();
    if (b == 0 && lw == 0) {
#pragma unroll
      for (int i = 0; i < 4; ++i) wait_words(ws, WS_F2 + (i * 64 + l) * 64, true, tag);
      if (l == 0) st8u(ws, WS_EP, tag);
    }
    STAMP(6);
    return;
  }

  const rsrc_t rws = __builtin_amdgcn_make_buffer_rsrc(ws, 0, (int)WS_END, 0x00020000);
  if (isq) p1q_consumer(P, rws, w, panel1, cb1, lds0, tag);
  else p1z_consumer(P, rws, w, panel1, cb1, lds0, tag);
  STAMP(2);

  {
    const int wt = w & 3, kh = w >> 2;
    const int rsw = ((wt * 16 + fr) >> 1) & 7;
    const unsigned ao0 = (unsigned)((wt * 16 + fr) * 128 + (((4 * kh + (g >> 1)) ^ rsw) << 4) + (g & 1) * 8);
    const unsigned ao1 = (unsigned)((wt * 16 + fr) * 128 + (((4 * kh + 2 + (g >> 1)) ^ rsw) << 4) + (g & 1) * 8);
    const unsigned bo = (unsigned)(8192 + kh * 6144 + l * 16);
    const bf16x8 zeros = {0, 0, 0, 0, 0, 0, 0, 0};
    f32x4 acc[6];
#pragma unroll
    for (int n = 0; n < 6; ++n) acc[n] = (f32x4){0.f, 0.f, 0.f, 0.f};
    bf16x8 af[2], bfr[2][6];
    af[1] = zeros;
#pragma unroll
    for (int n = 0; n < 6; ++n) bfr[1][n] = zeros;
    const int zi = (bi & 15) * 8 + xcd;
    const bool reducer = isq && zi < 72;
    const int rj = zi & 7, rf = zi >> 3;
    const int row = panel2 * 64 + wt * 16 + fr;
    u32x2 zreg[6];
    bf16x8 pf[3];
    const float bs = bf2f(P.bsp[row]);
    unsigned sl = lds0 + 4 * P2_SLOT;
    auto step = [&](int cur, int kt) {
      if (cur == 0) bar();
      af[cur] = lds_2b64(sl + ao0, sl + ao1);
#pragma unroll
      for (int n = 0; n < 6; ++n) bfr[cur][n] = lds_b128(sl + bo + n * 1024);
      SB0;
#pragma unroll
      for (int n = 0; n < 6; ++n) acc[n] = MFMA(bfr[cur ^ 1][n], af[cur ^ 1], acc[n]);
      SB0;
      sl += P2_SLOT; if (sl == lds0 + 6 * P2_SLOT) sl = lds0;
    };
    auto run = [&](int k0, int k1) {
#pragma unroll 1
      for (int kt = k0; kt < k1; kt += 2) { step(0, kt); step(1, kt + 1); }
    };
    if (reducer) {
      const int et = (rf + 1) >> 1, st = rf == 0 ? 0 : ((rf + 1) & 1) + (et >= 3 ? 1 : 0);
      run(0, 2);
      f32x4 pv[8];
      wait_words(ws, WS_F1 + ((l & 15) * 24 + rj) * 64, l < 16, tag);
#pragma unroll
      for (int t4 = 0; t4 < 4; ++t4) {
        const int i = 2 * st + (t4 & 1);
        const bool ok = i < 5 && (t4 >= 2 || (et - i <= 1 && i - et <= 1));
        const int job = t4 >= 2 ? 13 + i : 2 * i + et;
#pragma unroll
        for (int pp = 0; pp < 2; ++pp) {
          pv[t4 * 2 + pp] = (f32x4){0.f, 0.f, 0.f, 0.f};
          if (ok) pv[t4 * 2 + pp] = __builtin_bit_cast(f32x4, ld16(rws, WS_KVP + (unsigned)(((rj * 18 + job) * 16 + w * 2 + pp) * 1024) + l * 16));
        }
      }
      run(2, 6);
      const unsigned L_R = lds0 + L_KVT;
      if (w >= 4) {
#pragma unroll
        for (int t4 = 0; t4 < 4; ++t4) stsf4(L_R + ((w - 4) * 4 + t4) * 1024 + l * 16, pv[t4 * 2] + pv[t4 * 2 + 1]);
      }
      run(6, 8);
      if (w < 4) {
#pragma unroll
        for (int t4 = 0; t4 < 4; ++t4) {
          f32x4 v = pv[t4 * 2] + pv[t4 * 2 + 1] + ldsf4(L_R + (w * 4 + t4) * 1024 + l * 16);
          stsf4(L_R + (w * 4 + t4) * 1024 + l * 16, v);
        }
      }
      run(8, 10);
      if (w < 2) {
        f32x4 v[2];
#pragma unroll
        for (int h2 = 0; h2 < 2; ++h2) {
          v[h2] = (f32x4){0.f, 0.f, 0.f, 0.f};
#pragma unroll
          for (int ww = 0; ww < 4; ++ww) v[h2] += ldsf4(L_R + (ww * 4 + w * 2 + h2) * 1024 + l * 16);
          const int he = (et * 16 + fr) / 20;
#pragma unroll
          for (int r = 0; r < 4; ++r) { const int d = (2 * st + h2) * 16 + 4 * g + r; if (d / 20 != he) v[h2][r] = 0.f; }
        }
        st16(rws, WS_KVT + (unsigned)(((rj * 2 + w) * 9 + rf) * 1024) + l * 16,
             (u32x4){cvt_pk(v[0][0], v[0][1]), cvt_pk(v[0][2], v[0][3]), cvt_pk(v[1][0], v[1][1]), cvt_pk(v[1][2], v[1][3])});
      }
      run(10, 14);
      wait_vm<0>();
      run(14, 16);
      if (tid == 0) st8u(ws, WS_FK + (rj * 16 + rf) * 64, tag);
      run(16, 20);
    } else {
      run(0, 20);
    }
    const unsigned foff = kh == 1 ? (l < 9 ? WS_FK + (cb2 * 16 + l) * 64 : WS_F1 + ((panel2 >> 1) * 24 + cb2) * 64)
                                  : WS_F1 + ((panel2 >> 1) * 24 + 16 + cb2) * 64;
    const u64 fv = l < 10 ? ld8(ws, foff) : tag;
    run(20, 24);
    if (!__all(fv == tag)) wait_words(ws, foff, l < 10, tag);
    if (kh == 1) {
      const unsigned pqo = WS_PQ + (unsigned)((((panel2 >> 1) * 8 + cb2) * 8 + (panel2 & 1) * 4 + wt) * 3) * 1024 + l * 16;
#pragma unroll
      for (int s = 0; s < 3; ++s) pf[s] = __builtin_bit_cast(bf16x8, ld16(rws, pqo + s * 1024));
#pragma unroll
      for (int i = 0; i < 5; ++i) {
        int ins = wt + 4 * i; if (ins >= 18) ins = 17;
        ins = __builtin_amdgcn_readfirstlane(ins);
        glds16c(ws, WS_KVT + cb2 * 18432 + ins * 1024 + l * 16, __builtin_amdgcn_readfirstlane(lds0 + L_KVT + ins * 1024));
      }
    } else {
      const int m128 = (panel2 & 1) * 4 + wt;
      const unsigned zo = WS_Z1 + (unsigned)((((panel2 >> 1) * 8 + cb2) * 4 + (m128 >> 1)) * 6) * 1024 + l * 16 + (m128 & 1) * 8;
#pragma unroll
      for (int n = 0; n < 6; ++n) zreg[n] = __builtin_amdgcn_raw_buffer_load_b64(rws, zo + n * 1024, 0, 16);
    }
    run(24, 32);
#pragma unroll
    for (int n = 0; n < 6; ++n) acc[n] = MFMA(bfr[1][n], af[1], acc[n]);
    STAMP(4);
    wait_vm<0>();
    bar();
    const unsigned L_RED = lds0 + 3 * P3_SLOT;
    if (kh == 1) {
#pragma unroll
      for (int n = 0; n < 6; ++n) stsf4(L_RED + (wt * 6 + n) * 1024 + l * 16, acc[n]);
    }
    bar();
    if (kh == 1) {
      u32x2 ov[5];
#pragma unroll
      for (int et = 0; et < 5; ++et) {
        f32x4 num = {0.f, 0.f, 0.f, 0.f}, den = {0.f, 0.f, 0.f, 0.f};
        const int st0 = et >= 3 ? 1 : 0, nst = et == 0 ? 1 : 2, f0 = et == 0 ? 0 : 1 + (et - 1) * 2;
#pragma unroll
        for (int s = 0; s < nst; ++s) {
          bf16x8 ka = lds_b128(lds0 + L_KVT + (f0 + s) * 1024 + l * 16), za = lds_b128(lds0 + L_KVT + (9 + f0 + s) * 1024 + l * 16);
          num = MFMA(ka, pf[st0 + s], num); den = MFMA(za, pf[st0 + s], den);
        }
        float v[4];
#pragma unroll
        for (int j = 0; j < 4; ++j) v[j] = num[j] * __builtin_amdgcn_rcpf(den[j] + 1e-6f);
        ov[et] = (u32x2){cvt_pk(v[0], v[1]), cvt_pk(v[2], v[3])};
      }
      const unsigned ob = WS_OH + (unsigned)((panel2 * 44 + ((cb2 * 5) >> 1)) * 4 + wt) * 1024 + l * 16;
      if ((cb2 & 1) == 0) {
        st16(rws, ob, (u32x4){ov[0][0], ov[0][1], ov[1][0], ov[1][1]});
        st16(rws, ob + 4096, (u32x4){ov[2][0], ov[2][1], ov[3][0], ov[3][1]});
        st8(ws, ob + 8192, ov[4]);
      } else {
        st8(ws, ob + 8, ov[0]);
        st16(rws, ob + 4096, (u32x4){ov[1][0], ov[1][1], ov[2][0], ov[2][1]});
        st16(rws, ob + 8192, (u32x4){ov[3][0], ov[3][1], ov[4][0], ov[4][1]});
      }
      wait_vm<0>();
    } else {
#pragma unroll
      for (int n = 0; n < 6; ++n) acc[n] += ldsf4(L_RED + (wt * 6 + n) * 1024 + l * 16);
      u32x2 hv[6];
#pragma unroll
      for (int n = 0; n < 6; ++n) {
        f32x4 gg = acc[n] + bs;
        hv[n] = (u32x2){cvt_pk(lo16(zreg[n][0]) * gg[0], hi16(zreg[n][0]) * gg[1]), cvt_pk(lo16(zreg[n][1]) * gg[2], hi16(zreg[n][1]) * gg[3])};
      }
      const unsigned hb = WS_OH + (unsigned)((panel2 * 44 + 20 + cb2 * 3) * 4 + wt) * 1024 + l * 16;
#pragma unroll
      for (int s = 0; s < 3; ++s) st16(rws, hb + s * 4096, (u32x4){hv[2 * s][0], hv[2 * s][1], hv[2 * s + 1][0], hv[2 * s + 1][1]});
      wait_vm<0>();
    }
    bar();
    if (tid == 0) st8u(ws, WS_F2 + (panel2 * 8 + cb2) * 64, tag);
    STAMP(5);
  }
  {
    const int wsr = w & 3, kh = w >> 2;
    const unsigned ao = (unsigned)(kh * 8192 + wsr * 1024 + l * 16);
    const unsigned bb = 16384u + kh * 10240 + l * 16;
    const bf16x8 zeros = {0, 0, 0, 0, 0, 0, 0, 0};
    f32x4 acc[5];
#pragma unroll
    for (int n = 0; n < 5; ++n) acc[n] = (f32x4){0.f, 0.f, 0.f, 0.f};
    bf16x8 af[2], bfr[2][5];
    af[1] = zeros;
#pragma unroll
    for (int n = 0; n < 5; ++n) bfr[1][n] = zeros;
    u32x4 xv[2];
    const int c0 = tid, c1 = tid + 512;
    const size_t xo0 = (size_t)(panel3 * 64 + c0 / 10) * D_ + cb3 * 80 + (c0 % 10) * 8;
    const size_t xo1 = (size_t)(panel3 * 64 + c1 / 10) * D_ + cb3 * 80 + (c1 % 10) * 8;
    xv[0] = *(const u32x4*)(P.x + xo0);
    if (tid < 128) xv[1] = *(const u32x4*)(P.x + xo1);
    unsigned sb = lds0;
    auto rd = [&](int s) {
      af[s] = lds_b128(sb + ao + s * 4096);
#pragma unroll
      for (int n = 0; n < 5; ++n) bfr[s][n] = lds_b128(sb + bb + s * 5120 + n * 1024);
      SB0;
    };
    auto mm = [&](int s) {
#pragma unroll
      for (int n = 0; n < 5; ++n) acc[n] = MFMA(bfr[s][n], af[s], acc[n]);
      SB0;
    };
#pragma unroll 1
    for (int u = 0; u < 11; ++u) {
      bar();
      rd(0); mm(1);
      rd(1); mm(0);
      sb += P3_SLOT; if (sb == lds0 + 4 * P3_SLOT) sb = lds0;
    }
    mm(1);
    STAMP(6);
    bar();
#pragma unroll
    for (int n = 0; n < 5; ++n) stsf4(lds0 + (unsigned)(((kh * 64 + wsr * 16 + fr) * 84 + n * 16 + 4 * g) * 4), acc[n]);
    bar();
#pragma unroll
    for (int i = 0; i < 2; ++i) {
      if (i == 0 || tid < 128) {
        const int c = tid + i * 512, r = c / 10, ch = c - r * 10;
        const unsigned a = lds0 + (unsigned)((r * 84 + ch * 8) * 4);
        f32x4 a0 = ldsf4(a) + ldsf4(a + 64 * 84 * 4), a1 = ldsf4(a + 16) + ldsf4(a + 64 * 84 * 4 + 16);
        u32x4 o = {cvt_pk(lo16(xv[i][0]) + a0[0], hi16(xv[i][0]) + a0[1]), cvt_pk(lo16(xv[i][1]) + a0[2], hi16(xv[i][1]) + a0[3]),
                   cvt_pk(lo16(xv[i][2]) + a1[0], hi16(xv[i][2]) + a1[1]), cvt_pk(lo16(xv[i][3]) + a1[2], hi16(xv[i][3]) + a1[3])};
        *(u32x4*)(P.out + (i == 0 ? xo0 : xo1)) = o;
      }
    }
    STAMP(7);
  }
}

extern "C" void kernel_launch(void* const* d_in, const int* in_sizes, int n_in, void* d_out, int out_size,
                              void* d_ws, size_t ws_size, hipStream_t stream) {
  Params P;
  P.x = (const u16*)d_in[0];
  P.wqkv = (const u16*)d_in[1];
  P.wo = (const u16*)d_in[2];
  P.win = (const u16*)d_in[4];
  P.wsp = (const u16*)d_in[5];
  P.bsp = (const u16*)d_in[6];
  P.wout = (const u16*)d_in[7];
  P.out = (u16*)d_out;
  P.ws = (char*)d_ws;
  (void)hipFuncSetAttribute((const void*)mega_kernel, hipFuncAttributeMaxDynamicSharedMemorySize, LDS_TOTAL);
  mega_kernel<<<256, 768, LDS_TOTAL, stream>>>(P);
}
```

```cpp
#include <hip/hip_runtime.h>
#include <hip/hip_bf16.h>
#include <stdint.h>

typedef __attribute__((ext_vector_type(8))) short bf16x8;
typedef __attribute__((ext_vector_type(4))) short bf16x4;
typedef __attribute__((ext_vector_type(4))) float f32x4;
typedef __attribute__((ext_vector_type(2))) float f32x2;
typedef __attribute__((ext_vector_type(2))) __bf16 bf2;
typedef __attribute__((ext_vector_type(4))) unsigned u32x4;
typedef __attribute__((ext_vector_type(2))) unsigned u32x2;
typedef unsigned short u16;
typedef unsigned long long u64;
#define LAS __attribute__((address_space(3)))
#define GAS __attribute__((address_space(1)))
#define DI __device__ __forceinline__
#define RLX_AGENT __ATOMIC_RELAXED, __HIP_MEMORY_SCOPE_AGENT

constexpr int S_ = 2048, D_ = 640, NQKV = 1920, FIN = 1536, FH = 768;

constexpr unsigned WS_PQ  = 0;
constexpr unsigned WS_Z1  = WS_PQ + 16 * 8 * 8 * 3 * 1024;
constexpr unsigned WS_Z2N = WS_Z1 + 16 * 8 * 24 * 1024;
constexpr unsigned WS_OH  = WS_Z2N + 8 * 64 * 6 * 1024;
constexpr unsigned WS_WF  = WS_OH + 32 * 44 * 4 * 1024;
constexpr unsigned WS_KVT = WS_WF + 8 * 44 * 5 * 1024;
constexpr unsigned WS_KVP = WS_KVT + 8 * 18 * 1024;
constexpr unsigned WS_GR  = WS_KVP + 8 * 18 * 16 * 1024;
constexpr unsigned WS_GX  = WS_GR + 16 * 8 * 256 * 8;
constexpr unsigned WS_GC  = WS_GX + 2048 * 16;
constexpr unsigned WS_F1  = WS_GC + 24 * 10 * 2048;
constexpr unsigned WS_F2  = WS_F1 + 16 * 24 * 64;
constexpr unsigned WS_FK  = WS_F2 + 256 * 64;
constexpr unsigned WS_EP  = WS_FK + 128 * 64;
constexpr unsigned WS_END = WS_EP + 64;

#define STAMP(i)

DI float bf2f(u16 v) { return __uint_as_float(((unsigned)v) << 16); }
DI unsigned cvt_pk(float lo, float hi) {
  bf2 r; r[0] = (__bf16)lo; r[1] = (__bf16)hi; return __builtin_bit_cast(unsigned, r);
}
DI float rbf(float x) { return (float)((__bf16)x); }
DI float lo16(unsigned u) { return __uint_as_float(u << 16); }
DI float hi16(unsigned u) { return __uint_as_float(u & 0xffff0000u); }

DI void glds16(const void* gbase, unsigned voff, unsigned lds_dst) {
  unsigned keep;
  asm volatile("s_mov_b32 %0, m0\n\ts_mov_b32 m0, %3\n\ts_nop 0\n\tglobal_load_lds_dwordx4 %1, %2\n\ts_mov_b32 m0, %0"
               : "=&s"(keep) : "v"(voff), "s"(gbase), "s"(lds_dst) : "memory");
}
DI void glds16c(const void* gbase, unsigned voff, unsigned lds_dst) {
  unsigned keep;
  asm volatile("s_mov_b32 %0, m0\n\ts_mov_b32 m0, %3\n\ts_nop 0\n\tglobal_load_lds_dwordx4 %1, %2 sc1\n\ts_mov_b32 m0, %0"
               : "=&s"(keep) : "v"(voff), "s"(gbase), "s"(lds_dst) : "memory");
}
#define GL_PRE "s_mov_b32 %0, m0\n\ts_mov_b32 m0, %2\n\ts_nop 0\n\t"
#define GL_POST "s_mov_b32 m0, %0"
DI void gl2(const void* gb, unsigned lds, unsigned v0, unsigned v1) {
  unsigned keep;
  asm volatile(GL_PRE "global_load_lds_dwordx4 %3, %1\n\tglobal_load_lds_dwordx4 %4, %1 offset:1024\n\t" GL_POST
               : "=&s"(keep) : "s"(gb), "s"(lds), "v"(v0), "v"(v1) : "memory");
}
DI void gl3(const void* gb, unsigned lds, unsigned v0, unsigned v1, unsigned v2) {
  unsigned keep;
  asm volatile(GL_PRE "global_load_lds_dwordx4 %3, %1\n\tglobal_load_lds_dwordx4 %4, %1 offset:1024\n\tglobal_load_lds_dwordx4 %5, %1 offset:2048\n\t" GL_POST
               : "=&s"(keep) : "s"(gb), "s"(lds), "v"(v0), "v"(v1), "v"(v2) : "memory");
}
DI void gl4(const void* gb, unsigned lds, unsigned v0, unsigned v1, unsigned v2, unsigned v3) {
  unsigned keep;
  asm volatile(GL_PRE "global_load_lds_dwordx4 %3, %1\n\tglobal_load_lds_dwordx4 %4, %1 offset:1024\n\tglobal_load_lds_dwordx4 %5, %1 offset:2048\n\tglobal_load_lds_dwordx4 %6, %1 offset:3072\n\t" GL_POST
               : "=&s"(keep) : "s"(gb), "s"(lds), "v"(v0), "v"(v1), "v"(v2), "v"(v3) : "memory");
}
DI void gl3c(const void* gb, unsigned lds, unsigned v0) {
  unsigned keep;
  asm volatile(GL_PRE "global_load_lds_dwordx4 %3, %1 sc1\n\tglobal_load_lds_dwordx4 %3, %1 offset:1024 sc1\n\tglobal_load_lds_dwordx4 %3, %1 offset:2048 sc1\n\t" GL_POST
               : "=&s"(keep) : "s"(gb), "s"(lds), "v"(v0) : "memory");
}
DI void gl4c(const void* gb, unsigned lds, unsigned v0) {
  unsigned keep;
  asm volatile(GL_PRE "global_load_lds_dwordx4 %3, %1 sc1\n\tglobal_load_lds_dwordx4 %3, %1 offset:1024 sc1\n\tglobal_load_lds_dwordx4 %3, %1 offset:2048 sc1\n\tglobal_load_lds_dwordx4 %3, %1 offset:3072 sc1\n\t" GL_POST
               : "=&s"(keep) : "s"(gb), "s"(lds), "v"(v0) : "memory");
}
template <int N> DI void wait_vm() { asm volatile("s_waitcnt vmcnt(%0)" :: "n"(N) : "memory"); }

DI bf16x8 lds_b128(unsigned addr) { return *(LAS bf16x8*)(uintptr_t)addr; }
DI bf16x8 lds_2b64(unsigned addr0, unsigned addr1) {
  bf16x4 a = *(LAS bf16x4*)(uintptr_t)addr0, b = *(LAS bf16x4*)(uintptr_t)addr1;
  return __builtin_shufflevector(a, b, 0, 1, 2, 3, 4, 5, 6, 7);
}
DI bf16x8 lds_tr(unsigned addr0, unsigned addr1) {
  bf16x4 a = __builtin_amdgcn_ds_read_tr16_b64_v4i16((LAS bf16x4*)(uintptr_t)addr0);
  bf16x4 b = __builtin_amdgcn_ds_read_tr16_b64_v4i16((LAS bf16x4*)(uintptr_t)addr1);
  return __builtin_shufflevector(a, b, 0, 1, 2, 3, 4, 5, 6, 7);
}
DI float ldsf(unsigned a) { return *(LAS float*)(uintptr_t)a; }
DI void stsf(unsigned a, float v) { *(LAS float*)(uintptr_t)a = v; }
DI f32x4 ldsf4(unsigned a) { return *(LAS f32x4*)(uintptr_t)a; }
DI void stsf4(unsigned a, f32x4 v) { *(LAS f32x4*)(uintptr_t)a = v; }
#define SB0 __builtin_amdgcn_sched_barrier(0)
#define MFMA(a, b, c) __builtin_amdgcn_mfma_f32_16x16x32_bf16((a), (b), (c), 0, 0, 0)

template <int CTRL> DI float dpp(float v) {
  return __builtin_bit_cast(float, __builtin_amdgcn_update_dpp(0, __builtin_bit_cast(int, v), CTRL, 0xf, 0xf, true));
}
DI float sum8(float v) { v += dpp<0xB1>(v); v += dpp<0x4E>(v); v += dpp<0x141>(v); return v; }
DI float sum16(float v) { v = sum8(v); v += dpp<0x140>(v); return v; }

DI float gelu_t(float x) {
  float u = x * (0.7978845608f + 0.0356774081f * x * x);
  float e = __builtin_amdgcn_exp2f(-2.885390082f * u);
  return x * __builtin_amdgcn_rcpf(1.0f + e);
}
DI float phi_f(float t) {
  return (fmaxf(t, 0.f) + 1e-6f) + __builtin_amdgcn_exp2f(fminf(t, 0.f) * 1.4426950409f);
}

typedef __amdgpu_buffer_rsrc_t rsrc_t;
DI void st16(rsrc_t r, unsigned off, u32x4 v) { __builtin_amdgcn_raw_buffer_store_b128(v, r, off, 0, 16); }
DI u32x4 ld16(rsrc_t r, unsigned off) { return __builtin_amdgcn_raw_buffer_load_b128(r, off, 0, 16); }
DI void st8(char* ws, unsigned off, u32x2 v) { __hip_atomic_store((GAS u64*)(ws + off), ((u64)v[1] << 32) | v[0], RLX_AGENT); }
DI void st8u(char* ws, unsigned off, u64 v) { __hip_atomic_store((GAS u64*)(ws + off), v, RLX_AGENT); }
DI void st4(char* ws, unsigned off, float v) { __hip_atomic_store((GAS unsigned*)(ws + off), __float_as_uint(v), RLX_AGENT); }
DI u64 ld8(const char* ws, unsigned off) { return __hip_atomic_load((GAS u64*)(ws + off), RLX_AGENT); }
constexpr unsigned SPIN_CAP = 1u << 20;
DI void wait_words(const char* ws, unsigned off, bool act, u64 tag) {
#pragma unroll 1
  for (unsigned sp = 0; sp < SPIN_CAP; ++sp) {
    u64 v = act ? ld8(ws, off) : tag;
    if (__all(v == tag)) break;
    __builtin_amdgcn_s_sleep(1);
  }
  __builtin_amdgcn_fence(__ATOMIC_ACQUIRE, "wavefront");
}

struct Params { const u16 *x, *wqkv, *wo, *win, *wsp, *bsp, *wout; u16* out; char* ws; };

constexpr unsigned KBS = 4224;
constexpr unsigned K1_STAGE = 16384 + 8 * KBS;
constexpr unsigned L_MISC = 153600;
constexpr int LDS_TOTAL = 163840;
constexpr unsigned P2_SLOT = 20480;
constexpr unsigned L_KVT = 135168;
constexpr unsigned P3_SLOT = 36864;

DI void bar() { asm volatile("s_waitcnt lgkmcnt(0)" ::: "memory"); __builtin_amdgcn_s_barrier(); asm volatile("" ::: "memory"); }

struct Side { u32x4 r0, r1; unsigned wv[8]; };
DI void side_issue(const Params& P, Side& S, int row, int w) {
  const int l = threadIdx.x & 63;
  const u32x4* src = (const u32x4*)(P.x + (size_t)row * D_);
  S.r0 = src[l]; S.r1 = src[64 + (l & 15)];
  int item = blockIdx.x; if (item > 219) item = 219;
  const int st = item / 5, T = (item - st * 5) * 8 + w;
  const u16* wsrc = (st < 20 ? P.wo + (size_t)st * 32 * D_ : P.wout + (size_t)(st - 20) * 32 * D_) + (size_t)(l >> 4) * 4 * D_ + T * 16 + (l & 15);
#pragma unroll
  for (int e = 0; e < 8; ++e) S.wv[e] = wsrc[(size_t)(16 * (e >> 2) + (e & 3)) * D_];
}
DI float rdl(float v, int lane) { return __builtin_bit_cast(float, __builtin_amdgcn_readlane(__builtin_bit_cast(int, v), lane)); }
DI void side_finish(const Params& P, rsrc_t rws, const Side& S, int row, int w, unsigned tl, unsigned th) {
  const int l = threadIdx.x & 63;
  float s = 0.f, q = 0.f, s2 = 0.f, q2 = 0.f;
#pragma unroll
  for (int j = 0; j < 4; ++j) {
    float a = lo16(S.r0[j]), b = hi16(S.r0[j]), c = lo16(S.r1[j]), d = hi16(S.r1[j]);
    s += a + b; q += a * a + b * b; s2 += c + d; q2 += c * c + d * d;
  }
  s = sum16(s); q = sum16(q); s2 = sum16(s2); q2 = sum16(q2);
  s = (rdl(s, 0) + rdl(s, 16)) + (rdl(s, 32) + rdl(s, 48)) + s2;
  q = (rdl(q, 0) + rdl(q, 16)) + (rdl(q, 32) + rdl(q, 48)) + q2;
  const float m = s * (1.f / D_);
  const float v = rbf(fmaxf(q * (1.f / D_) - m * m, 0.f));
  const float r = 1.f / rbf(sqrtf(rbf(v + 1e-5f)));
  if (l < 2) st8u(P.ws, WS_GX + row * 16 + l * 8, l == 0 ? (((u64)tl << 32) | __float_as_uint(-rbf(m) * r)) : (((u64)th << 32) | __float_as_uint(r)));
  int item = blockIdx.x; if (item > 219) item = 219;
  const int st = item / 5, T = (item - st * 5) * 8 + w, cb = T / 5, nt = T - cb * 5;
  st16(rws, WS_WF + (unsigned)(((cb * 44 + st) * 5 + nt) * 1024) + l * 16,
       (u32x4){S.wv[0] | (S.wv[1] << 16), S.wv[2] | (S.wv[3] << 16), S.wv[4] | (S.wv[5] << 16), S.wv[6] | (S.wv[7] << 16)});
}
DI void xstat_gather(const char* ws, int panel, unsigned L_ST, int tid, unsigned tg, u64 gv) {
  if (tid < 256) {
#pragma unroll 1
    for (unsigned sp = 0; sp < SPIN_CAP; ++sp) {
      if (__all((unsigned)(gv >> 32) == tg)) break;
      __builtin_amdgcn_s_sleep(1);
      gv = ld8(ws, WS_GX + panel * 2048 + tid * 8);
    }
    stsf(L_ST + (tid & 1) * 512 + (tid >> 1) * 4, __uint_as_float((unsigned)gv));
  }
}

DI void csum_issue(const char* ws, int blk, int t, u64 (&cv)[10]) {
#pragma unroll
  for (int k = 0; k < 10; ++k) cv[k] = ld8(ws, WS_GC + (blk * 10 + k) * 2048 + t * 8);
}
DI void csum_gather(const char* ws, int blk, int t, unsigned tl, u64 (&cv)[10], unsigned dst) {
#pragma unroll 1
  for (unsigned sp = 0; sp < SPIN_CAP; ++sp) {
    bool ok = true;
#pragma unroll
    for (int k = 0; k < 10; ++k) ok &= (unsigned)(cv[k] >> 32) == tl;
    if (__all(ok)) break;
    __builtin_amdgcn_s_sleep(1);
    csum_issue(ws, blk, t, cv);
  }
  float a = 0.f;
#pragma unroll
  for (int k = 0; k < 10; ++k) a += __uint_as_float((unsigned)cv[k]);
  stsf(dst + t * 4, a);
}
DI void csum_store(char* ws, unsigned off, f32x4 d, int fr, int g, unsigned tl) {
  const float v = fr == 0 ? d[0] : fr == 1 ? d[1] : fr == 2 ? d[2] : d[3];
  if (fr < 4) st8u(ws, off + (4 * g + fr) * 8, ((u64)tl << 32) | __float_as_uint(v));
}

DI void p1q_consumer(const Params& P, rsrc_t rws, int w, int panel, int cbi, unsigned lds0, u64 tag) {
  char* ws = P.ws;
  const int tid = threadIdx.x, l = tid & 63;
  const int wm = w >> 2, wc = (w + 2 * wm) & 3;
  const int g = l >> 4, fr = l & 15, q = (l & 15) >> 2, p = l & 3, g1 = g & 1;
  const unsigned aoff0 = (unsigned)((wm * 64 + fr) * 128 + ((g ^ (l & 7)) << 4));
  const unsigned ttab = (unsigned)(0x4498E765DC32BA10ull >> (16 * wc)) & 0xffffu;
  int tile[4];
  unsigned bo[4];
#pragma unroll
  for (int n = 0; n < 4; ++n) {
    tile[n] = (ttab >> (4 * n)) & 15;
    bo[n] = 16384u + g * KBS + q * 480 + p * 8 + tile[n] * 32;
  }
  const unsigned tl = (unsigned)tag, th = tl * 0x9E3779B1u ^ (unsigned)(tag >> 32);
  {
    Side S; side_issue(P, S, panel * 128 + cbi * 8 + w, w);
    side_finish(P, rws, S, panel * 128 + cbi * 8 + w, w, tl, th);
  }
  f32x4 acc[4][4];
#pragma unroll
  for (int m = 0; m < 4; ++m)
#pragma unroll
    for (int n = 0; n < 4; ++n) acc[m][n] = (f32x4){0.f, 0.f, 0.f, 0.f};
  const short one = 0x3F80;
  const bf16x8 ones = {one, one, one, one, one, one, one, one};
  const bf16x8 zeros = {0, 0, 0, 0, 0, 0, 0, 0};

  bf16x8 af[2][4], bfr[2][4];
#pragma unroll
  for (int m = 0; m < 4; ++m) { af[1][m] = zeros; bfr[1][m] = zeros; }
  auto mm = [&](int ks) {
#pragma unroll
    for (int n = 0; n < 3; ++n)
#pragma unroll
      for (int m = 0; m < 4; ++m) acc[m][n] = MFMA(bfr[ks][n], af[ks][m], acc[m][n]);
    if (wc < 3) {
#pragma unroll
      for (int m = 0; m < 4; ++m) acc[m][3] = MFMA(bfr[ks][3], af[ks][m], acc[m][3]);
    }
  };
  auto step = [&](unsigned sb, int ks) {
#pragma unroll
    for (int n = 0; n < 4; ++n) bfr[ks][n] = lds_tr(sb + ks * 4 * KBS + bo[n], sb + ks * 4 * KBS + bo[n] + 1920);
#pragma unroll
    for (int m = 0; m < 4; ++m) af[ks][m] = lds_b128(sb + ((aoff0 ^ (ks * 64)) + m * 2048));
    SB0;
    mm(ks ^ 1);
    SB0;
  };
  unsigned sb = lds0;
  auto run = [&](int n) {
#pragma unroll 1
    for (int kt = 0; kt < n; ++kt) {
      bar();
      step(sb, 0);
      step(sb, 1);
      sb += K1_STAGE; if (sb == lds0 + 3 * K1_STAGE) sb = lds0;
    }
  };
  run(1);
  if (panel < 10) {
#pragma unroll
    for (int h = 0; h < 2; ++h) {
      const bf16x8 b0 = wm == 0 ? bfr[0][h] : bfr[0][2 + h], b1 = wm == 0 ? bfr[1][h] : bfr[1][2 + h];
      const int tt = wm == 0 ? tile[h] : tile[2 + h];
      f32x4 d = MFMA(b0, ones, ((f32x4){0.f, 0.f, 0.f, 0.f}));
      d = MFMA(b1, ones, d);
      csum_store(ws, WS_GC + (cbi * 10 + panel) * 2048 + tt * 128, d, fr, g, tl);
    }
  }
  run(9);
  mm(1);
  STAMP(1);
  bar();
  STAMP(10);
  const unsigned L_ST = lds0 + L_MISC, L_CS = lds0 + L_MISC + 5120;
  const unsigned L_K = lds0, L_V = L_K + 20480, L_Q = lds0 + 122880;
  float mr[4], rinv[4];
#pragma unroll
  for (int m = 0; m < 4; ++m) { const int rl = wm * 64 + m * 16 + fr; mr[m] = ldsf(L_ST + rl * 4); rinv[m] = ldsf(L_ST + 512 + rl * 4); }
#pragma unroll
  for (int n = 0; n < 4; ++n) {
    if (n < 3 || wc < 3) {
      const int t = tile[n];
      const f32x4 cs = ldsf4(L_CS + (t * 16 + 4 * g) * 4);
      unsigned dst, dstep;
      if (t < 5) { dst = L_Q + (wm * 12 + (t >> 1)) * 1024 + l * 16 + (t & 1) * 8; dstep = 3072; }
      else {
        const int kb = wm * 8 + (fr >> 3), pos = (fr & 7) ^ ((fr >> 3) << 2);
        dst = (t < 10 ? L_K + (t - 5) * 256 : L_V + (t - 10) * 256) + kb * 1280 + pos * 32 + g * 8; dstep = 2560;
      }
      if (t < 10) {
#pragma unroll
        for (int m = 0; m < 4; ++m) {
          f32x4 v = acc[m][n] * rinv[m] + cs * mr[m];
#pragma unroll
          for (int j = 0; j < 4; ++j) v[j] = phi_f(v[j]);
          *(LAS u32x2*)(uintptr_t)(dst + m * dstep) = (u32x2){cvt_pk(v[0], v[1]), cvt_pk(v[2], v[3])};
        }
      } else {
#pragma unroll
        for (int m = 0; m < 4; ++m) {
          f32x4 v = acc[m][n] * rinv[m] + cs * mr[m];
          *(LAS u32x2*)(uintptr_t)(dst + m * dstep) = (u32x2){cvt_pk(v[0], v[1]), cvt_pk(v[2], v[3])};
        }
      }
    }
  }
  STAMP(8);
  bar();
  {
    const unsigned troff = (unsigned)(g * 5 * 256 + ((4 * g1 + q) * 32) + p * 8);
#pragma unroll 1
    for (int job = w; job < 18; job += 8) {
      int i, j;
      if (job < 13) { i = (job + 1) / 3; j = job + 1 - 3 * i + i - 1; }
      else { i = job - 13; j = -1; }
      f32x4 d = {0.f, 0.f, 0.f, 0.f};
#pragma unroll
      for (int ks = 0; ks < 4; ++ks) {
        unsigned a0 = L_K + troff + (ks * 20 + i) * 256;
        bf16x8 ka = lds_tr(a0, a0 ^ 128);
        bf16x8 vb = ones;
        if (j >= 0) { unsigned b0 = L_V + troff + (ks * 20 + j) * 256; vb = lds_tr(b0, b0 ^ 128); }
        d = MFMA(ka, vb, d);
      }
      st16(rws, WS_KVP + (unsigned)(((cbi * 18 + job) * 16 + panel) * 1024) + l * 16, __builtin_bit_cast(u32x4, d));
    }
    const unsigned pqb = WS_PQ + (unsigned)(((panel * 8 + cbi) * 8 + w) * 3) * 1024 + l * 16;
    const u32x4 q0 = *(LAS u32x4*)(uintptr_t)(L_Q + w * 3072 + l * 16), q1 = *(LAS u32x4*)(uintptr_t)(L_Q + w * 3072 + 1024 + l * 16);
    const u32x2 q2 = *(LAS u32x2*)(uintptr_t)(L_Q + w * 3072 + 2048 + l * 16);
    st16(rws, pqb, q0); st16(rws, pqb + 1024, q1); st16(rws, pqb + 2048, (u32x4){q2[0], q2[1], 0u, 0u});
  }
  STAMP(9);
  wait_vm<0>();
  bar();
  if (tid == 0) st8u(ws, WS_F1 + (panel * 24 + cbi) * 64, tag);
}

DI void p1z_consumer(const Params& P, rsrc_t rws, int w, int panel, int cbj, unsigned lds0, u64 tag) {
  char* ws = P.ws;
  const int tid = threadIdx.x, l = tid & 63;
  const int wm = w & 3, wn = w >> 2;
  const int g = l >> 4, fr = l & 15, q = (l & 15) >> 2, p = l & 3, g1 = g & 1;
  const unsigned aoff0 = (unsigned)((wm * 32 + fr) * 128 + ((g ^ (l & 7)) << 4));
  unsigned bo[3];
  int ca[3];
#pragma unroll
  for (int n = 0; n < 3; ++n) {
    const int c = wn * 3 + n;
    ca[n] = c;
    int pc = c + g1; if (pc == 6) pc = 0;
    bo[n] = 16384u + g * 1536 + q * 192 + pc * 32 + p * 8;
  }
  const short one = 0x3F80;
  const bf16x8 ones = {one, one, one, one, one, one, one, one};
  const bf16x8 zeros = {0, 0, 0, 0, 0, 0, 0, 0};
  f32x4 accA[2][3], accB[2][3];
#pragma unroll
  for (int m = 0; m < 2; ++m)
#pragma unroll
    for (int n = 0; n < 3; ++n) { accA[m][n] = (f32x4){0.f, 0.f, 0.f, 0.f}; accB[m][n] = (f32x4){0.f, 0.f, 0.f, 0.f}; }
  const unsigned tl = (unsigned)tag, th = tl * 0x9E3779B1u ^ (unsigned)(tag >> 32), tg = (tid & 1) ? th : tl;
  bf16x8 af[2][2], bfr[2][3];
  af[1][0] = af[1][1] = zeros; bfr[1][0] = bfr[1][1] = bfr[1][2] = zeros;
  auto rd = [&](unsigned sb, int ks) {
#pragma unroll
    for (int m = 0; m < 2; ++m) af[ks][m] = lds_b128(sb + ((aoff0 ^ (ks * 64)) + m * 2048));
#pragma unroll
    for (int n = 0; n < 3; ++n) { const unsigned o = sb + ks * 4 * 1536 + bo[n]; bfr[ks][n] = lds_tr(o, o + 768); }
    SB0;
  };
  auto mmA = [&](int ks) {
#pragma unroll
    for (int n = 0; n < 3; ++n)
#pragma unroll
      for (int m = 0; m < 2; ++m) accA[m][n] = MFMA(af[ks][m], bfr[ks][n], accA[m][n]);
    SB0;
  };
  auto mmB = [&](int ks) {
#pragma unroll
    for (int n = 0; n < 3; ++n)
#pragma unroll
      for (int m = 0; m < 2; ++m) accB[m][n] = MFMA(bfr[ks][n], af[ks][m], accB[m][n]);
    SB0;
  };
  const unsigned L_ST = lds0 + L_MISC, L_CS = lds0 + L_MISC + 5120;
  const unsigned L_RS = lds0 + L_MISC + 2048;
  const unsigned L_FS = lds0 + L_MISC + 4096;
  unsigned sb = lds0;
  auto nxt = [&]() { sb += K1_STAGE; if (sb == lds0 + 3 * K1_STAGE) sb = lds0; };
  auto runA = [&](int n) {
#pragma unroll 1
    for (int kt = 0; kt < n; ++kt) { bar(); rd(sb, 0); mmA(1); rd(sb, 1); mmA(0); nxt(); }
  };
  auto runB = [&](int n) {
#pragma unroll 1
    for (int kt = 0; kt < n; ++kt) { bar(); rd(sb, 0); mmB(1); rd(sb, 1); mmB(0); nxt(); }
  };
  auto cpub = [&](int blk) {
    if (panel < 10 && wm == 0) {
#pragma unroll
      for (int n = 0; n < 3; ++n) {
        f32x4 d = MFMA(bfr[0][n], ones, ((f32x4){0.f, 0.f, 0.f, 0.f}));
        d = MFMA(bfr[1][n], ones, d);
        csum_store(ws, WS_GC + (blk * 10 + panel) * 2048 + ca[n] * 128, d, fr, g, tl);
      }
    }
  };
  {
    Side S; side_issue(P, S, panel * 128 + 64 + cbj * 8 + w, w);
    side_finish(P, rws, S, panel * 128 + 64 + cbj * 8 + w, w, tl, th);
  }
  runA(1);
  cpub(8 + cbj);
  runA(6);
  const int tc = tid - 256;
  {
    u64 cv[10];
    if (tid < 256) cv[0] = ld8(ws, WS_GX + panel * 2048 + tid * 8);
    else if (tc < 96) csum_issue(ws, 8 + cbj, tc, cv);
    runA(3);
    if (tid < 256) xstat_gather(ws, panel, L_ST, tid, tg, cv[0]);
    else if (tc < 96) csum_gather(ws, 8 + cbj, tc, tl, cv, L_CS);
  }
  bar(); rd(sb, 0); mmA(1); rd(sb, 1);
  cpub(16 + cbj);
  STAMP(8);
  {
    float cn[3];
#pragma unroll
    for (int n = 0; n < 3; ++n) cn[n] = ldsf(L_CS + (ca[n] * 16 + fr) * 4);
#pragma unroll
    for (int m = 0; m < 2; ++m) {
      const int rl = wm * 32 + m * 16 + 4 * g;
      const f32x4 mr = ldsf4(L_ST + rl * 4), rinv = ldsf4(L_ST + 512 + rl * 4);
      f32x4 rs = {0.f, 0.f, 0.f, 0.f}, rq = {0.f, 0.f, 0.f, 0.f};
#pragma unroll
      for (int n = 0; n < 3; ++n) {
        f32x4 v = accA[m][n] * rinv + mr * cn[n];
#pragma unroll
        for (int j = 0; j < 4; ++j) v[j] = gelu_t(v[j]);
        rs += v; rq += v * v;
        accA[m][n] = v;
      }
#pragma unroll
      for (int j = 0; j < 4; ++j) { rs[j] = sum16(rs[j]); rq[j] = sum16(rq[j]); }
      if (fr == 0) {
        stsf4(L_RS + ((wn * 128 + rl) * 2) * 4, (f32x4){rs[0], rq[0], rs[1], rq[1]});
        stsf4(L_RS + ((wn * 128 + rl) * 2 + 4) * 4, (f32x4){rs[2], rq[2], rs[3], rq[3]});
      }
    }
  }
  mmB(0); nxt();
  bar(); rd(sb, 0);
  float part = 0.f;
  const unsigned gbase = WS_GR + (unsigned)(panel * 8) * 2048 + tid * 8;
  if (tid < 256) {
    part = ldsf(L_RS + tid * 4) + ldsf(L_RS + 1024 + tid * 4);
    st8u(ws, gbase + cbj * 2048, ((u64)tg << 32) | __float_as_uint(part));
  }
  mmB(1); rd(sb, 1); mmB(0); nxt();
  STAMP(9);
  runB(1);
  {
    u64 cv[10];
    if (tid < 256) {
#pragma unroll
      for (int i = 1; i < 8; ++i) cv[i] = ld8(ws, gbase + ((cbj + i) & 7) * 2048);
    }
    runB(1);
    if (tid < 256) {
      float tot;
#pragma unroll 1
      for (unsigned sp = 0; sp < SPIN_CAP; ++sp) {
        bool ok = true; tot = part;
#pragma unroll
        for (int i = 1; i < 8; ++i) { ok &= (unsigned)(cv[i] >> 32) == tg; tot += __uint_as_float((unsigned)cv[i]); }
        if (__all(ok)) break;
#pragma unroll
        for (int i = 1; i < 8; ++i) cv[i] = ld8(ws, gbase + ((cbj + i) & 7) * 2048);
      }
      float other = dpp<0xB1>(tot);
      if ((tid & 1) == 0) {
        float m = tot * (1.f / FH);
        float v = rbf(fmaxf(other * (1.f / FH) - m * m, 0.f));
        float r = 1.f / rbf(sqrtf(rbf(v + 1e-5f)));
        stsf(L_FS + (tid >> 1) * 4, -rbf(m) * r);
        stsf(L_FS + 512 + (tid >> 1) * 4, r);
      }
    }
  }
  bar(); rd(sb, 0);
  {
    f32x4 mr[2], ri[2];
#pragma unroll
    for (int m = 0; m < 2; ++m) { const int rl = wm * 32 + m * 16 + 4 * g; mr[m] = ldsf4(L_FS + rl * 4); ri[m] = ldsf4(L_FS + 512 + rl * 4); }
#pragma unroll
    for (int n = 0; n < 3; ++n) {
      f32x4 a = accA[0][n] * ri[0] + mr[0], b = accA[1][n] * ri[1] + mr[1];
      st16(rws, WS_Z2N + (unsigned)(((cbj * 64 + panel * 4 + wm) * 6 + ca[n]) * 1024) + l * 16,
           (u32x4){cvt_pk(a[0], a[1]), cvt_pk(a[2], a[3]), cvt_pk(b[0], b[1]), cvt_pk(b[2], b[3])});
    }
  }
  mmB(1); rd(sb, 1); mmB(0); nxt();
  STAMP(10);
  runB(2);
  wait_vm<0>();
  bar();
  if (tid == 0) st8u(ws, WS_F1 + (panel * 24 + 8 + cbj) * 64, tag);
  STAMP(11);
  rd(sb, 0); mmB(1); rd(sb, 1); mmB(0); nxt();
  {
    u64 cv[10];
    if (tid >= 256 && tc < 96) csum_issue(ws, 16 + cbj, tc, cv);
    runB(2);
    if (tid >= 256 && tc < 96) csum_gather(ws, 16 + cbj, tc, tl, cv, L_CS + 512);
  }
  mmB(1);
  STAMP(1);
  bar();
  {
    f32x4 cn[3];
#pragma unroll
    for (int n = 0; n < 3; ++n) { const unsigned a = L_CS + 512 + (ca[n] * 16 + 4 * g) * 4; cn[n] = ldsf4(a); }
    float mr[2], rinv[2];
#pragma unroll
    for (int m = 0; m < 2; ++m) { const int rl = wm * 32 + m * 16 + fr; mr[m] = ldsf(L_ST + rl * 4); rinv[m] = ldsf(L_ST + 512 + rl * 4); }
#pragma unroll
    for (int n = 0; n < 3; ++n) {
      u32x4 o;
#pragma unroll
      for (int m = 0; m < 2; ++m) {
        f32x4 v = accB[m][n] * rinv[m] + cn[n] * mr[m];
#pragma unroll
        for (int j = 0; j < 4; ++j) v[j] = gelu_t(v[j]);
        o[2 * m] = cvt_pk(v[0], v[1]); o[2 * m + 1] = cvt_pk(v[2], v[3]);
      }
      st16(rws, WS_Z1 + (unsigned)(((panel * 8 + cbj) * 4 + wm) * 6 + ca[n]) * 1024 + l * 16, o);
    }
  }
  wait_vm<0>();
  bar();
  if (tid == 0) st8u(ws, WS_F1 + (panel * 24 + 16 + cbj) * 64, tag);
}

__global__ __launch_bounds__(768, 3) void mega_kernel(Params P) {
  extern __shared__ __attribute__((aligned(16))) char smem[];
  const unsigned lds0 = (unsigned)(uintptr_t)(LAS char*)smem;
  char* ws = P.ws;
  const int tid = threadIdx.x, l = tid & 63;
  const int w = __builtin_amdgcn_readfirstlane(tid >> 6);
  const int g = l >> 4, fr = l & 15;
  const int b = blockIdx.x, xcd = b & 7, bi = b >> 3;
  const int panel1 = (xcd & 1) * 8 + (bi & 7), ci = bi >> 3, cb1 = (xcd >> 1) * 2 + (ci & 1);
  const bool isq = ci < 2;
  const int panel2 = (xcd >> 1) * 8 + (bi & 7), cb2 = (xcd & 1) * 4 + (bi >> 3);
  const int panel3 = xcd * 4 + (bi & 3), cb3 = bi >> 2;
  STAMP(0);
  const u64 tag = ld8(ws, WS_EP) + 1;
  if (w == 7 && bi < 20) {
    u64 pc; asm volatile("s_getpc_b64 %0" : "=s"(pc));
    u32x4 d0, d1;
    asm volatile("global_load_dwordx4 %0, %2, %3\n\tglobal_load_dwordx4 %1, %2, %3 offset:1024\n\ts_waitcnt vmcnt(0)"
                 : "=&v"(d0), "=&v"(d1) : "v"(bi * 2048 + l * 16), "s"(pc & ~127ull) : "memory");
  }

  if (w >= 8) {
    const int lw = w - 8;
    const unsigned vA0 = (unsigned)(((l >> 3) * D_ + (((l & 7) ^ (l >> 3)) << 3)) * 2);
    const unsigned vA1 = vA0 + 8 * D_ * 2 - 1024, vA2 = vA0 + 16 * D_ * 2 - 2048, vA3 = vA0 + 24 * D_ * 2 - 3072;
    const char* gA = (const char*)(P.x + (size_t)(panel1 * 128 + lw * 32) * D_);
    const unsigned v20 = (unsigned)(((l >> 3) * S_ + (((l & 7) ^ (l >> 4)) << 3)) * 2);
    const unsigned v21 = (unsigned)((((l >> 3) + 8) * S_ + (((l & 7) ^ ((l >> 4) | 4)) << 3)) * 2) - 1024;
    const char* gA2 = (const char*)(P.wsp + (size_t)(panel2 * 64 + lw * 16) * S_);
    const int rot2 = 2 * ((2 * (bi & 7) + 4 * (bi >> 3)) & 15);
    auto ldA = [&](int kt, unsigned sl) { gl2(gA2 + ((kt + rot2) & 31) * 128, sl + lw * 2048, v20, v21); };
    auto sl2 = [&](int kt) { return lds0 + ((kt + 4) % 6) * P2_SLOT; };
    const unsigned tl = (unsigned)tag, th = tl * 0x9E3779B1u ^ (unsigned)(tag >> 32);
    const int lt = tid - 512;
    auto gather = [&](int blk, int ncol, unsigned dst, bool xs) {
      u64 cv[10]; u64 gv = 0;
      const int tc = lt < ncol ? lt : 0;
      if (xs) gv = ld8(ws, WS_GX + panel1 * 2048 + lt * 8);
      csum_issue(ws, blk, tc, cv);
      if (xs) xstat_gather(ws, panel1, lds0 + L_MISC, lt, (lt & 1) ? th : tl, gv);
      csum_gather(ws, blk, tc, tl, cv, dst);
    };
    const unsigned l16 = l * 16;
    const char* gB2 = ws + WS_Z2N + (size_t)cb2 * 64 * 6144 + lw * 3072;
    auto ldB = [&](int kt, unsigned sl) { gl3c(gB2 + ((kt + rot2) & 31) * 12288, sl + 8192 + lw * 3072, l16); };
    const unsigned zfl = WS_F1 + ((l & 15) * 24 + 8 + cb2) * 64;
    auto try_z = [&]() {
      const u64 v = l < 16 ? ld8(ws, zfl) : tag;
      return (bool)__builtin_amdgcn_readfirstlane((int)__all(v == tag));
    };
    bool early = false;
    const int rot = panel1 < 10 ? panel1 : 0;
    auto krot = [&](int kt) { const int k = kt >= 10 ? kt - 10 : kt; return __builtin_amdgcn_readfirstlane(k == 0 ? rot : (k - 1 < rot ? k - 1 : k)); };
    if (isq) {
      gl4(gA + rot * 128, lds0 + lw * 4096, vA0, vA1, vA2, vA3);
      unsigned vB[4];
#pragma unroll
      for (int i = 0; i < 4; ++i) {
        int c = i * 64 + l; if (c > 239) c = 239;
        const int row = c / 30, cc = c - row * 30, seg = cc / 10, ch = cc - seg * 10;
        vB[i] = (unsigned)((row * NQKV + seg * 640 + cb1 * 80 + ch * 8) * 2) + 4096 - i * 1024;
      }
      const char* gB = (const char*)P.wqkv + (size_t)lw * 16 * NQKV * 2 - 4096;
      auto stageB = [&](int kt, unsigned sb) {
        const char* bp = gB + (size_t)krot(kt) * 64 * NQKV * 2;
        gl4(bp, sb + 16384 + lw * 2 * KBS, vB[0], vB[1], vB[2], vB[3]);
        gl4(bp + 8 * NQKV * 2, sb + 16384 + (lw * 2 + 1) * KBS, vB[0], vB[1], vB[2], vB[3]);
      };
      auto stage = [&](int kt, unsigned sb) { gl4(gA + krot(kt) * 128, sb + lw * 4096, vA0, vA1, vA2, vA3); stageB(kt, sb); };
      stageB(0, lds0); stage(1, lds0 + K1_STAGE);
      wait_vm<12>(); bar();
      stage(2, lds0 + 2 * K1_STAGE);
      unsigned sb = lds0, sc = lds0 + K1_STAGE;
#pragma unroll 1
      for (int kt = 1; kt < 8; ++kt) {
        wait_vm<12>(); bar();
        if (kt == 2) STAMP(10);
        if (kt == 7) STAMP(11);
        stage(kt + 2, sb);
        sb = sc; sc += K1_STAGE; if (sc == lds0 + 3 * K1_STAGE) sc = lds0;
      }
      wait_vm<12>(); bar();
      ldA(5, sl2(5)); ldA(0, sl2(0));
      wait_vm<4>(); bar();
      ldA(1, sl2(1));
      gather(cb1, 240, lds0 + L_MISC + 5120, true);
      bar();
      ldA(4, sl2(4));
#pragma unroll 1
      for (int t = 0; t < 3 && !early; ++t) early = try_z();
      if (early) { ldB(0, sl2(0)); ldB(1, sl2(1)); ldB(4, sl2(4)); ldB(5, sl2(5)); }
      bar();
      if (!early) { early = try_z(); if (early) { ldB(0, sl2(0)); ldB(1, sl2(1)); ldB(4, sl2(4)); ldB(5, sl2(5)); } }
      bar();
    } else {
      gl4(gA + rot * 128, lds0 + lw * 4096, vA0, vA1, vA2, vA3);
      unsigned vB[3];
#pragma unroll
      for (int i = 0; i < 3; ++i) {
        const int c = i * 64 + l, row = c / 12, cc = c - row * 12;
        int pc = (cc >> 1) - (row >> 3); if (pc < 0) pc = 5;
        vB[i] = (unsigned)((row * FIN + cb1 * 96 + pc * 16 + (cc & 1) * 8) * 2) + 4096 - i * 1024;
      }
      const char* gB = (const char*)P.win + (size_t)lw * 16 * FIN * 2 - 4096;
      auto stageB = [&](int kt, unsigned sb) {
        const int k = krot(kt);
        gl3(gB + (kt < 10 ? FH * 2 : 0) + (size_t)k * 64 * FIN * 2, sb + 16384 + lw * 3072, vB[0], vB[1], vB[2]);
      };
      auto stage = [&](int kt, unsigned sb) {
        const int k = krot(kt);
        gl4(gA + k * 128, sb + lw * 4096, vA0, vA1, vA2, vA3); stageB(kt, sb);
      };
      stageB(0, lds0); stage(1, lds0 + K1_STAGE);
      wait_vm<7>(); bar();
      stage(2, lds0 + 2 * K1_STAGE);
      unsigned sb = lds0, sc = lds0 + K1_STAGE;
      auto runL = [&](int k0, int k1) {
#pragma unroll 1
        for (int kt = k0; kt < k1; ++kt) {
          wait_vm<7>(); bar();
          stage(kt + 2, sb);
          sb = sc; sc += K1_STAGE; if (sc == lds0 + 3 * K1_STAGE) sc = lds0;
        }
      };
      runL(1, 18);
      ldA(0, sl2(0)); ldA(4, sl2(4));
      wait_vm<11>(); bar();
      ldA(1, sl2(1));
      wait_vm<6>(); bar();
      bar();
      ldA(5, sl2(5));
#pragma unroll 1
      for (int t = 0; t < 2 && !early; ++t) early = try_z();
      if (early) { ldB(0, sl2(0)); ldB(1, sl2(1)); ldB(4, sl2(4)); ldB(5, sl2(5)); }
      bar();
    }
    STAMP(2);
    if (!early) {
      wait_words(ws, zfl, l < 16, tag);
      ldB(0, sl2(0)); ldB(1, sl2(1)); ldB(4, sl2(4)); ldB(5, sl2(5));
    }
    STAMP(3);
    ldA(2, sl2(2)); ldA(3, sl2(3)); ldB(2, sl2(2)); ldB(3, sl2(3));
    wait_vm<16>(); bar();
    {
      unsigned sl = sl2(0);
      auto ldU = [&](int u) {
        ldA(2 * u, sl); ldB(2 * u, sl); ldA(2 * u + 1, sl + P2_SLOT); ldB(2 * u + 1, sl + P2_SLOT);
        sl += 2 * P2_SLOT; if (sl == lds0 + 6 * P2_SLOT) sl = lds0;
      };
      wait_vm<0>(); bar(); ldU(3);
#pragma unroll 1
      for (int u = 2; u < 14; ++u) {
        wait_vm<10>(); bar();
        if (u == 4) STAMP(8);
        if (u == 10) STAMP(9);
        ldU(u + 2);
      }
    }
    wait_vm<10>(); bar();
    wait_words(ws, WS_F1 + (((lw * 64 + l) >> 4) * 24 + ((l & 8) ? 8 : 0) + (l & 15)) * 64, true, tag);
    wait_vm<0>(); bar();
    STAMP(4);
    const char* gA3 = ws + WS_OH + (size_t)panel3 * 44 * 4096 + lw * 4096;
    const char* gB3 = ws + WS_WF + (size_t)cb3 * 44 * 5120 + lw * 5120;
    int rot3 = cb3 + 3 * (bi & 3); if (rot3 >= 11) rot3 -= 11;
    auto ur = [&](int u) { int v = u + rot3; if (v >= 11) v -= 11; return v; };
    auto ldB3 = [&](int u, unsigned sl) {
      gl4c(gB3 + ur(u) * 20480, sl + 16384 + lw * 5120, l16);
      glds16c(gB3 + ur(u) * 20480 + 4096, l16, sl + 16384 + lw * 5120 + 4096);
    };
    auto ldA3 = [&](int u, unsigned sl) { gl4c(gA3 + ur(u) * 16384, sl + lw * 4096, l16); };
    bar();
    ldB3(0, lds0); ldB3(1, lds0 + P3_SLOT); ldB3(2, lds0 + 2 * P3_SLOT);
    bar(); bar();
    ldB3(3, lds0 + 3 * P3_SLOT);
    wait_words(ws, WS_F2 + (panel3 * 8 + (l & 7)) * 64, l < 8, tag);
    STAMP(5);
#pragma unroll
    for (int j = 0; j < 4; ++j) ldA3(j, lds0 + j * P3_SLOT);
    wait_vm<12>(); bar();
    wait_vm<8>(); bar(); ldA3(4, lds0); ldB3(4, lds0);
    wait_vm<13>(); bar(); ldA3(5, lds0 + P3_SLOT); ldB3(5, lds0 + P3_SLOT);
    {
      unsigned sl = lds0 + 2 * P3_SLOT;
#pragma unroll 1
      for (int u = 3; u < 8; ++u) {
        wait_vm<18>(); bar();
        ldA3(u + 3, sl); ldB3(u + 3, sl);
        sl += P3_SLOT; if (sl == lds0 + 4 * P3_SLOT) sl = lds0;
      }
    }
    wait_vm<18>(); bar(); wait_vm<9>(); bar(); wait_vm<0>(); bar();
    bar(); bar();
    if (b == 0 && lw == 0) {
#pragma unroll
      for (int i = 0; i < 4; ++i) wait_words(ws, WS_F2 + (i * 64 + l) * 64, true, tag);
      if (l == 0) st8u(ws, WS_EP, tag);
    }
    STAMP(6);
    return;
  }

  const rsrc_t rws = __builtin_amdgcn_make_buffer_rsrc(ws, 0, (int)WS_END, 0x00020000);
  if (isq) p1q_consumer(P, rws, w, panel1, cb1, lds0, tag);
  else p1z_consumer(P, rws, w, panel1, cb1, lds0, tag);
  STAMP(2);

  {
    const int wt = w & 3, kh = w >> 2;
    const int rsw = ((wt * 16 + fr) >> 1) & 7;
    const unsigned ao0 = (unsigned)((wt * 16 + fr) * 128 + (((4 * kh + (g >> 1)) ^ rsw) << 4) + (g & 1) * 8);
    const unsigned ao1 = (unsigned)((wt * 16 + fr) * 128 + (((4 * kh + 2 + (g >> 1)) ^ rsw) << 4) + (g & 1) * 8);
    const unsigned bo = (unsigned)(8192 + kh * 6144 + l * 16);
    const bf16x8 zeros = {0, 0, 0, 0, 0, 0, 0, 0};
    f32x4 acc[6];
#pragma unroll
    for (int n = 0; n < 6; ++n) acc[n] = (f32x4){0.f, 0.f, 0.f, 0.f};
    bf16x8 af[2], bfr[2][6];
    af[1] = zeros;
#pragma unroll
    for (int n = 0; n < 6; ++n) bfr[1][n] = zeros;
    const int zi = (bi & 15) * 8 + xcd;
    const bool reducer = isq && zi < 72;
    const int rj = zi & 7, rf = zi >> 3;
    const int row = panel2 * 64 + wt * 16 + fr;
    u32x2 zreg[6];
    bf16x8 pf[3];
    const float bs = bf2f(P.bsp[row]);
    unsigned sl = lds0 + 4 * P2_SLOT;
    auto step = [&](int cur, int kt) {
      if (cur == 0) bar();
      af[cur] = lds_2b64(sl + ao0, sl + ao1);
#pragma unroll
      for (int n = 0; n < 6; ++n) bfr[cur][n] = lds_b128(sl + bo + n * 1024);
      SB0;
#pragma unroll
      for (int n = 0; n < 6; ++n) acc[n] = MFMA(bfr[cur ^ 1][n], af[cur ^ 1], acc[n]);
      SB0;
      sl += P2_SLOT; if (sl == lds0 + 6 * P2_SLOT) sl = lds0;
    };
    auto run = [&](int k0, int k1) {
#pragma unroll 1
      for (int kt = k0; kt < k1; kt += 2) { step(0, kt); step(1, kt + 1); }
    };
    if (reducer) {
      const int et = (rf + 1) >> 1, st = rf == 0 ? 0 : ((rf + 1) & 1) + (et >= 3 ? 1 : 0);
      run(0, 2);
      f32x4 pv[8];
      wait_words(ws, WS_F1 + ((l & 15) * 24 + rj) * 64, l < 16, tag);
#pragma unroll
      for (int t4 = 0; t4 < 4; ++t4) {
        const int i = 2 * st + (t4 & 1);
        const bool ok = i < 5 && (t4 >= 2 || (et - i <= 1 && i - et <= 1));
        const int job = t4 >= 2 ? 13 + i : 2 * i + et;
#pragma unroll
        for (int pp = 0; pp < 2; ++pp) {
          pv[t4 * 2 + pp] = (f32x4){0.f, 0.f, 0.f, 0.f};
          if (ok) pv[t4 * 2 + pp] = __builtin_bit_cast(f32x4, ld16(rws, WS_KVP + (unsigned)(((rj * 18 + job) * 16 + w * 2 + pp) * 1024) + l * 16));
        }
      }
      run(2, 6);
      const unsigned L_R = lds0 + L_KVT;
      if (w >= 4) {
#pragma unroll
        for (int t4 = 0; t4 < 4; ++t4) stsf4(L_R + ((w - 4) * 4 + t4) * 1024 + l * 16, pv[t4 * 2] + pv[t4 * 2 + 1]);
      }
      run(6, 8);
      if (w < 4) {
#pragma unroll
        for (int t4 = 0; t4 < 4; ++t4) {
          f32x4 v = pv[t4 * 2] + pv[t4 * 2 + 1] + ldsf4(L_R + (w * 4 + t4) * 1024 + l * 16);
          stsf4(L_R + (w * 4 + t4) * 1024 + l * 16, v);
        }
      }
      run(8, 10);
      if (w < 2) {
        f32x4 v[2];
#pragma unroll
        for (int h2 = 0; h2 < 2; ++h2) {
          v[h2] = (f32x4){0.f, 0.f, 0.f, 0.f};
#pragma unroll
          for (int ww = 0; ww < 4; ++ww) v[h2] += ldsf4(L_R + (ww * 4 + w * 2 + h2) * 1024 + l * 16);
          const int he = (et * 16 + fr) / 20;
#pragma unroll
          for (int r = 0; r < 4; ++r) { const int d = (2 * st + h2) * 16 + 4 * g + r; if (d / 20 != he) v[h2][r] = 0.f; }
        }
        st16(rws, WS_KVT + (unsigned)(((rj * 2 + w) * 9 + rf) * 1024) + l * 16,
             (u32x4){cvt_pk(v[0][0], v[0][1]), cvt_pk(v[0][2], v[0][3]), cvt_pk(v[1][0], v[1][1]), cvt_pk(v[1][2], v[1][3])});
      }
      run(10, 14);
      wait_vm<0>();
      run(14, 16);
      if (tid == 0) st8u(ws, WS_FK + (rj * 16 + rf) * 64, tag);
      run(16, 20);
    } else {
      run(0, 20);
    }
    const unsigned foff = kh == 1 ? (l < 9 ? WS_FK + (cb2 * 16 + l) * 64 : WS_F1 + ((panel2 >> 1) * 24 + cb2) * 64)
                                  : WS_F1 + ((panel2 >> 1) * 24 + 16 + cb2) * 64;
    const u64 fv = l < 10 ? ld8(ws, foff) : tag;
    run(20, 24);
    if (!__all(fv == tag)) wait_words(ws, foff, l < 10, tag);
    if (kh == 1) {
      const unsigned pqo = WS_PQ + (unsigned)((((panel2 >> 1) * 8 + cb2) * 8 + (panel2 & 1) * 4 + wt) * 3) * 1024 + l * 16;
#pragma unroll
      for (int s = 0; s < 3; ++s) pf[s] = __builtin_bit_cast(bf16x8, ld16(rws, pqo + s * 1024));
#pragma unroll
      for (int i = 0; i < 5; ++i) {
        int ins = wt + 4 * i; if (ins >= 18) ins = 17;
        ins = __builtin_amdgcn_readfirstlane(ins);
        glds16c(ws, WS_KVT + cb2 * 18432 + ins * 1024 + l * 16, __builtin_amdgcn_readfirstlane(lds0 + L_KVT + ins * 1024));
      }
    } else {
      const int m128 = (panel2 & 1) * 4 + wt;
      const unsigned zo = WS_Z1 + (unsigned)((((panel2 >> 1) * 8 + cb2) * 4 + (m128 >> 1)) * 6) * 1024 + l * 16 + (m128 & 1) * 8;
#pragma unroll
      for (int n = 0; n < 6; ++n) zreg[n] = __builtin_amdgcn_raw_buffer_load_b64(rws, zo + n * 1024, 0, 16);
    }
    run(24, 32);
#pragma unroll
    for (int n = 0; n < 6; ++n) acc[n] = MFMA(bfr[1][n], af[1], acc[n]);
    STAMP(4);
    wait_vm<0>();
    bar();
    const unsigned L_RED = lds0 + 3 * P3_SLOT;
    if (kh == 1) {
#pragma unroll
      for (int n = 0; n < 6; ++n) stsf4(L_RED + (wt * 6 + n) * 1024 + l * 16, acc[n]);
    }
    bar();
    if (kh == 1) {
      u32x2 ov[5];
#pragma unroll
      for (int et = 0; et < 5; ++et) {
        f32x4 num = {0.f, 0.f, 0.f, 0.f}, den = {0.f, 0.f, 0.f, 0.f};
        const int st0 = et >= 3 ? 1 : 0, nst = et == 0 ? 1 : 2, f0 = et == 0 ? 0 : 1 + (et - 1) * 2;
#pragma unroll
        for (int s = 0; s < nst; ++s) {
          bf16x8 ka = lds_b128(lds0 + L_KVT + (f0 + s) * 1024 + l * 16), za = lds_b128(lds0 + L_KVT + (9 + f0 + s) * 1024 + l * 16);
          num = MFMA(ka, pf[st0 + s], num); den = MFMA(za, pf[st0 + s], den);
        }
        float v[4];
#pragma unroll
        for (int j = 0; j < 4; ++j) v[j] = num[j] * __builtin_amdgcn_rcpf(den[j] + 1e-6f);
        ov[et] = (u32x2){cvt_pk(v[0], v[1]), cvt_pk(v[2], v[3])};
      }
      const unsigned ob = WS_OH + (unsigned)((panel2 * 44 + ((cb2 * 5) >> 1)) * 4 + wt) * 1024 + l * 16;
      if ((cb2 & 1) == 0) {
        st16(rws, ob, (u32x4){ov[0][0], ov[0][1], ov[1][0], ov[1][1]});
        st16(rws, ob + 4096, (u32x4){ov[2][0], ov[2][1], ov[3][0], ov[3][1]});
        st8(ws, ob + 8192, ov[4]);
      } else {
        st8(ws, ob + 8, ov[0]);
        st16(rws, ob + 4096, (u32x4){ov[1][0], ov[1][1], ov[2][0], ov[2][1]});
        st16(rws, ob + 8192, (u32x4){ov[3][0], ov[3][1], ov[4][0], ov[4][1]});
      }
      wait_vm<0>();
    } else {
#pragma unroll
      for (int n = 0; n < 6; ++n) acc[n] += ldsf4(L_RED + (wt * 6 + n) * 1024 + l * 16);
      u32x2 hv[6];
#pragma unroll
      for (int n = 0; n < 6; ++n) {
        f32x4 gg = acc[n] + bs;
        hv[n] = (u32x2){cvt_pk(lo16(zreg[n][0]) * gg[0], hi16(zreg[n][0]) * gg[1]), cvt_pk(lo16(zreg[n][1]) * gg[2], hi16(zreg[n][1]) * gg[3])};
      }
      const unsigned hb = WS_OH + (unsigned)((panel2 * 44 + 20 + cb2 * 3) * 4 + wt) * 1024 + l * 16;
#pragma unroll
      for (int s = 0; s < 3; ++s) st16(rws, hb + s * 4096, (u32x4){hv[2 * s][0], hv[2 * s][1], hv[2 * s + 1][0], hv[2 * s + 1][1]});
      wait_vm<0>();
    }
    bar();
    if (tid == 0) st8u(ws, WS_F2 + (panel2 * 8 + cb2) * 64, tag);
    STAMP(5);
  }
  {
    const int wsr = w & 3, kh = w >> 2;
    const unsigned ao = (unsigned)(kh * 8192 + wsr * 1024 + l * 16);
    const unsigned bb = 16384u + kh * 10240 + l * 16;
    const bf16x8 zeros = {0, 0, 0, 0, 0, 0, 0, 0};
    f32x4 acc[5];
#pragma unroll
    for (int n = 0; n < 5; ++n) acc[n] = (f32x4){0.f, 0.f, 0.f, 0.f};
    bf16x8 af[2], bfr[2][5];
    af[1] = zeros;
#pragma unroll
    for (int n = 0; n < 5; ++n) bfr[1][n] = zeros;
    u32x4 xv[2];
    const int c0 = tid, c1 = tid + 512;
    const size_t xo0 = (size_t)(panel3 * 64 + c0 / 10) * D_ + cb3 * 80 + (c0 % 10) * 8;
    const size_t xo1 = (size_t)(panel3 * 64 + c1 / 10) * D_ + cb3 * 80 + (c1 % 10) * 8;
    xv[0] = *(const u32x4*)(P.x + xo0);
    if (tid < 128) xv[1] = *(const u32x4*)(P.x + xo1);
    unsigned sb = lds0;
    auto rd = [&](int s) {
      af[s] = lds_b128(sb + ao + s * 4096);
#pragma unroll
      for (int n = 0; n < 5; ++n) bfr[s][n] = lds_b128(sb + bb + s * 5120 + n * 1024);
      SB0;
    };
    auto mm = [&](int s) {
#pragma unroll
      for (int n = 0; n < 5; ++n) acc[n] = MFMA(bfr[s][n], af[s], acc[n]);
      SB0;
    };
#pragma unroll 1
    for (int u = 0; u < 11; ++u) {
      bar();
      rd(0); mm(1);
      rd(1); mm(0);
      sb += P3_SLOT; if (sb == lds0 + 4 * P3_SLOT) sb = lds0;
    }
    mm(1);
    STAMP(6);
    bar();
#pragma unroll
    for (int n = 0; n < 5; ++n) stsf4(lds0 + (unsigned)(((kh * 64 + wsr * 16 + fr) * 84 + n * 16 + 4 * g) * 4), acc[n]);
    bar();
#pragma unroll
    for (int i = 0; i < 2; ++i) {
      if (i == 0 || tid < 128) {
        const int c = tid + i * 512, r = c / 10, ch = c - r * 10;
        const unsigned a = lds0 + (unsigned)((r * 84 + ch * 8) * 4);
        f32x4 a0 = ldsf4(a) + ldsf4(a + 64 * 84 * 4), a1 = ldsf4(a + 16) + ldsf4(a + 64 * 84 * 4 + 16);
        u32x4 o = {cvt_pk(lo16(xv[i][0]) + a0[0], hi16(xv[i][0]) + a0[1]), cvt_pk(lo16(xv[i][1]) + a0[2], hi16(xv[i][1]) + a0[3]),
                   cvt_pk(lo16(xv[i][2]) + a1[0], hi16(xv[i][2]) + a1[1]), cvt_pk(lo16(xv[i][3]) + a1[2], hi16(xv[i][3]) + a1[3])};
        *(u32x4*)(P.out + (i == 0 ? xo0 : xo1)) = o;
      }
    }
    STAMP(7);
  }
}

extern "C" void kernel_launch(void* const* d_in, const int* in_sizes, int n_in, void* d_out, int out_size,
                              void* d_ws, size_t ws_size, hipStream_t stream) {
  Params P;
  P.x = (const u16*)d_in[0];
  P.wqkv = (const u16*)d_in[1];
  P.wo = (const u16*)d_in[2];
  P.win = (const u16*)d_in[4];
  P.wsp = (const u16*)d_in[5];
  P.bsp = (const u16*)d_in[6];
  P.wout = (const u16*)d_in[7];
  P.out = (u16*)d_out;
  P.ws = (char*)d_ws;
  (void)hipFuncSetAttribute((const void*)mega_kernel, hipFuncAttributeMaxDynamicSharedMemorySize, LDS_TOTAL);
  mega_kernel<<<256, 768, LDS_TOTAL, stream>>>(P);
}
```

```cpp
#include <hip/hip_runtime.h>
#include <hip/hip_bf16.h>
#include <stdint.h>

typedef __attribute__((ext_vector_type(8))) short bf16x8;
typedef __attribute__((ext_vector_type(4))) short bf16x4;
typedef __attribute__((ext_vector_type(4))) float f32x4;
typedef __attribute__((ext_vector_type(2))) float f32x2;
typedef __attribute__((ext_vector_type(2))) __bf16 bf2;
typedef __attribute__((ext_vector_type(4))) unsigned u32x4;
typedef __attribute__((ext_vector_type(2))) unsigned u32x2;
typedef unsigned short u16;
typedef unsigned long long u64;
#define LAS __attribute__((address_space(3)))
#define GAS __attribute__((address_space(1)))
#define DI __device__ __forceinline__
#define RLX_AGENT __ATOMIC_RELAXED, __HIP_MEMORY_SCOPE_AGENT

constexpr int S_ = 2048, D_ = 640, NQKV = 1920, FIN = 1536, FH = 768;

constexpr unsigned WS_PQ  = 0;
constexpr unsigned WS_Z1  = WS_PQ + 16 * 8 * 8 * 3 * 1024;
constexpr unsigned WS_Z2N = WS_Z1 + 16 * 8 * 24 * 1024;
constexpr unsigned WS_OH  = WS_Z2N + 8 * 64 * 6 * 1024;
constexpr unsigned WS_WF  = WS_OH + 32 * 44 * 4 * 1024;
constexpr unsigned WS_KVT = WS_WF + 8 * 44 * 5 * 1024;
constexpr unsigned WS_KVP = WS_KVT + 8 * 18 * 1024;
constexpr unsigned WS_GR  = WS_KVP + 8 * 18 * 16 * 1024;
constexpr unsigned WS_GX  = WS_GR + 16 * 8 * 256 * 8;
constexpr unsigned WS_GC  = WS_GX + 2048 * 16;
constexpr unsigned WS_F1  = WS_GC + 24 * 10 * 2048;
constexpr unsigned WS_F2  = WS_F1 + 16 * 24 * 64;
constexpr unsigned WS_FO  = WS_F2 + 1024 * 64;
constexpr unsigned WS_FK  = WS_FO + 256 * 64;
constexpr unsigned WS_EP  = WS_FK + 256 * 64;
constexpr unsigned WS_END = WS_EP + 64;

#define STAMP(i)

DI float bf2f(u16 v) { return __uint_as_float(((unsigned)v) << 16); }
DI unsigned cvt_pk(float lo, float hi) {
  bf2 r; r[0] = (__bf16)lo; r[1] = (__bf16)hi; return __builtin_bit_cast(unsigned, r);
}
DI float rbf(float x) { return (float)((__bf16)x); }
DI float lo16(unsigned u) { return __uint_as_float(u << 16); }
DI float hi16(unsigned u) { return __uint_as_float(u & 0xffff0000u); }

DI void glds16(const void* gbase, unsigned voff, unsigned lds_dst) {
  unsigned keep;
  asm volatile("s_mov_b32 %0, m0\n\ts_mov_b32 m0, %3\n\ts_nop 0\n\tglobal_load_lds_dwordx4 %1, %2\n\ts_mov_b32 m0, %0"
               : "=&s"(keep) : "v"(voff), "s"(gbase), "s"(lds_dst) : "memory");
}
DI void glds16c(const void* gbase, unsigned voff, unsigned lds_dst) {
  unsigned keep;
  asm volatile("s_mov_b32 %0, m0\n\ts_mov_b32 m0, %3\n\ts_nop 0\n\tglobal_load_lds_dwordx4 %1, %2 sc1\n\ts_mov_b32 m0, %0"
               : "=&s"(keep) : "v"(voff), "s"(gbase), "s"(lds_dst) : "memory");
}
#define GL_PRE "s_mov_b32 %0, m0\n\ts_mov_b32 m0, %2\n\ts_nop 0\n\t"
#define GL_POST "s_mov_b32 m0, %0"
DI void gl2(const void* gb, unsigned lds, unsigned v0, unsigned v1) {
  unsigned keep;
  asm volatile(GL_PRE "global_load_lds_dwordx4 %3, %1\n\tglobal_load_lds_dwordx4 %4, %1 offset:1024\n\t" GL_POST
               : "=&s"(keep) : "s"(gb), "s"(lds), "v"(v0), "v"(v1) : "memory");
}
DI void gl3(const void* gb, unsigned lds, unsigned v0, unsigned v1, unsigned v2) {
  unsigned keep;
  asm volatile(GL_PRE "global_load_lds_dwordx4 %3, %1\n\tglobal_load_lds_dwordx4 %4, %1 offset:1024\n\tglobal_load_lds_dwordx4 %5, %1 offset:2048\n\t" GL_POST
               : "=&s"(keep) : "s"(gb), "s"(lds), "v"(v0), "v"(v1), "v"(v2) : "memory");
}
DI void gl4(const void* gb, unsigned lds, unsigned v0, unsigned v1, unsigned v2, unsigned v3) {
  unsigned keep;
  asm volatile(GL_PRE "global_load_lds_dwordx4 %3, %1\n\tglobal_load_lds_dwordx4 %4, %1 offset:1024\n\tglobal_load_lds_dwordx4 %5, %1 offset:2048\n\tglobal_load_lds_dwordx4 %6, %1 offset:3072\n\t" GL_POST
               : "=&s"(keep) : "s"(gb), "s"(lds), "v"(v0), "v"(v1), "v"(v2), "v"(v3) : "memory");
}
DI void gl3c(const void* gb, unsigned lds, unsigned v0) {
  unsigned keep;
  asm volatile(GL_PRE "global_load_lds_dwordx4 %3, %1 sc1\n\tglobal_load_lds_dwordx4 %3, %1 offset:1024 sc1\n\tglobal_load_lds_dwordx4 %3, %1 offset:2048 sc1\n\t" GL_POST
               : "=&s"(keep) : "s"(gb), "s"(lds), "v"(v0) : "memory");
}
DI void gl4c(const void* gb, unsigned lds, unsigned v0) {
  unsigned keep;
  asm volatile(GL_PRE "global_load_lds_dwordx4 %3, %1 sc1\n\tglobal_load_lds_dwordx4 %3, %1 offset:1024 sc1\n\tglobal_load_lds_dwordx4 %3, %1 offset:2048 sc1\n\tglobal_load_lds_dwordx4 %3, %1 offset:3072 sc1\n\t" GL_POST
               : "=&s"(keep) : "s"(gb), "s"(lds), "v"(v0) : "memory");
}
template <int N> DI void wait_vm() { asm volatile("s_waitcnt vmcnt(%0)" :: "n"(N) : "memory"); }

DI bf16x8 lds_b128(unsigned addr) { return *(LAS bf16x8*)(uintptr_t)addr; }
DI bf16x8 lds_2b64(unsigned addr0, unsigned addr1) {
  bf16x4 a = *(LAS bf16x4*)(uintptr_t)addr0, b = *(LAS bf16x4*)(uintptr_t)addr1;
  return __builtin_shufflevector(a, b, 0, 1, 2, 3, 4, 5, 6, 7);
}
DI bf16x8 lds_tr(unsigned addr0, unsigned addr1) {
  bf16x4 a = __builtin_amdgcn_ds_read_tr16_b64_v4i16((LAS bf16x4*)(uintptr_t)addr0);
  bf16x4 b = __builtin_amdgcn_ds_read_tr16_b64_v4i16((LAS bf16x4*)(uintptr_t)addr1);
  return __builtin_shufflevector(a, b, 0, 1, 2, 3, 4, 5, 6, 7);
}
DI float ldsf(unsigned a) { return *(LAS float*)(uintptr_t)a; }
DI void stsf(unsigned a, float v) { *(LAS float*)(uintptr_t)a = v; }
DI f32x4 ldsf4(unsigned a) { return *(LAS f32x4*)(uintptr_t)a; }
DI void stsf4(unsigned a, f32x4 v) { *(LAS f32x4*)(uintptr_t)a = v; }
#define SB0 __builtin_amdgcn_sched_barrier(0)
#define MFMA(a, b, c) __builtin_amdgcn_mfma_f32_16x16x32_bf16((a), (b), (c), 0, 0, 0)

template <int CTRL> DI float dpp(float v) {
  return __builtin_bit_cast(float, __builtin_amdgcn_update_dpp(0, __builtin_bit_cast(int, v), CTRL, 0xf, 0xf, true));
}
DI float sum8(float v) { v += dpp<0xB1>(v); v += dpp<0x4E>(v); v += dpp<0x141>(v); return v; }
DI float sum16(float v) { v = sum8(v); v += dpp<0x140>(v); return v; }

DI float gelu_t(float x) {
  float u = x * (0.7978845608f + 0.0356774081f * x * x);
  float e = __builtin_amdgcn_exp2f(-2.885390082f * u);
  return x * __builtin_amdgcn_rcpf(1.0f + e);
}
DI float phi_f(float t) {
  return (fmaxf(t, 0.f) + 1e-6f) + __builtin_amdgcn_exp2f(fminf(t, 0.f) * 1.4426950409f);
}

typedef __amdgpu_buffer_rsrc_t rsrc_t;
DI void st16(rsrc_t r, unsigned off, u32x4 v) { __builtin_amdgcn_raw_buffer_store_b128(v, r, off, 0, 16); }
DI u32x4 ld16(rsrc_t r, unsigned off) { return __builtin_amdgcn_raw_buffer_load_b128(r, off, 0, 16); }
DI void st8(char* ws, unsigned off, u32x2 v) { __hip_atomic_store((GAS u64*)(ws + off), ((u64)v[1] << 32) | v[0], RLX_AGENT); }
DI void st8u(char* ws, unsigned off, u64 v) { __hip_atomic_store((GAS u64*)(ws + off), v, RLX_AGENT); }
DI void st4(char* ws, unsigned off, float v) { __hip_atomic_store((GAS unsigned*)(ws + off), __float_as_uint(v), RLX_AGENT); }
DI u64 ld8(const char* ws, unsigned off) { return __hip_atomic_load((GAS u64*)(ws + off), RLX_AGENT); }
constexpr unsigned SPIN_CAP = 1u << 20;
DI void wait_words(const char* ws, unsigned off, bool act, u64 tag) {
#pragma unroll 1
  for (unsigned sp = 0; sp < SPIN_CAP; ++sp) {
    u64 v = act ? ld8(ws, off) : tag;
    if (__all(v == tag)) break;
    __builtin_amdgcn_s_sleep(1);
  }
  __builtin_amdgcn_fence(__ATOMIC_ACQUIRE, "wavefront");
}

struct Params { const u16 *x, *wqkv, *wo, *win, *wsp, *bsp, *wout; u16* out; char* ws; };

constexpr unsigned KBS = 4224;
constexpr unsigned K1_STAGE = 16384 + 8 * KBS;
constexpr unsigned L_MISC = 153600;
constexpr int LDS_TOTAL = 163840;
constexpr unsigned P2_SLOT = 20480;
constexpr unsigned L_KVT = 135168;
constexpr unsigned P3_SLOT = 36864;

DI void bar() { asm volatile("s_waitcnt lgkmcnt(0)" ::: "memory"); __builtin_amdgcn_s_barrier(); asm volatile("" ::: "memory"); }

struct Side { u32x4 r0, r1; unsigned wv[8]; };
DI void side_issue(const Params& P, Side& S, int row, int w) {
  const int l = threadIdx.x & 63;
  const u32x4* src = (const u32x4*)(P.x + (size_t)row * D_);
  S.r0 = src[l]; S.r1 = src[64 + (l & 15)];
  int item = blockIdx.x; if (item > 219) item = 219;
  const int st = item / 5, T = (item - st * 5) * 8 + w;
  const u16* wsrc = (st < 20 ? P.wo + (size_t)st * 32 * D_ : P.wout + (size_t)(st - 20) * 32 * D_) + (size_t)(l >> 4) * 4 * D_ + T * 16 + (l & 15);
#pragma unroll
  for (int e = 0; e < 8; ++e) S.wv[e] = wsrc[(size_t)(16 * (e >> 2) + (e & 3)) * D_];
}
DI float rdl(float v, int lane) { return __builtin_bit_cast(float, __builtin_amdgcn_readlane(__builtin_bit_cast(int, v), lane)); }
DI void side_finish(const Params& P, rsrc_t rws, const Side& S, int row, int w, unsigned tl, unsigned th) {
  const int l = threadIdx.x & 63;
  float s = 0.f, q = 0.f, s2 = 0.f, q2 = 0.f;
#pragma unroll
  for (int j = 0; j < 4; ++j) {
    float a = lo16(S.r0[j]), b = hi16(S.r0[j]), c = lo16(S.r1[j]), d = hi16(S.r1[j]);
    s += a + b; q += a * a + b * b; s2 += c + d; q2 += c * c + d * d;
  }
  s = sum16(s); q = sum16(q); s2 = sum16(s2); q2 = sum16(q2);
  s = (rdl(s, 0) + rdl(s, 16)) + (rdl(s, 32) + rdl(s, 48)) + s2;
  q = (rdl(q, 0) + rdl(q, 16)) + (rdl(q, 32) + rdl(q, 48)) + q2;
  const float m = s * (1.f / D_);
  const float v = rbf(fmaxf(q * (1.f / D_) - m * m, 0.f));
  const float r = 1.f / rbf(sqrtf(rbf(v + 1e-5f)));
  if (l < 2) st8u(P.ws, WS_GX + row * 16 + l * 8, l == 0 ? (((u64)tl << 32) | __float_as_uint(-rbf(m) * r)) : (((u64)th << 32) | __float_as_uint(r)));
  int item = blockIdx.x; if (item > 219) item = 219;
  const int st = item / 5, T = (item - st * 5) * 8 + w, cb = T / 5, nt = T - cb * 5;
  st16(rws, WS_WF + (unsigned)(((cb * 44 + st) * 5 + nt) * 1024) + l * 16,
       (u32x4){S.wv[0] | (S.wv[1] << 16), S.wv[2] | (S.wv[3] << 16), S.wv[4] | (S.wv[5] << 16), S.wv[6] | (S.wv[7] << 16)});
}
DI void xstat_gather(const char* ws, int panel, unsigned L_ST, int tid, unsigned tg, u64 gv) {
  if (tid < 256) {
#pragma unroll 1
    for (unsigned sp = 0; sp < SPIN_CAP; ++sp) {
      if (__all((unsigned)(gv >> 32) == tg)) break;
      __builtin_amdgcn_s_sleep(1);
      gv = ld8(ws, WS_GX + panel * 2048 + tid * 8);
    }
    stsf(L_ST + (tid & 1) * 512 + (tid >> 1) * 4, __uint_as_float((unsigned)gv));
  }
}

DI void csum_issue(const char* ws, int blk, int t, u64 (&cv)[10]) {
#pragma unroll
  for (int k = 0; k < 10; ++k) cv[k] = ld8(ws, WS_GC + (blk * 10 + k) * 2048 + t * 8);
}
DI void csum_gather(const char* ws, int blk, int t, unsigned tl, u64 (&cv)[10], unsigned dst) {
#pragma unroll 1
  for (unsigned sp = 0; sp < SPIN_CAP; ++sp) {
    bool ok = true;
#pragma unroll
    for (int k = 0; k < 10; ++k) ok &= (unsigned)(cv[k] >> 32) == tl;
    if (__all(ok)) break;
    __builtin_amdgcn_s_sleep(1);
    csum_issue(ws, blk, t, cv);
  }
  float a = 0.f;
#pragma unroll
  for (int k = 0; k < 10; ++k) a += __uint_as_float((unsigned)cv[k]);
  stsf(dst + t * 4, a);
}
DI void csum_store(char* ws, unsigned off, f32x4 d, int fr, int g, unsigned tl) {
  const float v = fr == 0 ? d[0] : fr == 1 ? d[1] : fr == 2 ? d[2] : d[3];
  if (fr < 4) st8u(ws, off + (4 * g + fr) * 8, ((u64)tl << 32) | __float_as_uint(v));
}

DI void p1q_consumer(const Params& P, rsrc_t rws, int w, int panel, int cbi, unsigned lds0, u64 tag, bf16x8 (&pf)[3]) {
  char* ws = P.ws;
  const int tid = threadIdx.x, l = tid & 63;
  const int wm = w >> 2, wc = (w + 2 * wm) & 3;
  const int g = l >> 4, fr = l & 15, q = (l & 15) >> 2, p = l & 3, g1 = g & 1;
  const unsigned aoff0 = (unsigned)((wm * 64 + fr) * 128 + ((g ^ (l & 7)) << 4));
  const unsigned ttab = (unsigned)(0x4498E765DC32BA10ull >> (16 * wc)) & 0xffffu;
  int tile[4];
  unsigned bo[4];
#pragma unroll
  for (int n = 0; n < 4; ++n) {
    tile[n] = (ttab >> (4 * n)) & 15;
    bo[n] = 16384u + g * KBS + q * 480 + p * 8 + tile[n] * 32;
  }
  const unsigned tl = (unsigned)tag, th = tl * 0x9E3779B1u ^ (unsigned)(tag >> 32);
  {
    Side S; side_issue(P, S, panel * 128 + cbi * 8 + w, w);
    side_finish(P, rws, S, panel * 128 + cbi * 8 + w, w, tl, th);
  }
  f32x4 acc[4][4];
#pragma unroll
  for (int m = 0; m < 4; ++m)
#pragma unroll
    for (int n = 0; n < 4; ++n) acc[m][n] = (f32x4){0.f, 0.f, 0.f, 0.f};
  const short one = 0x3F80;
  const bf16x8 ones = {one, one, one, one, one, one, one, one};
  const bf16x8 zeros = {0, 0, 0, 0, 0, 0, 0, 0};

  bf16x8 af[2][4], bfr[2][4];
#pragma unroll
  for (int m = 0; m < 4; ++m) { af[1][m] = zeros; bfr[1][m] = zeros; }
  auto mm = [&](int ks) {
#pragma unroll
    for (int n = 0; n < 3; ++n)
#pragma unroll
      for (int m = 0; m < 4; ++m) acc[m][n] = MFMA(bfr[ks][n], af[ks][m], acc[m][n]);
    if (wc < 3) {
#pragma unroll
      for (int m = 0; m < 4; ++m) acc[m][3] = MFMA(bfr[ks][3], af[ks][m], acc[m][3]);
    }
  };
  auto step = [&](unsigned sb, int ks) {
#pragma unroll
    for (int n = 0; n < 4; ++n) bfr[ks][n] = lds_tr(sb + ks * 4 * KBS + bo[n], sb + ks * 4 * KBS + bo[n] + 1920);
#pragma unroll
    for (int m = 0; m < 4; ++m) af[ks][m] = lds_b128(sb + ((aoff0 ^ (ks * 64)) + m * 2048));
    SB0;
    mm(ks ^ 1);
    SB0;
  };
  unsigned sb = lds0;
  auto run = [&](int n) {
#pragma unroll 1
    for (int kt = 0; kt < n; ++kt) {
      bar();
      step(sb, 0);
      step(sb, 1);
      sb += K1_STAGE; if (sb == lds0 + 3 * K1_STAGE) sb = lds0;
    }
  };
  run(1);
  if (panel < 10) {
#pragma unroll
    for (int h = 0; h < 2; ++h) {
      const bf16x8 b0 = wm == 0 ? bfr[0][h] : bfr[0][2 + h], b1 = wm == 0 ? bfr[1][h] : bfr[1][2 + h];
      const int tt = wm == 0 ? tile[h] : tile[2 + h];
      f32x4 d = MFMA(b0, ones, ((f32x4){0.f, 0.f, 0.f, 0.f}));
      d = MFMA(b1, ones, d);
      csum_store(ws, WS_GC + (cbi * 10 + panel) * 2048 + tt * 128, d, fr, g, tl);
    }
  }
  run(9);
  mm(1);
  STAMP(1);
  bar();
  STAMP(10);
  const unsigned L_ST = lds0 + L_MISC, L_CS = lds0 + L_MISC + 5120;
  const unsigned L_K = lds0, L_V = L_K + 20480, L_Q = lds0 + 122880;
  float mr[4], rinv[4];
#pragma unroll
  for (int m = 0; m < 4; ++m) { const int rl = wm * 64 + m * 16 + fr; mr[m] = ldsf(L_ST + rl * 4); rinv[m] = ldsf(L_ST + 512 + rl * 4); }
#pragma unroll
  for (int n = 0; n < 4; ++n) {
    if (n < 3 || wc < 3) {
      const int t = tile[n];
      const f32x4 cs = ldsf4(L_CS + (t * 16 + 4 * g) * 4);
      unsigned dst, dstep;
      if (t < 5) { dst = L_Q + (wm * 12 + (t >> 1)) * 1024 + l * 16 + (t & 1) * 8; dstep = 3072; }
      else {
        const int kb = wm * 8 + (fr >> 3), pos = (fr & 7) ^ ((fr >> 3) << 2);
        dst = (t < 10 ? L_K + (t - 5) * 256 : L_V + (t - 10) * 256) + kb * 1280 + pos * 32 + g * 8; dstep = 2560;
      }
      if (t < 10) {
#pragma unroll
        for (int m = 0; m < 4; ++m) {
          f32x4 v = acc[m][n] * rinv[m] + cs * mr[m];
#pragma unroll
          for (int j = 0; j < 4; ++j) v[j] = phi_f(v[j]);
          *(LAS u32x2*)(uintptr_t)(dst + m * dstep) = (u32x2){cvt_pk(v[0], v[1]), cvt_pk(v[2], v[3])};
        }
      } else {
#pragma unroll
        for (int m = 0; m < 4; ++m) {
          f32x4 v = acc[m][n] * rinv[m] + cs * mr[m];
          *(LAS u32x2*)(uintptr_t)(dst + m * dstep) = (u32x2){cvt_pk(v[0], v[1]), cvt_pk(v[2], v[3])};
        }
      }
    }
  }
  STAMP(8);
  bar();
  {
    const unsigned troff = (unsigned)(g * 5 * 256 + ((4 * g1 + q) * 32) + p * 8);
    f32x4 d[3];
    int jb[3];
#pragma unroll
    for (int r = 0; r < 3; ++r) {
      int job = w + 8 * r; if (job >= 18) job = w;
      jb[r] = job;
      const int i = job < 13 ? (job + 1) / 3 : job - 13;
      const int j = job < 13 ? job - 2 * i : 0;
      d[r] = (f32x4){0.f, 0.f, 0.f, 0.f};
#pragma unroll
      for (int ks = 0; ks < 4; ++ks) {
        const unsigned a0 = L_K + troff + (ks * 20 + i) * 256, b0 = L_V + troff + (ks * 20 + j) * 256;
        const bf16x8 ka = lds_tr(a0, a0 ^ 128);
        bf16x8 vb = lds_tr(b0, b0 ^ 128);
        if (job >= 13) vb = ones;
        d[r] = MFMA(ka, vb, d[r]);
      }
    }
#pragma unroll
    for (int r = 0; r < 3; ++r)
      st16(rws, WS_KVP + (unsigned)(((cbi * 18 + jb[r]) * 16 + panel) * 1024) + l * 16, __builtin_bit_cast(u32x4, d[r]));
    pf[0] = lds_b128(L_Q + w * 3072 + l * 16); pf[1] = lds_b128(L_Q + w * 3072 + 1024 + l * 16);
    const u32x2 q2 = *(LAS u32x2*)(uintptr_t)(L_Q + w * 3072 + 2048 + l * 16);
    pf[2] = __builtin_bit_cast(bf16x8, ((u32x4){q2[0], q2[1], 0u, 0u}));
  }
  STAMP(9);
  wait_vm<0>();
  bar();
  if (tid == 0) st8u(ws, WS_F1 + (panel * 24 + cbi) * 64, tag);
}

DI void p1z_consumer(const Params& P, rsrc_t rws, int w, int panel, int cbj, unsigned lds0, u64 tag) {
  char* ws = P.ws;
  const int tid = threadIdx.x, l = tid & 63;
  const int wm = w & 3, wn = w >> 2;
  const int g = l >> 4, fr = l & 15, q = (l & 15) >> 2, p = l & 3, g1 = g & 1;
  const unsigned aoff0 = (unsigned)((wm * 32 + fr) * 128 + ((g ^ (l & 7)) << 4));
  unsigned bo[3];
  int ca[3];
#pragma unroll
  for (int n = 0; n < 3; ++n) {
    const int c = wn * 3 + n;
    ca[n] = c;
    int pc = c + g1; if (pc == 6) pc = 0;
    bo[n] = 16384u + g * 1536 + q * 192 + pc * 32 + p * 8;
  }
  const short one = 0x3F80;
  const bf16x8 ones = {one, one, one, one, one, one, one, one};
  const bf16x8 zeros = {0, 0, 0, 0, 0, 0, 0, 0};
  f32x4 accA[2][3], accB[2][3];
#pragma unroll
  for (int m = 0; m < 2; ++m)
#pragma unroll
    for (int n = 0; n < 3; ++n) { accA[m][n] = (f32x4){0.f, 0.f, 0.f, 0.f}; accB[m][n] = (f32x4){0.f, 0.f, 0.f, 0.f}; }
  const unsigned tl = (unsigned)tag, th = tl * 0x9E3779B1u ^ (unsigned)(tag >> 32), tg = (tid & 1) ? th : tl;
  bf16x8 af[2][2], bfr[2][3];
  af[1][0] = af[1][1] = zeros; bfr[1][0] = bfr[1][1] = bfr[1][2] = zeros;
  auto rd = [&](unsigned sb, int ks) {
#pragma unroll
    for (int m = 0; m < 2; ++m) af[ks][m] = lds_b128(sb + ((aoff0 ^ (ks * 64)) + m * 2048));
#pragma unroll
    for (int n = 0; n < 3; ++n) { const unsigned o = sb + ks * 4 * 1536 + bo[n]; bfr[ks][n] = lds_tr(o, o + 768); }
    SB0;
  };
  auto mmA = [&](int ks) {
#pragma unroll
    for (int n = 0; n < 3; ++n)
#pragma unroll
      for (int m = 0; m < 2; ++m) accA[m][n] = MFMA(af[ks][m], bfr[ks][n], accA[m][n]);
    SB0;
  };
  auto mmB = [&](int ks) {
#pragma unroll
    for (int n = 0; n < 3; ++n)
#pragma unroll
      for (int m = 0; m < 2; ++m) accB[m][n] = MFMA(bfr[ks][n], af[ks][m], accB[m][n]);
    SB0;
  };
  const unsigned L_ST = lds0 + L_MISC, L_CS = lds0 + L_MISC + 5120;
  const unsigned L_RS = lds0 + L_MISC + 2048;
  const unsigned L_FS = lds0 + L_MISC + 4096;
  unsigned sb = lds0;
  auto nxt = [&]() { sb += K1_STAGE; if (sb == lds0 + 3 * K1_STAGE) sb = lds0; };
  auto runA = [&](int n) {
#pragma unroll 1
    for (int kt = 0; kt < n; ++kt) { bar(); rd(sb, 0); mmA(1); rd(sb, 1); mmA(0); nxt(); }
  };
  auto runB = [&](int n) {
#pragma unroll 1
    for (int kt = 0; kt < n; ++kt) { bar(); rd(sb, 0); mmB(1); rd(sb, 1); mmB(0); nxt(); }
  };
  auto cpub = [&](int blk) {
    if (panel < 10 && wm == 0) {
#pragma unroll
      for (int n = 0; n < 3; ++n) {
        f32x4 d = MFMA(bfr[0][n], ones, ((f32x4){0.f, 0.f, 0.f, 0.f}));
        d = MFMA(bfr[1][n], ones, d);
        csum_store(ws, WS_GC + (blk * 10 + panel) * 2048 + ca[n] * 128, d, fr, g, tl);
      }
    }
  };
  {
    Side S; side_issue(P, S, panel * 128 + 64 + cbj * 8 + w, w);
    side_finish(P, rws, S, panel * 128 + 64 + cbj * 8 + w, w, tl, th);
  }
  runA(1);
  cpub(8 + cbj);
  runA(6);
  const int tc = tid - 256;
  {
    u64 cv[10];
    if (tid < 256) cv[0] = ld8(ws, WS_GX + panel * 2048 + tid * 8);
    else if (tc < 96) csum_issue(ws, 8 + cbj, tc, cv);
    runA(3);
    if (tid < 256) xstat_gather(ws, panel, L_ST, tid, tg, cv[0]);
    else if (tc < 96) csum_gather(ws, 8 + cbj, tc, tl, cv, L_CS);
  }
  bar(); rd(sb, 0); mmA(1); rd(sb, 1);
  cpub(16 + cbj);
  STAMP(8);
  {
    float cn[3];
#pragma unroll
    for (int n = 0; n < 3; ++n) cn[n] = ldsf(L_CS + (ca[n] * 16 + fr) * 4);
#pragma unroll
    for (int m = 0; m < 2; ++m) {
      const int rl = wm * 32 + m * 16 + 4 * g;
      const f32x4 mr = ldsf4(L_ST + rl * 4), rinv = ldsf4(L_ST + 512 + rl * 4);
      f32x4 rs = {0.f, 0.f, 0.f, 0.f}, rq = {0.f, 0.f, 0.f, 0.f};
#pragma unroll
      for (int n = 0; n < 3; ++n) {
        f32x4 v = accA[m][n] * rinv + mr * cn[n];
#pragma unroll
        for (int j = 0; j < 4; ++j) v[j] = gelu_t(v[j]);
        rs += v; rq += v * v;
        accA[m][n] = v;
      }
#pragma unroll
      for (int j = 0; j < 4; ++j) { rs[j] = sum16(rs[j]); rq[j] = sum16(rq[j]); }
      if (fr == 0) {
        stsf4(L_RS + ((wn * 128 + rl) * 2) * 4, (f32x4){rs[0], rq[0], rs[1], rq[1]});
        stsf4(L_RS + ((wn * 128 + rl) * 2 + 4) * 4, (f32x4){rs[2], rq[2], rs[3], rq[3]});
      }
    }
  }
  mmB(0); nxt();
  bar(); rd(sb, 0);
  float part = 0.f;
  const unsigned gbase = WS_GR + (unsigned)(panel * 8) * 2048 + tid * 8;
  if (tid < 256) {
    part = ldsf(L_RS + tid * 4) + ldsf(L_RS + 1024 + tid * 4);
    st8u(ws, gbase + cbj * 2048, ((u64)tg << 32) | __float_as_uint(part));
  }
  mmB(1); rd(sb, 1); mmB(0); nxt();
  STAMP(9);
  runB(1);
  {
    u64 cv[10];
    if (tid < 256) {
#pragma unroll
      for (int i = 1; i < 8; ++i) cv[i] = ld8(ws, gbase + ((cbj + i) & 7) * 2048);
    }
    runB(1);
    if (tid < 256) {
      float tot;
#pragma unroll 1
      for (unsigned sp = 0; sp < SPIN_CAP; ++sp) {
        bool ok = true; tot = part;
#pragma unroll
        for (int i = 1; i < 8; ++i) { ok &= (unsigned)(cv[i] >> 32) == tg; tot += __uint_as_float((unsigned)cv[i]); }
        if (__all(ok)) break;
#pragma unroll
        for (int i = 1; i < 8; ++i) cv[i] = ld8(ws, gbase + ((cbj + i) & 7) * 2048);
      }
      float other = dpp<0xB1>(tot);
      if ((tid & 1) == 0) {
        float m = tot * (1.f / FH);
        float v = rbf(fmaxf(other * (1.f / FH) - m * m, 0.f));
        float r = 1.f / rbf(sqrtf(rbf(v + 1e-5f)));
        stsf(L_FS + (tid >> 1) * 4, -rbf(m) * r);
        stsf(L_FS + 512 + (tid >> 1) * 4, r);
      }
    }
  }
  bar(); rd(sb, 0);
  {
    f32x4 mr[2], ri[2];
#pragma unroll
    for (int m = 0; m < 2; ++m) { const int rl = wm * 32 + m * 16 + 4 * g; mr[m] = ldsf4(L_FS + rl * 4); ri[m] = ldsf4(L_FS + 512 + rl * 4); }
#pragma unroll
    for (int n = 0; n < 3; ++n) {
      f32x4 a = accA[0][n] * ri[0] + mr[0], b = accA[1][n] * ri[1] + mr[1];
      st16(rws, WS_Z2N + (unsigned)(((cbj * 64 + panel * 4 + wm) * 6 + ca[n]) * 1024) + l * 16,
           (u32x4){cvt_pk(a[0], a[1]), cvt_pk(a[2], a[3]), cvt_pk(b[0], b[1]), cvt_pk(b[2], b[3])});
    }
  }
  mmB(1); rd(sb, 1); mmB(0); nxt();
  STAMP(10);
  runB(2);
  wait_vm<0>();
  bar();
  if (tid == 0) st8u(ws, WS_F1 + (panel * 24 + 8 + cbj) * 64, tag);
  STAMP(11);
  rd(sb, 0); mmB(1); rd(sb, 1); mmB(0); nxt();
  {
    u64 cv[10];
    if (tid >= 256 && tc < 96) csum_issue(ws, 16 + cbj, tc, cv);
    runB(2);
    if (tid >= 256 && tc < 96) csum_gather(ws, 16 + cbj, tc, tl, cv, L_CS + 512);
  }
  mmB(1);
  STAMP(1);
  bar();
  {
    f32x4 cn[3];
#pragma unroll
    for (int n = 0; n < 3; ++n) { const unsigned a = L_CS + 512 + (ca[n] * 16 + 4 * g) * 4; cn[n] = ldsf4(a); }
    float mr[2], rinv[2];
#pragma unroll
    for (int m = 0; m < 2; ++m) { const int rl = wm * 32 + m * 16 + fr; mr[m] = ldsf(L_ST + rl * 4); rinv[m] = ldsf(L_ST + 512 + rl * 4); }
#pragma unroll
    for (int n = 0; n < 3; ++n) {
      u32x4 o;
#pragma unroll
      for (int m = 0; m < 2; ++m) {
        f32x4 v = accB[m][n] * rinv[m] + cn[n] * mr[m];
#pragma unroll
        for (int j = 0; j < 4; ++j) v[j] = gelu_t(v[j]);
        o[2 * m] = cvt_pk(v[0], v[1]); o[2 * m + 1] = cvt_pk(v[2], v[3]);
      }
      if (wm < 2) *(LAS u32x4*)(uintptr_t)(lds0 + L_KVT + 6144 + (wm * 6 + ca[n]) * 1024 + l * 16) = o;
      else st16(rws, WS_Z1 + (unsigned)(((panel * 8 + cbj) * 4 + wm) * 6 + ca[n]) * 1024 + l * 16, o);
    }
  }
}

__global__ __launch_bounds__(768, 3) void mega_kernel(Params P) {
  extern __shared__ __attribute__((aligned(16))) char smem[];
  const unsigned lds0 = (unsigned)(uintptr_t)(LAS char*)smem;
  char* ws = P.ws;
  const int tid = threadIdx.x, l = tid & 63;
  const int w = __builtin_amdgcn_readfirstlane(tid >> 6);
  const int g = l >> 4, fr = l & 15;
  const int b = blockIdx.x, xcd = b & 7, bi = b >> 3;
  const int panel1 = (xcd >> 1) * 4 + (bi & 3), cb1 = (xcd & 1) * 4 + ((bi >> 2) & 3);
  const bool isq = bi < 16;
  const int panel2 = 2 * panel1 + (isq ? 1 : 0), cb2 = cb1;
  const int panel3 = xcd * 4 + (bi & 3), cb3 = bi >> 2;
  STAMP(0);
  const u64 tag = ld8(ws, WS_EP) + 1;
  if (w == 7 && bi < 20) {
    u64 pc; asm volatile("s_getpc_b64 %0" : "=s"(pc));
    u32x4 d0, d1;
    asm volatile("global_load_dwordx4 %0, %2, %3\n\tglobal_load_dwordx4 %1, %2, %3 offset:1024\n\ts_waitcnt vmcnt(0)"
                 : "=&v"(d0), "=&v"(d1) : "v"(bi * 2048 + l * 16), "s"(pc & ~127ull) : "memory");
  }

  if (w >= 8) {
    const int lw = w - 8;
    const unsigned vA0 = (unsigned)(((l >> 3) * D_ + (((l & 7) ^ (l >> 3)) << 3)) * 2);
    const unsigned vA1 = vA0 + 8 * D_ * 2 - 1024, vA2 = vA0 + 16 * D_ * 2 - 2048, vA3 = vA0 + 24 * D_ * 2 - 3072;
    const char* gA = (const char*)(P.x + (size_t)(panel1 * 128 + lw * 32) * D_);
    const unsigned v20 = (unsigned)(((l >> 3) * S_ + (((l & 7) ^ (l >> 4)) << 3)) * 2);
    const unsigned v21 = (unsigned)((((l >> 3) + 8) * S_ + (((l & 7) ^ ((l >> 4) | 4)) << 3)) * 2) - 1024;
    const char* gA2 = (const char*)(P.wsp + (size_t)(panel2 * 64 + lw * 16) * S_);
    const int rot2 = 2 * ((2 * (panel2 & 7) + 4 * (cb2 & 3)) & 15);
    auto ldA = [&](int kt, unsigned sl) { gl2(gA2 + ((kt + rot2) & 31) * 128, sl + lw * 2048, v20, v21); };
    auto sl2 = [&](int kt) { return lds0 + ((kt + 4) % 6) * P2_SLOT; };
    const unsigned tl = (unsigned)tag, th = tl * 0x9E3779B1u ^ (unsigned)(tag >> 32);
    const int lt = tid - 512;
    auto gather = [&](int blk, int ncol, unsigned dst, bool xs) {
      u64 cv[10]; u64 gv = 0;
      const int tc = lt < ncol ? lt : 0;
      if (xs) gv = ld8(ws, WS_GX + panel1 * 2048 + lt * 8);
      csum_issue(ws, blk, tc, cv);
      if (xs) xstat_gather(ws, panel1, lds0 + L_MISC, lt, (lt & 1) ? th : tl, gv);
      csum_gather(ws, blk, tc, tl, cv, dst);
    };
    const unsigned l16 = l * 16;
    const char* gB2 = ws + WS_Z2N + (size_t)cb2 * 64 * 6144 + lw * 3072;
    auto ldB = [&](int kt, unsigned sl) { gl3c(gB2 + ((kt + rot2) & 31) * 12288, sl + 8192 + lw * 3072, l16); };
    const unsigned zfl = WS_F1 + ((l & 15) * 24 + 8 + cb2) * 64;
    auto try_z = [&]() {
      const u64 v = l < 16 ? ld8(ws, zfl) : tag;
      return (bool)__builtin_amdgcn_readfirstlane((int)__all(v == tag));
    };
    bool early = false;
    const int rot = panel1 < 10 ? panel1 : 0;
    auto krot = [&](int kt) { const int k = kt >= 10 ? kt - 10 : kt; return __builtin_amdgcn_readfirstlane(k == 0 ? rot : (k - 1 < rot ? k - 1 : k)); };
    if (isq) {
      gl4(gA + rot * 128, lds0 + lw * 4096, vA0, vA1, vA2, vA3);
      unsigned vB[4];
#pragma unroll
      for (int i = 0; i < 4; ++i) {
        int c = i * 64 + l; if (c > 239) c = 239;
        const int row = c / 30, cc = c - row * 30, seg = cc / 10, ch = cc - seg * 10;
        vB[i] = (unsigned)((row * NQKV + seg * 640 + cb1 * 80 + ch * 8) * 2) + 4096 - i * 1024;
      }
      const char* gB = (const char*)P.wqkv + (size_t)lw * 16 * NQKV * 2 - 4096;
      auto stageB = [&](int kt, unsigned sb) {
        const char* bp = gB + (size_t)krot(kt) * 64 * NQKV * 2;
        gl4(bp, sb + 16384 + lw * 2 * KBS, vB[0], vB[1], vB[2], vB[3]);
        gl4(bp + 8 * NQKV * 2, sb + 16384 + (lw * 2 + 1) * KBS, vB[0], vB[1], vB[2], vB[3]);
      };
      auto stage = [&](int kt, unsigned sb) { gl4(gA + krot(kt) * 128, sb + lw * 4096, vA0, vA1, vA2, vA3); stageB(kt, sb); };
      stageB(0, lds0); stage(1, lds0 + K1_STAGE);
      wait_vm<12>(); bar();
      stage(2, lds0 + 2 * K1_STAGE);
      unsigned sb = lds0, sc = lds0 + K1_STAGE;
#pragma unroll 1
      for (int kt = 1; kt < 8; ++kt) {
        wait_vm<12>(); bar();
        if (kt == 2) STAMP(10);
        if (kt == 7) STAMP(11);
        stage(kt + 2, sb);
        sb = sc; sc += K1_STAGE; if (sc == lds0 + 3 * K1_STAGE) sc = lds0;
      }
      wait_vm<12>(); bar();
      gather(cb1, 240, lds0 + L_MISC + 5120, true);
      bar();
      ldA(0, sl2(0)); ldA(1, sl2(1)); ldA(5, sl2(5));
      bar();
      ldA(4, sl2(4));
#pragma unroll 1
      for (int t = 0; t < 3 && !early; ++t) early = try_z();
      if (early) { ldB(0, sl2(0)); ldB(1, sl2(1)); ldB(4, sl2(4)); ldB(5, sl2(5)); }
      bar();
      if (!early) { early = try_z(); if (early) { ldB(0, sl2(0)); ldB(1, sl2(1)); ldB(4, sl2(4)); ldB(5, sl2(5)); } }
      bar();
    } else {
      gl4(gA + rot * 128, lds0 + lw * 4096, vA0, vA1, vA2, vA3);
      unsigned vB[3];
#pragma unroll
      for (int i = 0; i < 3; ++i) {
        const int c = i * 64 + l, row = c / 12, cc = c - row * 12;
        int pc = (cc >> 1) - (row >> 3); if (pc < 0) pc = 5;
        vB[i] = (unsigned)((row * FIN + cb1 * 96 + pc * 16 + (cc & 1) * 8) * 2) + 4096 - i * 1024;
      }
      const char* gB = (const char*)P.win + (size_t)lw * 16 * FIN * 2 - 4096;
      auto stageB = [&](int kt, unsigned sb) {
        const int k = krot(kt);
        gl3(gB + (kt < 10 ? FH * 2 : 0) + (size_t)k * 64 * FIN * 2, sb + 16384 + lw * 3072, vB[0], vB[1], vB[2]);
      };
      auto stage = [&](int kt, unsigned sb) {
        const int k = krot(kt);
        gl4(gA + k * 128, sb + lw * 4096, vA0, vA1, vA2, vA3); stageB(kt, sb);
      };
      stageB(0, lds0); stage(1, lds0 + K1_STAGE);
      wait_vm<7>(); bar();
      stage(2, lds0 + 2 * K1_STAGE);
      unsigned sb = lds0, sc = lds0 + K1_STAGE;
      auto runL = [&](int k0, int k1) {
#pragma unroll 1
        for (int kt = k0; kt < k1; ++kt) {
          wait_vm<7>(); bar();
          stage(kt + 2, sb);
          sb = sc; sc += K1_STAGE; if (sc == lds0 + 3 * K1_STAGE) sc = lds0;
        }
      };
      runL(1, 11);
      {
        const char* wp = (const char*)(P.wsp + (size_t)(panel1 * 128 + lw * 32) * S_) + (cb1 & 3) * 1024;
        const unsigned vo = (unsigned)((l >> 3) * S_ * 2 + (l & 7) * 128);
        unsigned d0, d1, d2, d3;
        asm volatile("global_load_dword %0, %4, %5\n\tglobal_load_dword %1, %4, %6\n\tglobal_load_dword %2, %4, %7\n\tglobal_load_dword %3, %4, %8"
                     : "=&v"(d0), "=&v"(d1), "=&v"(d2), "=&v"(d3)
                     : "v"(vo), "s"(wp), "s"(wp + 8 * S_ * 2), "s"(wp + 16 * S_ * 2), "s"(wp + 24 * S_ * 2) : "memory");
        runL(11, 13);
        asm volatile("" :: "v"(d0), "v"(d1), "v"(d2), "v"(d3));
      }
      runL(13, 18);
      wait_vm<7>(); bar();
      wait_vm<0>(); bar();
      bar();
      wait_words(ws, zfl, l < 16, tag);
      early = true;
      ldA(0, sl2(0)); ldB(0, sl2(0)); ldA(1, sl2(1)); ldB(1, sl2(1));
      ldA(4, sl2(4)); ldA(5, sl2(5)); ldB(4, sl2(4)); ldB(5, sl2(5));
    }
    STAMP(2);
    if (!early) {
      wait_words(ws, zfl, l < 16, tag);
      ldB(0, sl2(0)); ldB(1, sl2(1)); ldB(4, sl2(4)); ldB(5, sl2(5));
    }
    STAMP(3);
    ldA(2, sl2(2)); ldA(3, sl2(3)); ldB(2, sl2(2)); ldB(3, sl2(3));
    wait_vm<16>(); bar();
    {
      unsigned sl = sl2(0);
      auto ldU = [&](int u) {
        ldA(2 * u, sl); ldB(2 * u, sl); ldA(2 * u + 1, sl + P2_SLOT); ldB(2 * u + 1, sl + P2_SLOT);
        sl += 2 * P2_SLOT; if (sl == lds0 + 6 * P2_SLOT) sl = lds0;
      };
      wait_vm<0>(); bar(); ldU(3);
#pragma unroll 1
      for (int u = 2; u < 14; ++u) {
        wait_vm<10>(); bar();
        if (u == 4) STAMP(8);
        if (u == 10) STAMP(9);
        ldU(u + 2);
      }
    }
    wait_vm<10>(); bar();
    wait_words(ws, WS_F1 + (((lw * 64 + l) >> 4) * 24 + ((l & 8) ? 8 : 0) + (l & 15)) * 64, true, tag);
    wait_vm<0>(); bar();
    STAMP(4);
    const char* gA3 = ws + WS_OH + (size_t)panel3 * 44 * 4096 + lw * 4096;
    const char* gB3 = ws + WS_WF + (size_t)cb3 * 44 * 5120 + lw * 5120;
    int r5 = cb3 + 2 * (bi & 3); r5 = r5 >= 10 ? r5 - 10 : r5 >= 5 ? r5 - 5 : r5;
    int r6 = cb3 + 2 * (bi & 3); r6 = r6 >= 12 ? r6 - 12 : r6 >= 6 ? r6 - 6 : r6;
    auto ur = [&](int u) {
      int v;
      if (u < 5) { v = u + r5; if (v >= 5) v -= 5; } else { v = u + r6; if (v >= 11) v -= 6; }
      return __builtin_amdgcn_readfirstlane(v);
    };
    auto ldB3 = [&](int u, unsigned sl) {
      gl4c(gB3 + ur(u) * 20480, sl + 16384 + lw * 5120, l16);
      glds16c(gB3 + ur(u) * 20480 + 4096, l16, sl + 16384 + lw * 5120 + 4096);
    };
    auto ldA3 = [&](int u, unsigned sl) { gl4c(gA3 + ur(u) * 16384, sl + lw * 4096, l16); };
    bar(); bar();
    ldB3(0, lds0); ldB3(1, lds0 + P3_SLOT); ldB3(2, lds0 + 2 * P3_SLOT);
    wait_words(ws, WS_FO + ((panel3 >> 1) * 8 + (l & 7)) * 64, l < 8, tag);
    ldA3(0, lds0); ldA3(1, lds0 + P3_SLOT); ldA3(2, lds0 + 2 * P3_SLOT);
    bar();
    ldA3(3, lds0 + 3 * P3_SLOT); ldB3(3, lds0 + 3 * P3_SLOT);
    wait_vm<17>(); bar();
    wait_vm<13>(); bar(); ldA3(4, lds0); ldB3(4, lds0);
    wait_vm<18>(); bar(); ldB3(5, lds0 + P3_SLOT);
    wait_vm<14>(); bar(); ldB3(6, lds0 + 2 * P3_SLOT);
    wait_vm<10>(); bar(); ldB3(7, lds0 + 3 * P3_SLOT);
    wait_words(ws, WS_F2 + (panel3 * 32 + (l & 31)) * 64, l < 32, tag);
    STAMP(5);
    ldA3(5, lds0 + P3_SLOT); ldA3(6, lds0 + 2 * P3_SLOT); ldA3(7, lds0 + 3 * P3_SLOT);
    wait_vm<8>(); bar(); ldA3(8, lds0); ldB3(8, lds0);
    wait_vm<13>(); bar(); ldA3(9, lds0 + P3_SLOT); ldB3(9, lds0 + P3_SLOT);
    wait_vm<18>(); bar(); ldA3(10, lds0 + 2 * P3_SLOT); ldB3(10, lds0 + 2 * P3_SLOT);
    wait_vm<18>(); bar(); wait_vm<9>(); bar(); wait_vm<0>(); bar();
    bar(); bar();
    if (b == 0 && lw == 0) {
#pragma unroll
      for (int i = 0; i < 4; ++i) wait_words(ws, WS_F2 + (i * 64 + l) * 256, true, tag);
      if (l == 0) st8u(ws, WS_EP, tag);
    }
    STAMP(6);
    return;
  }

  const rsrc_t rws = __builtin_amdgcn_make_buffer_rsrc(ws, 0, (int)WS_END, 0x00020000);
  bf16x8 pf[3];
  if (isq) p1q_consumer(P, rws, w, panel1, cb1, lds0, tag, pf);
  else p1z_consumer(P, rws, w, panel1, cb1, lds0, tag);
  STAMP(2);

  {
    const int wt = w & 3, kh = w >> 2;
    const int rsw = ((wt * 16 + fr) >> 1) & 7;
    const unsigned ao0 = (unsigned)((wt * 16 + fr) * 128 + (((4 * kh + (g >> 1)) ^ rsw) << 4) + (g & 1) * 8);
    const unsigned ao1 = (unsigned)((wt * 16 + fr) * 128 + (((4 * kh + 2 + (g >> 1)) ^ rsw) << 4) + (g & 1) * 8);
    const unsigned bo = (unsigned)(8192 + kh * 6144 + l * 16);
    const bf16x8 zeros = {0, 0, 0, 0, 0, 0, 0, 0};
    f32x4 acc[6];
#pragma unroll
    for (int n = 0; n < 6; ++n) acc[n] = (f32x4){0.f, 0.f, 0.f, 0.f};
    bf16x8 af[2], bfr[2][6];
    af[1] = zeros;
#pragma unroll
    for (int n = 0; n < 6; ++n) bfr[1][n] = zeros;
    const int zi = (bi & 15) * 8 + xcd;
    const bool reducer = !isq && zi < 72;
    const int rj = zi & 7, rf = zi >> 3;
    const int row = panel2 * 64 + wt * 16 + fr;
    u32x2 zreg[6];
    const float bs = bf2f(P.bsp[row]);
    unsigned sl = lds0 + 4 * P2_SLOT;
    auto step = [&](int cur, int kt) {
      if (cur == 0) bar();
      af[cur] = lds_2b64(sl + ao0, sl + ao1);
#pragma unroll
      for (int n = 0; n < 6; ++n) bfr[cur][n] = lds_b128(sl + bo + n * 1024);
      SB0;
#pragma unroll
      for (int n = 0; n < 6; ++n) acc[n] = MFMA(bfr[cur ^ 1][n], af[cur ^ 1], acc[n]);
      SB0;
      sl += P2_SLOT; if (sl == lds0 + 6 * P2_SLOT) sl = lds0;
    };
    auto run = [&](int k0, int k1) {
#pragma unroll 1
      for (int kt = k0; kt < k1; kt += 2) { step(0, kt); step(1, kt + 1); }
    };
    if (reducer) {
      const int et = (rf + 1) >> 1, st = rf == 0 ? 0 : ((rf + 1) & 1) + (et >= 3 ? 1 : 0);
      wait_words(ws, WS_F1 + ((l & 15) * 24 + rj) * 64, true, tag);
      f32x4 pv[8];
      bool okv[4];
#pragma unroll
      for (int t4 = 0; t4 < 4; ++t4) {
        const int i = 2 * st + (t4 & 1);
        okv[t4] = i < 5 && (t4 >= 2 || (et - i <= 1 && i - et <= 1));
        const int job = okv[t4] ? (t4 >= 2 ? 13 + i : 2 * i + et) : 0;
#pragma unroll
        for (int pp = 0; pp < 2; ++pp)
          pv[t4 * 2 + pp] = __builtin_bit_cast(f32x4, ld16(rws, WS_KVP + (unsigned)(((rj * 18 + job) * 16 + w * 2 + pp) * 1024) + l * 16));
      }
      run(0, 2);
      if (tid == 0) st8u(ws, WS_F1 + (panel1 * 24 + 16 + cb1) * 64, tag);
      const unsigned L_R = lds0 + 122880;
      f32x4 sv[4];
#pragma unroll
      for (int t4 = 0; t4 < 4; ++t4) { sv[t4] = pv[t4 * 2] + pv[t4 * 2 + 1]; if (!okv[t4]) sv[t4] = (f32x4){0.f, 0.f, 0.f, 0.f}; }
      if (w >= 4) {
#pragma unroll
        for (int t4 = 0; t4 < 4; ++t4) stsf4(L_R + ((w - 4) * 4 + t4) * 1024 + l * 16, sv[t4]);
      }
      run(2, 4);
      if (w < 4) {
#pragma unroll
        for (int t4 = 0; t4 < 4; ++t4) stsf4(L_R + (w * 4 + t4) * 1024 + l * 16, sv[t4] + ldsf4(L_R + (w * 4 + t4) * 1024 + l * 16));
      }
      run(4, 6);
      if (w < 2) {
        f32x4 v[2];
#pragma unroll
        for (int h2 = 0; h2 < 2; ++h2) {
          v[h2] = (f32x4){0.f, 0.f, 0.f, 0.f};
#pragma unroll
          for (int ww = 0; ww < 4; ++ww) v[h2] += ldsf4(L_R + (ww * 4 + w * 2 + h2) * 1024 + l * 16);
          const int he = (et * 16 + fr) / 20;
#pragma unroll
          for (int r = 0; r < 4; ++r) { const int d = (2 * st + h2) * 16 + 4 * g + r; if (d / 20 != he) v[h2][r] = 0.f; }
        }
        st16(rws, WS_KVT + (unsigned)(((rj * 2 + w) * 9 + rf) * 1024) + l * 16,
             (u32x4){cvt_pk(v[0][0], v[0][1]), cvt_pk(v[0][2], v[0][3]), cvt_pk(v[1][0], v[1][1]), cvt_pk(v[1][2], v[1][3])});
      }
      run(6, 10);
      wait_vm<0>();
      if (w < 2 && l == 0) st8u(ws, WS_FK + (rj * 32 + w * 16 + rf) * 64, tag);
      run(10, 18);
    } else {
      run(0, 6);
      wait_vm<0>();
      run(6, 8);
      if (!isq && tid == 0) st8u(ws, WS_F1 + (panel1 * 24 + 16 + cb1) * 64, tag);
      run(8, 18);
    }
    if (isq) {
      const unsigned foff = (l & 15) < 9 ? WS_FK + (cb2 * 32 + (l & 31)) * 64 : WS_F1 + (panel1 * 24 + 16 + cb2) * 64;
      const u64 fv = l < 32 ? ld8(ws, foff) : tag;
      run(18, 22);
      if (!__all(fv == tag)) wait_words(ws, foff, l < 32, tag);
#pragma unroll
      for (int i = 0; i < 3; ++i) {
        int ins = w + 8 * i; if (ins >= 18) ins = 17;
        ins = __builtin_amdgcn_readfirstlane(ins);
        glds16c(ws, WS_KVT + cb2 * 18432 + ins * 1024 + l * 16, __builtin_amdgcn_readfirstlane(lds0 + L_KVT + ins * 1024));
      }
      const unsigned zo = WS_Z1 + (unsigned)(((panel1 * 8 + cb2) * 4 + 2 + (wt >> 1)) * 6) * 1024 + l * 16 + (wt & 1) * 8;
#pragma unroll
      for (int n = 0; n < 6; ++n) zreg[n] = __builtin_amdgcn_raw_buffer_load_b64(rws, zo + n * 1024, 0, 16);
      run(22, 24);
      wait_vm<0>();
      run(24, 26);
      u32x2 ov[5];
#pragma unroll
      for (int et = 0; et < 5; ++et) {
        f32x4 num = {0.f, 0.f, 0.f, 0.f}, den = {0.f, 0.f, 0.f, 0.f};
        const int st0 = et >= 3 ? 1 : 0, nst = et == 0 ? 1 : 2, f0 = et == 0 ? 0 : 1 + (et - 1) * 2;
#pragma unroll
        for (int s = 0; s < nst; ++s) {
          bf16x8 ka = lds_b128(lds0 + L_KVT + (f0 + s) * 1024 + l * 16), za = lds_b128(lds0 + L_KVT + (9 + f0 + s) * 1024 + l * 16);
          num = MFMA(ka, pf[st0 + s], num); den = MFMA(za, pf[st0 + s], den);
        }
        float v[4];
#pragma unroll
        for (int j = 0; j < 4; ++j) v[j] = num[j] * __builtin_amdgcn_rcpf(den[j] + 1e-6f);
        ov[et] = (u32x2){cvt_pk(v[0], v[1]), cvt_pk(v[2], v[3])};
      }
      const unsigned ob = WS_OH + (unsigned)(((2 * panel1 + kh) * 44 + ((cb2 * 5) >> 1)) * 4 + wt) * 1024 + l * 16;
      if ((cb2 & 1) == 0) {
        st16(rws, ob, (u32x4){ov[0][0], ov[0][1], ov[1][0], ov[1][1]});
        st16(rws, ob + 4096, (u32x4){ov[2][0], ov[2][1], ov[3][0], ov[3][1]});
        st8(ws, ob + 8192, ov[4]);
      } else {
        st8(ws, ob + 8, ov[0]);
        st16(rws, ob + 4096, (u32x4){ov[1][0], ov[1][1], ov[2][0], ov[2][1]});
        st16(rws, ob + 8192, (u32x4){ov[3][0], ov[3][1], ov[4][0], ov[4][1]});
      }
      run(26, 30);
      wait_vm<0>();
      run(30, 32);
      if (tid == 0) st8u(ws, WS_FO + (panel1 * 8 + cb2) * 64, tag);
    } else {
      run(18, 32);
#pragma unroll
      for (int n = 0; n < 6; ++n) zreg[n] = *(LAS u32x2*)(uintptr_t)(lds0 + L_KVT + 6144 + ((wt >> 1) * 6 + n) * 1024 + l * 16 + (wt & 1) * 8);
    }
#pragma unroll
    for (int n = 0; n < 6; ++n) acc[n] = MFMA(bfr[1][n], af[1], acc[n]);
    STAMP(4);
    bar();
    const unsigned L_RED = lds0 + 3 * P3_SLOT;
    if (kh == 1) {
#pragma unroll
      for (int n = 0; n < 6; ++n) stsf4(L_RED + (wt * 6 + n) * 1024 + l * 16, acc[n]);
    }
    bar();
    if (kh == 0) {
#pragma unroll
      for (int n = 0; n < 6; ++n) acc[n] += ldsf4(L_RED + (wt * 6 + n) * 1024 + l * 16);
      u32x2 hv[6];
#pragma unroll
      for (int n = 0; n < 6; ++n) {
        f32x4 gg = acc[n] + bs;
        hv[n] = (u32x2){cvt_pk(lo16(zreg[n][0]) * gg[0], hi16(zreg[n][0]) * gg[1]), cvt_pk(lo16(zreg[n][1]) * gg[2], hi16(zreg[n][1]) * gg[3])};
      }
      const unsigned hb = WS_OH + (unsigned)((panel2 * 44 + 20 + cb2 * 3) * 4 + wt) * 1024 + l * 16;
#pragma unroll
      for (int s = 0; s < 3; ++s) st16(rws, hb + s * 4096, (u32x4){hv[2 * s][0], hv[2 * s][1], hv[2 * s + 1][0], hv[2 * s + 1][1]});
      wait_vm<0>();
      if (l == 0) st8u(ws, WS_F2 + ((panel2 * 8 + cb2) * 4 + wt) * 64, tag);
    }
    bar();
    STAMP(5);
  }
  {
    const int wsr = w & 3, kh = w >> 2;
    const unsigned ao = (unsigned)(kh * 8192 + wsr * 1024 + l * 16);
    const unsigned bb = 16384u + kh * 10240 + l * 16;
    const bf16x8 zeros = {0, 0, 0, 0, 0, 0, 0, 0};
    f32x4 acc[5];
#pragma unroll
    for (int n = 0; n < 5; ++n) acc[n] = (f32x4){0.f, 0.f, 0.f, 0.f};
    bf16x8 af[2][2], bfr[2][2][5];
#pragma unroll
    for (int s2 = 0; s2 < 2; ++s2) {
      af[1][s2] = zeros;
#pragma unroll
      for (int n = 0; n < 5; ++n) bfr[1][s2][n] = zeros;
    }
    u32x4 xv[2];
    const int c0 = tid, c1 = tid + 512;
    const size_t xo0 = (size_t)(panel3 * 64 + c0 / 10) * D_ + cb3 * 80 + (c0 % 10) * 8;
    const size_t xo1 = (size_t)(panel3 * 64 + c1 / 10) * D_ + cb3 * 80 + (c1 % 10) * 8;
    xv[0] = *(const u32x4*)(P.x + xo0);
    if (tid < 128) xv[1] = *(const u32x4*)(P.x + xo1);
    unsigned sb = lds0;
    auto mm = [&](int c) {
#pragma unroll
      for (int s2 = 0; s2 < 2; ++s2)
#pragma unroll
        for (int n = 0; n < 5; ++n) acc[n] = MFMA(bfr[c][s2][n], af[c][s2], acc[n]);
      SB0;
    };
    auto unit = [&](int c) {
      bar();
#pragma unroll
      for (int s2 = 0; s2 < 2; ++s2) {
        af[c][s2] = lds_b128(sb + ao + s2 * 4096);
#pragma unroll
        for (int n = 0; n < 5; ++n) bfr[c][s2][n] = lds_b128(sb + bb + s2 * 5120 + n * 1024);
      }
      SB0;
      mm(c ^ 1);
      sb += P3_SLOT; if (sb == lds0 + 4 * P3_SLOT) sb = lds0;
    };
#pragma unroll 1
    for (int u = 0; u < 5; ++u) { unit(0); unit(1); }
    unit(0);
    mm(0);
    STAMP(6);
    bar();
#pragma unroll
    for (int n = 0; n < 5; ++n) stsf4(lds0 + (unsigned)(((kh * 64 + wsr * 16 + fr) * 84 + n * 16 + 4 * g) * 4), acc[n]);
    bar();
#pragma unroll
    for (int i = 0; i < 2; ++i) {
      if (i == 0 || tid < 128) {
        const int c = tid + i * 512, r = c / 10, ch = c - r * 10;
        const unsigned a = lds0 + (unsigned)((r * 84 + ch * 8) * 4);
        f32x4 a0 = ldsf4(a) + ldsf4(a + 64 * 84 * 4), a1 = ldsf4(a + 16) + ldsf4(a + 64 * 84 * 4 + 16);
        u32x4 o = {cvt_pk(lo16(xv[i][0]) + a0[0], hi16(xv[i][0]) + a0[1]), cvt_pk(lo16(xv[i][1]) + a0[2], hi16(xv[i][1]) + a0[3]),
                   cvt_pk(lo16(xv[i][2]) + a1[0], hi16(xv[i][2]) + a1[1]), cvt_pk(lo16(xv[i][3]) + a1[2], hi16(xv[i][3]) + a1[3])};
        *(u32x4*)(P.out + (i == 0 ? xo0 : xo1)) = o;
      }
    }
    STAMP(7);
  }
}

extern "C" void kernel_launch(void* const* d_in, const int* in_sizes, int n_in, void* d_out, int out_size,
                              void* d_ws, size_t ws_size, hipStream_t stream) {
  Params P;
  P.x = (const u16*)d_in[0];
  P.wqkv = (const u16*)d_in[1];
  P.wo = (const u16*)d_in[2];
  P.win = (const u16*)d_in[4];
  P.wsp = (const u16*)d_in[5];
  P.bsp = (const u16*)d_in[6];
  P.wout = (const u16*)d_in[7];
  P.out = (u16*)d_out;
  P.ws = (char*)d_ws;
  (void)hipFuncSetAttribute((const void*)mega_kernel, hipFuncAttributeMaxDynamicSharedMemorySize, LDS_TOTAL);
  mega_kernel<<<256, 768, LDS_TOTAL, stream>>>(P);
}
```

```cpp
#include <hip/hip_runtime.h>
#include <hip/hip_bf16.h>
#include <stdint.h>

typedef __attribute__((ext_vector_type(8))) short bf16x8;
typedef __attribute__((ext_vector_type(4))) short bf16x4;
typedef __attribute__((ext_vector_type(4))) float f32x4;
typedef __attribute__((ext_vector_type(2))) float f32x2;
typedef __attribute__((ext_vector_type(2))) __bf16 bf2;
typedef __attribute__((ext_vector_type(4))) unsigned u32x4;
typedef __attribute__((ext_vector_type(2))) unsigned u32x2;
typedef unsigned short u16;
typedef unsigned long long u64;
#define LAS __attribute__((address_space(3)))
#define GAS __attribute__((address_space(1)))
#define DI __device__ __forceinline__
#define RLX_AGENT __ATOMIC_RELAXED, __HIP_MEMORY_SCOPE_AGENT

constexpr int S_ = 2048, D_ = 640, NQKV = 1920, FIN = 1536, FH = 768;

constexpr unsigned WS_PQ  = 0;
constexpr unsigned WS_Z1  = WS_PQ + 16 * 8 * 8 * 3 * 1024;
constexpr unsigned WS_Z2N = WS_Z1 + 16 * 8 * 24 * 1024;
constexpr unsigned WS_OH  = WS_Z2N + 8 * 64 * 6 * 1024;
constexpr unsigned WS_WF  = WS_OH + 32 * 44 * 4 * 1024;
constexpr unsigned WS_KVT = WS_WF + 8 * 44 * 5 * 1024;
constexpr unsigned WS_KVP = WS_KVT + 8 * 18 * 1024;
constexpr unsigned WS_GR  = WS_KVP + 8 * 18 * 16 * 1024;
constexpr unsigned WS_GX  = WS_GR + 16 * 8 * 256 * 8;
constexpr unsigned WS_GC  = WS_GX + 2048 * 16;
constexpr unsigned WS_F1  = WS_GC + 24 * 10 * 2048;
constexpr unsigned WS_F2  = WS_F1 + 16 * 24 * 64;
constexpr unsigned WS_FO  = WS_F2 + 1024 * 64;
constexpr unsigned WS_FK  = WS_FO + 256 * 64;
constexpr unsigned WS_EP  = WS_FK + 256 * 64;
constexpr unsigned WS_END = WS_EP + 64;

#define STAMP(i)

DI float bf2f(u16 v) { return __uint_as_float(((unsigned)v) << 16); }
DI unsigned cvt_pk(float lo, float hi) {
  bf2 r; r[0] = (__bf16)lo; r[1] = (__bf16)hi; return __builtin_bit_cast(unsigned, r);
}
DI float rbf(float x) { return (float)((__bf16)x); }
DI float lo16(unsigned u) { return __uint_as_float(u << 16); }
DI float hi16(unsigned u) { return __uint_as_float(u & 0xffff0000u); }

DI void glds16(const void* gbase, unsigned voff, unsigned lds_dst) {
  unsigned keep;
  asm volatile("s_mov_b32 %0, m0\n\ts_mov_b32 m0, %3\n\ts_nop 0\n\tglobal_load_lds_dwordx4 %1, %2\n\ts_mov_b32 m0, %0"
               : "=&s"(keep) : "v"(voff), "s"(gbase), "s"(lds_dst) : "memory");
}
DI void glds16c(const void* gbase, unsigned voff, unsigned lds_dst) {
  unsigned keep;
  asm volatile("s_mov_b32 %0, m0\n\ts_mov_b32 m0, %3\n\ts_nop 0\n\tglobal_load_lds_dwordx4 %1, %2 sc1\n\ts_mov_b32 m0, %0"
               : "=&s"(keep) : "v"(voff), "s"(gbase), "s"(lds_dst) : "memory");
}
#define GL_PRE "s_mov_b32 %0, m0\n\ts_mov_b32 m0, %2\n\ts_nop 0\n\t"
#define GL_POST "s_mov_b32 m0, %0"
DI void gl2(const void* gb, unsigned lds, unsigned v0, unsigned v1) {
  unsigned keep;
  asm volatile(GL_PRE "global_load_lds_dwordx4 %3, %1\n\tglobal_load_lds_dwordx4 %4, %1 offset:1024\n\t" GL_POST
               : "=&s"(keep) : "s"(gb), "s"(lds), "v"(v0), "v"(v1) : "memory");
}
DI void gl3(const void* gb, unsigned lds, unsigned v0, unsigned v1, unsigned v2) {
  unsigned keep;
  asm volatile(GL_PRE "global_load_lds_dwordx4 %3, %1\n\tglobal_load_lds_dwordx4 %4, %1 offset:1024\n\tglobal_load_lds_dwordx4 %5, %1 offset:2048\n\t" GL_POST
               : "=&s"(keep) : "s"(gb), "s"(lds), "v"(v0), "v"(v1), "v"(v2) : "memory");
}
DI void gl4(const void* gb, unsigned lds, unsigned v0, unsigned v1, unsigned v2, unsigned v3) {
  unsigned keep;
  asm volatile(GL_PRE "global_load_lds_dwordx4 %3, %1\n\tglobal_load_lds_dwordx4 %4, %1 offset:1024\n\tglobal_load_lds_dwordx4 %5, %1 offset:2048\n\tglobal_load_lds_dwordx4 %6, %1 offset:3072\n\t" GL_POST
               : "=&s"(keep) : "s"(gb), "s"(lds), "v"(v0), "v"(v1), "v"(v2), "v"(v3) : "memory");
}
DI void gl3c(const void* gb, unsigned lds, unsigned v0) {
  unsigned keep;
  asm volatile(GL_PRE "global_load_lds_dwordx4 %3, %1 sc1\n\tglobal_load_lds_dwordx4 %3, %1 offset:1024 sc1\n\tglobal_load_lds_dwordx4 %3, %1 offset:2048 sc1\n\t" GL_POST
               : "=&s"(keep) : "s"(gb), "s"(lds), "v"(v0) : "memory");
}
DI void gl4c(const void* gb, unsigned lds, unsigned v0) {
  unsigned keep;
  asm volatile(GL_PRE "global_load_lds_dwordx4 %3, %1 sc1\n\tglobal_load_lds_dwordx4 %3, %1 offset:1024 sc1\n\tglobal_load_lds_dwordx4 %3, %1 offset:2048 sc1\n\tglobal_load_lds_dwordx4 %3, %1 offset:3072 sc1\n\t" GL_POST
               : "=&s"(keep) : "s"(gb), "s"(lds), "v"(v0) : "memory");
}
template <int N> DI void wait_vm() { asm volatile("s_waitcnt vmcnt(%0)" :: "n"(N) : "memory"); }

DI bf16x8 lds_b128(unsigned addr) { return *(LAS bf16x8*)(uintptr_t)addr; }
DI bf16x8 lds_2b64(unsigned addr0, unsigned addr1) {
  bf16x4 a = *(LAS bf16x4*)(uintptr_t)addr0, b = *(LAS bf16x4*)(uintptr_t)addr1;
  return __builtin_shufflevector(a, b, 0, 1, 2, 3, 4, 5, 6, 7);
}
DI bf16x8 lds_tr(unsigned addr0, unsigned addr1) {
  bf16x4 a = __builtin_amdgcn_ds_read_tr16_b64_v4i16((LAS bf16x4*)(uintptr_t)addr0);
  bf16x4 b = __builtin_amdgcn_ds_read_tr16_b64_v4i16((LAS bf16x4*)(uintptr_t)addr1);
  return __builtin_shufflevector(a, b, 0, 1, 2, 3, 4, 5, 6, 7);
}
DI float ldsf(unsigned a) { return *(LAS float*)(uintptr_t)a; }
DI void stsf(unsigned a, float v) { *(LAS float*)(uintptr_t)a = v; }
DI f32x4 ldsf4(unsigned a) { return *(LAS f32x4*)(uintptr_t)a; }
DI void stsf4(unsigned a, f32x4 v) { *(LAS f32x4*)(uintptr_t)a = v; }
#define SB0 __builtin_amdgcn_sched_barrier(0)
#define MFMA(a, b, c) __builtin_amdgcn_mfma_f32_16x16x32_bf16((a), (b), (c), 0, 0, 0)

template <int CTRL> DI float dpp(float v) {
  return __builtin_bit_cast(float, __builtin_amdgcn_update_dpp(0, __builtin_bit_cast(int, v), CTRL, 0xf, 0xf, true));
}
DI float sum8(float v) { v += dpp<0xB1>(v); v += dpp<0x4E>(v); v += dpp<0x141>(v); return v; }
DI float sum16(float v) { v = sum8(v); v += dpp<0x140>(v); return v; }

DI float gelu_t(float x) {
  float u = x * (0.7978845608f + 0.0356774081f * x * x);
  float e = __builtin_amdgcn_exp2f(-2.885390082f * u);
  return x * __builtin_amdgcn_rcpf(1.0f + e);
}
DI float phi_f(float t) {
  return (fmaxf(t, 0.f) + 1e-6f) + __builtin_amdgcn_exp2f(fminf(t, 0.f) * 1.4426950409f);
}

typedef __amdgpu_buffer_rsrc_t rsrc_t;
DI void st16(rsrc_t r, unsigned off, u32x4 v) { __builtin_amdgcn_raw_buffer_store_b128(v, r, off, 0, 16); }
DI u32x4 ld16(rsrc_t r, unsigned off) { return __builtin_amdgcn_raw_buffer_load_b128(r, off, 0, 16); }
DI void st8(char* ws, unsigned off, u32x2 v) { __hip_atomic_store((GAS u64*)(ws + off), ((u64)v[1] << 32) | v[0], RLX_AGENT); }
DI void st8u(char* ws, unsigned off, u64 v) { __hip_atomic_store((GAS u64*)(ws + off), v, RLX_AGENT); }
DI void st4(char* ws, unsigned off, float v) { __hip_atomic_store((GAS unsigned*)(ws + off), __float_as_uint(v), RLX_AGENT); }
DI u64 ld8(const char* ws, unsigned off) { return __hip_atomic_load((GAS u64*)(ws + off), RLX_AGENT); }
constexpr unsigned SPIN_CAP = 1u << 20;
DI void wait_words(const char* ws, unsigned off, bool act, u64 tag) {
#pragma unroll 1
  for (unsigned sp = 0; sp < SPIN_CAP; ++sp) {
    u64 v = act ? ld8(ws, off) : tag;
    if (__all(v == tag)) break;
    __builtin_amdgcn_s_sleep(1);
  }
  __builtin_amdgcn_fence(__ATOMIC_ACQUIRE, "wavefront");
}

struct Params { const u16 *x, *wqkv, *wo, *win, *wsp, *bsp, *wout; u16* out; char* ws; };

constexpr unsigned KBS = 4224;
constexpr unsigned K1_STAGE = 16384 + 8 * KBS;
constexpr unsigned L_MISC = 153600;
constexpr int LDS_TOTAL = 163840;
constexpr unsigned P2_SLOT = 20480;
constexpr unsigned L_KVT = 135168;
constexpr unsigned P3_SLOT = 36864;

DI void bar() { asm volatile("s_waitcnt lgkmcnt(0)" ::: "memory"); __builtin_amdgcn_s_barrier(); asm volatile("" ::: "memory"); }

struct Side { u32x4 r0, r1; unsigned wv[8]; };
DI void side_issue(const Params& P, Side& S, int row, int w) {
  const int l = threadIdx.x & 63;
  const u32x4* src = (const u32x4*)(P.x + (size_t)row * D_);
  S.r0 = src[l]; S.r1 = src[64 + (l & 15)];
  int item = blockIdx.x; if (item > 219) item = 219;
  const int st = item / 5, T = (item - st * 5) * 8 + w;
  const u16* wsrc = (st < 20 ? P.wo + (size_t)st * 32 * D_ : P.wout + (size_t)(st - 20) * 32 * D_) + (size_t)(l >> 4) * 4 * D_ + T * 16 + (l & 15);
#pragma unroll
  for (int e = 0; e < 8; ++e) S.wv[e] = wsrc[(size_t)(16 * (e >> 2) + (e & 3)) * D_];
}
DI float rdl(float v, int lane) { return __builtin_bit_cast(float, __builtin_amdgcn_readlane(__builtin_bit_cast(int, v), lane)); }
DI void side_finish(const Params& P, rsrc_t rws, const Side& S, int row, int w, unsigned tl, unsigned th) {
  const int l = threadIdx.x & 63;
  float s = 0.f, q = 0.f, s2 = 0.f, q2 = 0.f;
#pragma unroll
  for (int j = 0; j < 4; ++j) {
    float a = lo16(S.r0[j]), b = hi16(S.r0[j]), c = lo16(S.r1[j]), d = hi16(S.r1[j]);
    s += a + b; q += a * a + b * b; s2 += c + d; q2 += c * c + d * d;
  }
  s = sum16(s); q = sum16(q); s2 = sum16(s2); q2 = sum16(q2);
  s = (rdl(s, 0) + rdl(s, 16)) + (rdl(s, 32) + rdl(s, 48)) + s2;
  q = (rdl(q, 0) + rdl(q, 16)) + (rdl(q, 32) + rdl(q, 48)) + q2;
  const float m = s * (1.f / D_);
  const float v = rbf(fmaxf(q * (1.f / D_) - m * m, 0.f));
  const float r = 1.f / rbf(sqrtf(rbf(v + 1e-5f)));
  if (l < 2) st8u(P.ws, WS_GX + row * 16 + l * 8, l == 0 ? (((u64)tl << 32) | __float_as_uint(-rbf(m) * r)) : (((u64)th << 32) | __float_as_uint(r)));
  int item = blockIdx.x; if (item > 219) item = 219;
  const int st = item / 5, T = (item - st * 5) * 8 + w, cb = T / 5, nt = T - cb * 5;
  st16(rws, WS_WF + (unsigned)(((cb * 44 + st) * 5 + nt) * 1024) + l * 16,
       (u32x4){S.wv[0] | (S.wv[1] << 16), S.wv[2] | (S.wv[3] << 16), S.wv[4] | (S.wv[5] << 16), S.wv[6] | (S.wv[7] << 16)});
}
DI void xstat_gather(const char* ws, int panel, unsigned L_ST, int tid, unsigned tg, u64 gv) {
  if (tid < 256) {
#pragma unroll 1
    for (unsigned sp = 0; sp < SPIN_CAP; ++sp) {
      if (__all((unsigned)(gv >> 32) == tg)) break;
      __builtin_amdgcn_s_sleep(1);
      gv = ld8(ws, WS_GX + panel * 2048 + tid * 8);
    }
    stsf(L_ST + (tid & 1) * 512 + (tid >> 1) * 4, __uint_as_float((unsigned)gv));
  }
}

DI void csum_issue(const char* ws, int blk, int t, u64 (&cv)[10]) {
#pragma unroll
  for (int k = 0; k < 10; ++k) cv[k] = ld8(ws, WS_GC + (blk * 10 + k) * 2048 + t * 8);
}
DI void csum_gather(const char* ws, int blk, int t, unsigned tl, u64 (&cv)[10], unsigned dst) {
#pragma unroll 1
  for (unsigned sp = 0; sp < SPIN_CAP; ++sp) {
    bool ok = true;
#pragma unroll
    for (int k = 0; k < 10; ++k) ok &= (unsigned)(cv[k] >> 32) == tl;
    if (__all(ok)) break;
    __builtin_amdgcn_s_sleep(1);
    csum_issue(ws, blk, t, cv);
  }
  float a = 0.f;
#pragma unroll
  for (int k = 0; k < 10; ++k) a += __uint_as_float((unsigned)cv[k]);
  stsf(dst + t * 4, a);
}
DI void csum_store(char* ws, unsigned off, f32x4 d, int fr, int g, unsigned tl) {
  const float v = fr == 0 ? d[0] : fr == 1 ? d[1] : fr == 2 ? d[2] : d[3];
  if (fr < 4) st8u(ws, off + (4 * g + fr) * 8, ((u64)tl << 32) | __float_as_uint(v));
}

DI void p1q_consumer(const Params& P, rsrc_t rws, int w, int panel, int cbi, unsigned lds0, u64 tag, bf16x8 (&pf)[3]) {
  char* ws = P.ws;
  const int tid = threadIdx.x, l = tid & 63;
  const int wm = w >> 2, wc = (w + 2 * wm) & 3;
  const int g = l >> 4, fr = l & 15, q = (l & 15) >> 2, p = l & 3, g1 = g & 1;
  const unsigned aoff0 = (unsigned)((wm * 64 + fr) * 128 + ((g ^ (l & 7)) << 4));
  const unsigned ttab = (unsigned)(0x4498E765DC32BA10ull >> (16 * wc)) & 0xffffu;
  int tile[4];
  unsigned bo[4];
#pragma unroll
  for (int n = 0; n < 4; ++n) {
    tile[n] = (ttab >> (4 * n)) & 15;
    bo[n] = 16384u + g * KBS + q * 480 + p * 8 + tile[n] * 32;
  }
  const unsigned tl = (unsigned)tag, th = tl * 0x9E3779B1u ^ (unsigned)(tag >> 32);
  {
    Side S; side_issue(P, S, panel * 128 + cbi * 8 + w, w);
    side_finish(P, rws, S, panel * 128 + cbi * 8 + w, w, tl, th);
  }
  f32x4 acc[4][4];
#pragma unroll
  for (int m = 0; m < 4; ++m)
#pragma unroll
    for (int n = 0; n < 4; ++n) acc[m][n] = (f32x4){0.f, 0.f, 0.f, 0.f};
  const short one = 0x3F80;
  const bf16x8 ones = {one, one, one, one, one, one, one, one};
  const bf16x8 zeros = {0, 0, 0, 0, 0, 0, 0, 0};

  bf16x8 af[2][4], bfr[2][4];
#pragma unroll
  for (int m = 0; m < 4; ++m) { af[1][m] = zeros; bfr[1][m] = zeros; }
  auto mm = [&](int ks) {
#pragma unroll
    for (int n = 0; n < 3; ++n)
#pragma unroll
      for (int m = 0; m < 4; ++m) acc[m][n] = MFMA(bfr[ks][n], af[ks][m], acc[m][n]);
    if (wc < 3) {
#pragma unroll
      for (int m = 0; m < 4; ++m) acc[m][3] = MFMA(bfr[ks][3], af[ks][m], acc[m][3]);
    }
  };
  auto step = [&](unsigned sb, int ks) {
#pragma unroll
    for (int n = 0; n < 4; ++n) bfr[ks][n] = lds_tr(sb + ks * 4 * KBS + bo[n], sb + ks * 4 * KBS + bo[n] + 1920);
#pragma unroll
    for (int m = 0; m < 4; ++m) af[ks][m] = lds_b128(sb + ((aoff0 ^ (ks * 64)) + m * 2048));
    SB0;
    mm(ks ^ 1);
    SB0;
  };
  unsigned sb = lds0;
  auto run = [&](int n) {
#pragma unroll 1
    for (int kt = 0; kt < n; ++kt) {
      bar();
      step(sb, 0);
      step(sb, 1);
      sb += K1_STAGE; if (sb == lds0 + 3 * K1_STAGE) sb = lds0;
    }
  };
  run(1);
  if (panel < 10) {
#pragma unroll
    for (int h = 0; h < 2; ++h) {
      const bf16x8 b0 = wm == 0 ? bfr[0][h] : bfr[0][2 + h], b1 = wm == 0 ? bfr[1][h] : bfr[1][2 + h];
      const int tt = wm == 0 ? tile[h] : tile[2 + h];
      f32x4 d = MFMA(b0, ones, ((f32x4){0.f, 0.f, 0.f, 0.f}));
      d = MFMA(b1, ones, d);
      csum_store(ws, WS_GC + (cbi * 10 + panel) * 2048 + tt * 128, d, fr, g, tl);
    }
  }
  run(9);
  mm(1);
  STAMP(1);
  bar();
  STAMP(10);
  const unsigned L_ST = lds0 + L_MISC, L_CS = lds0 + L_MISC + 5120;
  const unsigned L_K = lds0, L_V = L_K + 20480, L_Q = lds0 + 122880;
  float mr[4], rinv[4];
#pragma unroll
  for (int m = 0; m < 4; ++m) { const int rl = wm * 64 + m * 16 + fr; mr[m] = ldsf(L_ST + rl * 4); rinv[m] = ldsf(L_ST + 512 + rl * 4); }
#pragma unroll
  for (int n = 0; n < 4; ++n) {
    if (n < 3 || wc < 3) {
      const int t = tile[n];
      const f32x4 cs = ldsf4(L_CS + (t * 16 + 4 * g) * 4);
      unsigned dst, dstep;
      if (t < 5) { dst = L_Q + (wm * 12 + (t >> 1)) * 1024 + l * 16 + (t & 1) * 8; dstep = 3072; }
      else {
        const int kb = wm * 8 + (fr >> 3), pos = (fr & 7) ^ ((fr >> 3) << 2);
        dst = (t < 10 ? L_K + (t - 5) * 256 : L_V + (t - 10) * 256) + kb * 1280 + pos * 32 + g * 8; dstep = 2560;
      }
      if (t < 10) {
#pragma unroll
        for (int m = 0; m < 4; ++m) {
          f32x4 v = acc[m][n] * rinv[m] + cs * mr[m];
#pragma unroll
          for (int j = 0; j < 4; ++j) v[j] = phi_f(v[j]);
          *(LAS u32x2*)(uintptr_t)(dst + m * dstep) = (u32x2){cvt_pk(v[0], v[1]), cvt_pk(v[2], v[3])};
        }
      } else {
#pragma unroll
        for (int m = 0; m < 4; ++m) {
          f32x4 v = acc[m][n] * rinv[m] + cs * mr[m];
          *(LAS u32x2*)(uintptr_t)(dst + m * dstep) = (u32x2){cvt_pk(v[0], v[1]), cvt_pk(v[2], v[3])};
        }
      }
    }
  }
  STAMP(8);
  bar();
  {
    const unsigned troff = (unsigned)(g * 5 * 256 + ((4 * g1 + q) * 32) + p * 8);
    f32x4 d[3];
    int jb[3];
#pragma unroll
    for (int r = 0; r < 3; ++r) {
      int job = w + 8 * r; if (job >= 18) job = w;
      jb[r] = job;
      const int i = job < 13 ? (job + 1) / 3 : job - 13;
      const int j = job < 13 ? job - 2 * i : 0;
      d[r] = (f32x4){0.f, 0.f, 0.f, 0.f};
#pragma unroll
      for (int ks = 0; ks < 4; ++ks) {
        const unsigned a0 = L_K + troff + (ks * 20 + i) * 256, b0 = L_V + troff + (ks * 20 + j) * 256;
        const bf16x8 ka = lds_tr(a0, a0 ^ 128);
        bf16x8 vb = lds_tr(b0, b0 ^ 128);
        if (job >= 13) vb = ones;
        d[r] = MFMA(ka, vb, d[r]);
      }
    }
#pragma unroll
    for (int r = 0; r < 3; ++r)
      st16(rws, WS_KVP + (unsigned)(((cbi * 18 + jb[r]) * 16 + panel) * 1024) + l * 16, __builtin_bit_cast(u32x4, d[r]));
    pf[0] = lds_b128(L_Q + w * 3072 + l * 16); pf[1] = lds_b128(L_Q + w * 3072 + 1024 + l * 16);
    const u32x2 q2 = *(LAS u32x2*)(uintptr_t)(L_Q + w * 3072 + 2048 + l * 16);
    pf[2] = __builtin_bit_cast(bf16x8, ((u32x4){q2[0], q2[1], 0u, 0u}));
  }
  STAMP(9);
  wait_vm<0>();
  bar();
  if (tid == 0) st8u(ws, WS_F1 + (panel * 24 + cbi) * 64, tag);
}

DI void p1z_consumer(const Params& P, rsrc_t rws, int w, int panel, int cbj, unsigned lds0, u64 tag) {
  char* ws = P.ws;
  const int tid = threadIdx.x, l = tid & 63;
  const int wm = w & 3, wn = w >> 2;
  const int g = l >> 4, fr = l & 15, q = (l & 15) >> 2, p = l & 3, g1 = g & 1;
  const unsigned aoff0 = (unsigned)((wm * 32 + fr) * 128 + ((g ^ (l & 7)) << 4));
  unsigned bo[3];
  int ca[3];
#pragma unroll
  for (int n = 0; n < 3; ++n) {
    const int c = wn * 3 + n;
    ca[n] = c;
    int pc = c + g1; if (pc == 6) pc = 0;
    bo[n] = 16384u + g * 1536 + q * 192 + pc * 32 + p * 8;
  }
  const short one = 0x3F80;
  const bf16x8 ones = {one, one, one, one, one, one, one, one};
  const bf16x8 zeros = {0, 0, 0, 0, 0, 0, 0, 0};
  f32x4 accA[2][3], accB[2][3];
#pragma unroll
  for (int m = 0; m < 2; ++m)
#pragma unroll
    for (int n = 0; n < 3; ++n) { accA[m][n] = (f32x4){0.f, 0.f, 0.f, 0.f}; accB[m][n] = (f32x4){0.f, 0.f, 0.f, 0.f}; }
  const unsigned tl = (unsigned)tag, th = tl * 0x9E3779B1u ^ (unsigned)(tag >> 32), tg = (tid & 1) ? th : tl;
  bf16x8 af[2][2], bfr[2][3];
  af[1][0] = af[1][1] = zeros; bfr[1][0] = bfr[1][1] = bfr[1][2] = zeros;
  auto rd = [&](unsigned sb, int ks) {
#pragma unroll
    for (int m = 0; m < 2; ++m) af[ks][m] = lds_b128(sb + ((aoff0 ^ (ks * 64)) + m * 2048));
#pragma unroll
    for (int n = 0; n < 3; ++n) { const unsigned o = sb + ks * 4 * 1536 + bo[n]; bfr[ks][n] = lds_tr(o, o + 768); }
    SB0;
  };
  auto mmA = [&](int ks) {
#pragma unroll
    for (int n = 0; n < 3; ++n)
#pragma unroll
      for (int m = 0; m < 2; ++m) accA[m][n] = MFMA(af[ks][m], bfr[ks][n], accA[m][n]);
    SB0;
  };
  auto mmB = [&](int ks) {
#pragma unroll
    for (int n = 0; n < 3; ++n)
#pragma unroll
      for (int m = 0; m < 2; ++m) accB[m][n] = MFMA(bfr[ks][n], af[ks][m], accB[m][n]);
    SB0;
  };
  const unsigned L_ST = lds0 + L_MISC, L_CS = lds0 + L_MISC + 5120;
  const unsigned L_RS = lds0 + L_MISC + 2048;
  const unsigned L_FS = lds0 + L_MISC + 4096;
  unsigned sb = lds0;
  auto nxt = [&]() { sb += K1_STAGE; if (sb == lds0 + 3 * K1_STAGE) sb = lds0; };
  auto runA = [&](int n) {
#pragma unroll 1
    for (int kt = 0; kt < n; ++kt) { bar(); rd(sb, 0); mmA(1); rd(sb, 1); mmA(0); nxt(); }
  };
  auto runB = [&](int n) {
#pragma unroll 1
    for (int kt = 0; kt < n; ++kt) { bar(); rd(sb, 0); mmB(1); rd(sb, 1); mmB(0); nxt(); }
  };
  auto cpub = [&](int blk) {
    if (panel < 10 && wm == 0) {
#pragma unroll
      for (int n = 0; n < 3; ++n) {
        f32x4 d = MFMA(bfr[0][n], ones, ((f32x4){0.f, 0.f, 0.f, 0.f}));
        d = MFMA(bfr[1][n], ones, d);
        csum_store(ws, WS_GC + (blk * 10 + panel) * 2048 + ca[n] * 128, d, fr, g, tl);
      }
    }
  };
  {
    Side S; side_issue(P, S, panel * 128 + 64 + cbj * 8 + w, w);
    side_finish(P, rws, S, panel * 128 + 64 + cbj * 8 + w, w, tl, th);
  }
  runA(1);
  cpub(8 + cbj);
  runA(6);
  const int tc = tid - 256;
  {
    u64 cv[10];
    if (tid < 256) cv[0] = ld8(ws, WS_GX + panel * 2048 + tid * 8);
    else if (tc < 96) csum_issue(ws, 8 + cbj, tc, cv);
    runA(3);
    if (tid < 256) xstat_gather(ws, panel, L_ST, tid, tg, cv[0]);
    else if (tc < 96) csum_gather(ws, 8 + cbj, tc, tl, cv, L_CS);
  }
  bar(); rd(sb, 0); mmA(1); rd(sb, 1);
  cpub(16 + cbj);
  STAMP(8);
  {
    float cn[3];
#pragma unroll
    for (int n = 0; n < 3; ++n) cn[n] = ldsf(L_CS + (ca[n] * 16 + fr) * 4);
#pragma unroll
    for (int m = 0; m < 2; ++m) {
      const int rl = wm * 32 + m * 16 + 4 * g;
      const f32x4 mr = ldsf4(L_ST + rl * 4), rinv = ldsf4(L_ST + 512 + rl * 4);
      f32x4 rs = {0.f, 0.f, 0.f, 0.f}, rq = {0.f, 0.f, 0.f, 0.f};
#pragma unroll
      for (int n = 0; n < 3; ++n) {
        f32x4 v = accA[m][n] * rinv + mr * cn[n];
#pragma unroll
        for (int j = 0; j < 4; ++j) v[j] = gelu_t(v[j]);
        rs += v; rq += v * v;
        accA[m][n] = v;
      }
#pragma unroll
      for (int j = 0; j < 4; ++j) { rs[j] = sum16(rs[j]); rq[j] = sum16(rq[j]); }
      if (fr == 0) {
        stsf4(L_RS + ((wn * 128 + rl) * 2) * 4, (f32x4){rs[0], rq[0], rs[1], rq[1]});
        stsf4(L_RS + ((wn * 128 + rl) * 2 + 4) * 4, (f32x4){rs[2], rq[2], rs[3], rq[3]});
      }
    }
  }
  mmB(0); nxt();
  bar(); rd(sb, 0);
  float part = 0.f;
  const unsigned gbase = WS_GR + (unsigned)(panel * 8) * 2048 + tid * 8;
  if (tid < 256) {
    part = ldsf(L_RS + tid * 4) + ldsf(L_RS + 1024 + tid * 4);
    st8u(ws, gbase + cbj * 2048, ((u64)tg << 32) | __float_as_uint(part));
  }
  mmB(1); rd(sb, 1); mmB(0); nxt();
  STAMP(9);
  runB(1);
  {
    u64 cv[10];
    if (tid < 256) {
#pragma unroll
      for (int i = 1; i < 8; ++i) cv[i] = ld8(ws, gbase + ((cbj + i) & 7) * 2048);
    }
    runB(1);
    if (tid < 256) {
      float tot;
#pragma unroll 1
      for (unsigned sp = 0; sp < SPIN_CAP; ++sp) {
        bool ok = true; tot = part;
#pragma unroll
        for (int i = 1; i < 8; ++i) { ok &= (unsigned)(cv[i] >> 32) == tg; tot += __uint_as_float((unsigned)cv[i]); }
        if (__all(ok)) break;
#pragma unroll
        for (int i = 1; i < 8; ++i) cv[i] = ld8(ws, gbase + ((cbj + i) & 7) * 2048);
      }
      float other = dpp<0xB1>(tot);
      if ((tid & 1) == 0) {
        float m = tot * (1.f / FH);
        float v = rbf(fmaxf(other * (1.f / FH) - m * m, 0.f));
        float r = 1.f / rbf(sqrtf(rbf(v + 1e-5f)));
        stsf(L_FS + (tid >> 1) * 4, -rbf(m) * r);
        stsf(L_FS + 512 + (tid >> 1) * 4, r);
      }
    }
  }
  bar(); rd(sb, 0);
  {
    f32x4 mr[2], ri[2];
#pragma unroll
    for (int m = 0; m < 2; ++m) { const int rl = wm * 32 + m * 16 + 4 * g; mr[m] = ldsf4(L_FS + rl * 4); ri[m] = ldsf4(L_FS + 512 + rl * 4); }
#pragma unroll
    for (int n = 0; n < 3; ++n) {
      f32x4 a = accA[0][n] * ri[0] + mr[0], b = accA[1][n] * ri[1] + mr[1];
      st16(rws, WS_Z2N + (unsigned)(((cbj * 64 + panel * 4 + wm) * 6 + ca[n]) * 1024) + l * 16,
           (u32x4){cvt_pk(a[0], a[1]), cvt_pk(a[2], a[3]), cvt_pk(b[0], b[1]), cvt_pk(b[2], b[3])});
    }
  }
  mmB(1); rd(sb, 1); mmB(0); nxt();
  STAMP(10);
  runB(2);
  wait_vm<0>();
  bar();
  if (tid == 0) st8u(ws, WS_F1 + (panel * 24 + 8 + cbj) * 64, tag);
  STAMP(11);
  rd(sb, 0); mmB(1); rd(sb, 1); mmB(0); nxt();
  {
    u64 cv[10];
    if (tid >= 256 && tc < 96) csum_issue(ws, 16 + cbj, tc, cv);
    runB(2);
    if (tid >= 256 && tc < 96) csum_gather(ws, 16 + cbj, tc, tl, cv, L_CS + 512);
  }
  mmB(1);
  STAMP(1);
  bar();
  {
    f32x4 cn[3];
#pragma unroll
    for (int n = 0; n < 3; ++n) { const unsigned a = L_CS + 512 + (ca[n] * 16 + 4 * g) * 4; cn[n] = ldsf4(a); }
    float mr[2], rinv[2];
#pragma unroll
    for (int m = 0; m < 2; ++m) { const int rl = wm * 32 + m * 16 + fr; mr[m] = ldsf(L_ST + rl * 4); rinv[m] = ldsf(L_ST + 512 + rl * 4); }
#pragma unroll
    for (int n = 0; n < 3; ++n) {
      u32x4 o;
#pragma unroll
      for (int m = 0; m < 2; ++m) {
        f32x4 v = accB[m][n] * rinv[m] + cn[n] * mr[m];
#pragma unroll
        for (int j = 0; j < 4; ++j) v[j] = gelu_t(v[j]);
        o[2 * m] = cvt_pk(v[0], v[1]); o[2 * m + 1] = cvt_pk(v[2], v[3]);
      }
      if (wm < 2) *(LAS u32x4*)(uintptr_t)(lds0 + L_KVT + 6144 + (wm * 6 + ca[n]) * 1024 + l * 16) = o;
      else st16(rws, WS_Z1 + (unsigned)(((panel * 8 + cbj) * 4 + wm) * 6 + ca[n]) * 1024 + l * 16, o);
    }
  }
}

__global__ __launch_bounds__(768, 3) void mega_kernel(Params P) {
  extern __shared__ __attribute__((aligned(16))) char smem[];
  const unsigned lds0 = (unsigned)(uintptr_t)(LAS char*)smem;
  char* ws = P.ws;
  const int tid = threadIdx.x, l = tid & 63;
  const int w = __builtin_amdgcn_readfirstlane(tid >> 6);
  const int g = l >> 4, fr = l & 15;
  const int b = blockIdx.x, xcd = b & 7, bi = b >> 3;
  const int panel1 = (xcd >> 1) * 4 + (bi & 3), cb1 = (xcd & 1) * 4 + ((bi >> 2) & 3);
  const bool isq = bi < 16;
  const int panel2 = 2 * panel1 + (isq ? 1 : 0), cb2 = cb1;
  const int panel3 = xcd * 4 + (bi & 3), cb3 = bi >> 2;
  STAMP(0);
  const u64 tag = ld8(ws, WS_EP) + 1;
  if (w == 7 && bi < 20) {
    u64 pc; asm volatile("s_getpc_b64 %0" : "=s"(pc));
    u32x4 d0, d1;
    asm volatile("global_load_dwordx4 %0, %2, %3\n\tglobal_load_dwordx4 %1, %2, %3 offset:1024\n\ts_waitcnt vmcnt(0)"
                 : "=&v"(d0), "=&v"(d1) : "v"(bi * 2048 + l * 16), "s"(pc & ~127ull) : "memory");
  }

  if (w >= 8) {
    const int lw = w - 8;
    const unsigned vA0 = (unsigned)(((l >> 3) * D_ + (((l & 7) ^ (l >> 3)) << 3)) * 2);
    const unsigned vA1 = vA0 + 8 * D_ * 2 - 1024, vA2 = vA0 + 16 * D_ * 2 - 2048, vA3 = vA0 + 24 * D_ * 2 - 3072;
    const char* gA = (const char*)(P.x + (size_t)(panel1 * 128 + lw * 32) * D_);
    const unsigned v20 = (unsigned)(((l >> 3) * S_ + (((l & 7) ^ (l >> 4)) << 3)) * 2);
    const unsigned v21 = (unsigned)((((l >> 3) + 8) * S_ + (((l & 7) ^ ((l >> 4) | 4)) << 3)) * 2) - 1024;
    const char* gA2 = (const char*)(P.wsp + (size_t)(panel2 * 64 + lw * 16) * S_);
    const int rot2 = 2 * ((2 * (panel2 & 7) + 4 * (cb2 & 3)) & 15);
    auto ldA = [&](int kt, unsigned sl) { gl2(gA2 + ((kt + rot2) & 31) * 128, sl + lw * 2048, v20, v21); };
    auto sl2 = [&](int kt) { return lds0 + ((kt + 4) % 6) * P2_SLOT; };
    const unsigned tl = (unsigned)tag, th = tl * 0x9E3779B1u ^ (unsigned)(tag >> 32);
    const int lt = tid - 512;
    auto gather = [&](int blk, int ncol, unsigned dst, bool xs) {
      u64 cv[10]; u64 gv = 0;
      const int tc = lt < ncol ? lt : 0;
      if (xs) gv = ld8(ws, WS_GX + panel1 * 2048 + lt * 8);
      csum_issue(ws, blk, tc, cv);
      if (xs) xstat_gather(ws, panel1, lds0 + L_MISC, lt, (lt & 1) ? th : tl, gv);
      csum_gather(ws, blk, tc, tl, cv, dst);
    };
    const unsigned l16 = l * 16;
    const char* gB2 = ws + WS_Z2N + (size_t)cb2 * 64 * 6144 + lw * 3072;
    auto ldB = [&](int kt, unsigned sl) { gl3c(gB2 + ((kt + rot2) & 31) * 12288, sl + 8192 + lw * 3072, l16); };
    const unsigned zfl = WS_F1 + ((l & 15) * 24 + 8 + cb2) * 64;
    auto try_z = [&]() {
      const u64 v = l < 16 ? ld8(ws, zfl) : tag;
      return (bool)__builtin_amdgcn_readfirstlane((int)__all(v == tag));
    };
    bool early = false;
    const int rot = panel1 < 10 ? panel1 : 0;
    auto krot = [&](int kt) { const int k = kt >= 10 ? kt - 10 : kt; return __builtin_amdgcn_readfirstlane(k == 0 ? rot : (k - 1 < rot ? k - 1 : k)); };
    if (isq) {
      gl4(gA + rot * 128, lds0 + lw * 4096, vA0, vA1, vA2, vA3);
      unsigned vB[4];
#pragma unroll
      for (int i = 0; i < 4; ++i) {
        int c = i * 64 + l; if (c > 239) c = 239;
        const int row = c / 30, cc = c - row * 30, seg = cc / 10, ch = cc - seg * 10;
        vB[i] = (unsigned)((row * NQKV + seg * 640 + cb1 * 80 + ch * 8) * 2) + 4096 - i * 1024;
      }
      const char* gB = (const char*)P.wqkv + (size_t)lw * 16 * NQKV * 2 - 4096;
      auto stageB = [&](int kt, unsigned sb) {
        const char* bp = gB + (size_t)krot(kt) * 64 * NQKV * 2;
        gl4(bp, sb + 16384 + lw * 2 * KBS, vB[0], vB[1], vB[2], vB[3]);
        gl4(bp + 8 * NQKV * 2, sb + 16384 + (lw * 2 + 1) * KBS, vB[0], vB[1], vB[2], vB[3]);
      };
      auto stage = [&](int kt, unsigned sb) { gl4(gA + krot(kt) * 128, sb + lw * 4096, vA0, vA1, vA2, vA3); stageB(kt, sb); };
      stageB(0, lds0); stage(1, lds0 + K1_STAGE);
      wait_vm<12>(); bar();
      stage(2, lds0 + 2 * K1_STAGE);
      unsigned sb = lds0, sc = lds0 + K1_STAGE;
#pragma unroll 1
      for (int kt = 1; kt < 8; ++kt) {
        wait_vm<12>(); bar();
        if (kt == 2) STAMP(10);
        if (kt == 7) STAMP(11);
        stage(kt + 2, sb);
        sb = sc; sc += K1_STAGE; if (sc == lds0 + 3 * K1_STAGE) sc = lds0;
      }
      wait_vm<12>(); bar();
      gather(cb1, 240, lds0 + L_MISC + 5120, true);
      bar();
      ldA(0, sl2(0)); ldA(1, sl2(1)); ldA(5, sl2(5));
      bar();
      ldA(4, sl2(4));
#pragma unroll 1
      for (int t = 0; t < 3 && !early; ++t) early = try_z();
      if (early) { ldB(0, sl2(0)); ldB(1, sl2(1)); ldB(4, sl2(4)); ldB(5, sl2(5)); }
      bar();
      if (!early) { early = try_z(); if (early) { ldB(0, sl2(0)); ldB(1, sl2(1)); ldB(4, sl2(4)); ldB(5, sl2(5)); } }
      bar();
    } else {
      gl4(gA + rot * 128, lds0 + lw * 4096, vA0, vA1, vA2, vA3);
      unsigned vB[3];
#pragma unroll
      for (int i = 0; i < 3; ++i) {
        const int c = i * 64 + l, row = c / 12, cc = c - row * 12;
        int pc = (cc >> 1) - (row >> 3); if (pc < 0) pc = 5;
        vB[i] = (unsigned)((row * FIN + cb1 * 96 + pc * 16 + (cc & 1) * 8) * 2) + 4096 - i * 1024;
      }
      const char* gB = (const char*)P.win + (size_t)lw * 16 * FIN * 2 - 4096;
      auto stageB = [&](int kt, unsigned sb) {
        const int k = krot(kt);
        gl3(gB + (kt < 10 ? FH * 2 : 0) + (size_t)k * 64 * FIN * 2, sb + 16384 + lw * 3072, vB[0], vB[1], vB[2]);
      };
      auto stage = [&](int kt, unsigned sb) {
        const int k = krot(kt);
        gl4(gA + k * 128, sb + lw * 4096, vA0, vA1, vA2, vA3); stageB(kt, sb);
      };
      stageB(0, lds0); stage(1, lds0 + K1_STAGE);
      wait_vm<7>(); bar();
      stage(2, lds0 + 2 * K1_STAGE);
      unsigned sb = lds0, sc = lds0 + K1_STAGE;
      auto runL = [&](int k0, int k1) {
#pragma unroll 1
        for (int kt = k0; kt < k1; ++kt) {
          wait_vm<7>(); bar();
          stage(kt + 2, sb);
          sb = sc; sc += K1_STAGE; if (sc == lds0 + 3 * K1_STAGE) sc = lds0;
        }
      };
      runL(1, 11);
      {
        const char* wp = (const char*)(P.wsp + (size_t)(panel1 * 128 + lw * 32) * S_) + (cb1 & 3) * 1024;
        const unsigned vo = (unsigned)((l >> 3) * S_ * 2 + (l & 7) * 128);
        unsigned d0, d1, d2, d3;
        asm volatile("global_load_dword %0, %4, %5\n\tglobal_load_dword %1, %4, %6\n\tglobal_load_dword %2, %4, %7\n\tglobal_load_dword %3, %4, %8"
                     : "=&v"(d0), "=&v"(d1), "=&v"(d2), "=&v"(d3)
                     : "v"(vo), "s"(wp), "s"(wp + 8 * S_ * 2), "s"(wp + 16 * S_ * 2), "s"(wp + 24 * S_ * 2) : "memory");
        runL(11, 13);
        asm volatile("" :: "v"(d0), "v"(d1), "v"(d2), "v"(d3));
      }
      runL(13, 18);
      wait_vm<7>(); bar();
      wait_vm<0>(); bar();
      bar();
      wait_words(ws, zfl, l < 16, tag);
      early = true;
      ldA(0, sl2(0)); ldB(0, sl2(0)); ldA(1, sl2(1)); ldB(1, sl2(1));
      ldA(4, sl2(4)); ldA(5, sl2(5)); ldB(4, sl2(4)); ldB(5, sl2(5));
    }
    STAMP(2);
    if (!early) {
      wait_words(ws, zfl, l < 16, tag);
      ldB(0, sl2(0)); ldB(1, sl2(1)); ldB(4, sl2(4)); ldB(5, sl2(5));
    }
    STAMP(3);
    ldA(2, sl2(2)); ldA(3, sl2(3)); ldB(2, sl2(2)); ldB(3, sl2(3));
    wait_vm<16>(); bar();
    {
      unsigned sl = sl2(0);
      auto ldU = [&](int u) {
        ldA(2 * u, sl); ldB(2 * u, sl); ldA(2 * u + 1, sl + P2_SLOT); ldB(2 * u + 1, sl + P2_SLOT);
        sl += 2 * P2_SLOT; if (sl == lds0 + 6 * P2_SLOT) sl = lds0;
      };
      wait_vm<0>(); bar(); ldU(3);
#pragma unroll 1
      for (int u = 2; u < 14; ++u) {
        wait_vm<10>(); bar();
        if (u == 4) STAMP(8);
        if (u == 10) STAMP(9);
        ldU(u + 2);
      }
    }
    wait_vm<10>(); bar();
    wait_words(ws, WS_F1 + (((lw * 64 + l) >> 4) * 24 + ((l & 8) ? 8 : 0) + (l & 15)) * 64, true, tag);
    wait_vm<0>(); bar();
    if (b == 0 && tid == 512) st8u(ws, WS_EP, tag);
    STAMP(4);
    const char* gA3 = ws + WS_OH + (size_t)panel3 * 44 * 4096 + lw * 4096;
    const char* gB3 = ws + WS_WF + (size_t)cb3 * 44 * 5120 + lw * 5120;
    int r5 = cb3 + 2 * (bi & 3); r5 = r5 >= 10 ? r5 - 10 : r5 >= 5 ? r5 - 5 : r5;
    int r6 = cb3 + 2 * (bi & 3); r6 = r6 >= 12 ? r6 - 12 : r6 >= 6 ? r6 - 6 : r6;
    auto ur = [&](int u) {
      int v;
      if (u < 5) { v = u + r5; if (v >= 5) v -= 5; } else { v = u + r6; if (v >= 11) v -= 6; }
      return __builtin_amdgcn_readfirstlane(v);
    };
    auto ldB3 = [&](int u, unsigned sl) {
      gl4c(gB3 + ur(u) * 20480, sl + 16384 + lw * 5120, l16);
      glds16c(gB3 + ur(u) * 20480 + 4096, l16, sl + 16384 + lw * 5120 + 4096);
    };
    auto ldA3 = [&](int u, unsigned sl) { gl4c(gA3 + ur(u) * 16384, sl + lw * 4096, l16); };
    bar(); bar();
    ldB3(0, lds0); ldB3(1, lds0 + P3_SLOT); ldB3(2, lds0 + 2 * P3_SLOT);
    wait_words(ws, WS_FO + ((panel3 >> 1) * 8 + (l & 7)) * 64, l < 8, tag);
    ldA3(0, lds0); ldA3(1, lds0 + P3_SLOT); ldA3(2, lds0 + 2 * P3_SLOT);
    bar();
    ldA3(3, lds0 + 3 * P3_SLOT); ldB3(3, lds0 + 3 * P3_SLOT);
    wait_vm<17>(); bar();
    wait_vm<13>(); bar(); ldA3(4, lds0); ldB3(4, lds0);
    wait_vm<18>(); bar(); ldB3(5, lds0 + P3_SLOT);
    wait_vm<14>(); bar(); ldB3(6, lds0 + 2 * P3_SLOT);
    wait_vm<10>(); bar(); ldB3(7, lds0 + 3 * P3_SLOT);
    wait_words(ws, WS_F2 + (panel3 * 32 + (l & 31)) * 64, l < 32, tag);
    STAMP(5);
    ldA3(5, lds0 + P3_SLOT); ldA3(6, lds0 + 2 * P3_SLOT); ldA3(7, lds0 + 3 * P3_SLOT);
    wait_vm<8>(); bar(); ldA3(8, lds0); ldB3(8, lds0);
    wait_vm<13>(); bar(); ldA3(9, lds0 + P3_SLOT); ldB3(9, lds0 + P3_SLOT);
    wait_vm<18>(); bar(); ldA3(10, lds0 + 2 * P3_SLOT); ldB3(10, lds0 + 2 * P3_SLOT);
    wait_vm<18>(); bar(); wait_vm<9>(); bar(); wait_vm<0>(); bar();
    bar(); bar();
    STAMP(6);
    return;
  }

  const rsrc_t rws = __builtin_amdgcn_make_buffer_rsrc(ws, 0, (int)WS_END, 0x00020000);
  bf16x8 pf[3];
  if (isq) p1q_consumer(P, rws, w, panel1, cb1, lds0, tag, pf);
  else p1z_consumer(P, rws, w, panel1, cb1, lds0, tag);
  STAMP(2);

  {
    const int wt = w & 3, kh = w >> 2;
    const int rsw = ((wt * 16 + fr) >> 1) & 7;
    const unsigned ao0 = (unsigned)((wt * 16 + fr) * 128 + (((4 * kh + (g >> 1)) ^ rsw) << 4) + (g & 1) * 8);
    const unsigned ao1 = (unsigned)((wt * 16 + fr) * 128 + (((4 * kh + 2 + (g >> 1)) ^ rsw) << 4) + (g & 1) * 8);
    const unsigned bo = (unsigned)(8192 + kh * 6144 + l * 16);
    const bf16x8 zeros = {0, 0, 0, 0, 0, 0, 0, 0};
    f32x4 acc[6];
#pragma unroll
    for (int n = 0; n < 6; ++n) acc[n] = (f32x4){0.f, 0.f, 0.f, 0.f};
    bf16x8 af[2], bfr[2][6];
    af[1] = zeros;
#pragma unroll
    for (int n = 0; n < 6; ++n) bfr[1][n] = zeros;
    const int zi = (bi & 15) * 8 + xcd;
    const bool reducer = !isq && zi < 72;
    const int rj = zi & 7, rf = zi >> 3;
    const int row = panel2 * 64 + wt * 16 + fr;
    u32x2 zreg[6];
    const float bs = bf2f(P.bsp[row]);
    unsigned sl = lds0 + 4 * P2_SLOT;
    auto step = [&](int cur, int kt) {
      if (cur == 0) bar();
      af[cur] = lds_2b64(sl + ao0, sl + ao1);
#pragma unroll
      for (int n = 0; n < 6; ++n) bfr[cur][n] = lds_b128(sl + bo + n * 1024);
      SB0;
#pragma unroll
      for (int n = 0; n < 6; ++n) acc[n] = MFMA(bfr[cur ^ 1][n], af[cur ^ 1], acc[n]);
      SB0;
      sl += P2_SLOT; if (sl == lds0 + 6 * P2_SLOT) sl = lds0;
    };
    auto run = [&](int k0, int k1) {
#pragma unroll 1
      for (int kt = k0; kt < k1; kt += 2) { step(0, kt); step(1, kt + 1); }
    };
    if (reducer) {
      const int et = (rf + 1) >> 1, st = rf == 0 ? 0 : ((rf + 1) & 1) + (et >= 3 ? 1 : 0);
      wait_words(ws, WS_F1 + ((l & 15) * 24 + rj) * 64, true, tag);
      f32x4 pv[8];
      bool okv[4];
#pragma unroll
      for (int t4 = 0; t4 < 4; ++t4) {
        const int i = 2 * st + (t4 & 1);
        okv[t4] = i < 5 && (t4 >= 2 || (et - i <= 1 && i - et <= 1));
        const int job = okv[t4] ? (t4 >= 2 ? 13 + i : 2 * i + et) : 0;
#pragma unroll
        for (int pp = 0; pp < 2; ++pp)
          pv[t4 * 2 + pp] = __builtin_bit_cast(f32x4, ld16(rws, WS_KVP + (unsigned)(((rj * 18 + job) * 16 + w * 2 + pp) * 1024) + l * 16));
      }
      run(0, 2);
      if (tid == 0) st8u(ws, WS_F1 + (panel1 * 24 + 16 + cb1) * 64, tag);
      const unsigned L_R = lds0 + 122880;
      f32x4 sv[4];
#pragma unroll
      for (int t4 = 0; t4 < 4; ++t4) { sv[t4] = pv[t4 * 2] + pv[t4 * 2 + 1]; if (!okv[t4]) sv[t4] = (f32x4){0.f, 0.f, 0.f, 0.f}; }
      if (w >= 4) {
#pragma unroll
        for (int t4 = 0; t4 < 4; ++t4) stsf4(L_R + ((w - 4) * 4 + t4) * 1024 + l * 16, sv[t4]);
      }
      run(2, 4);
      if (w < 4) {
#pragma unroll
        for (int t4 = 0; t4 < 4; ++t4) stsf4(L_R + (w * 4 + t4) * 1024 + l * 16, sv[t4] + ldsf4(L_R + (w * 4 + t4) * 1024 + l * 16));
      }
      run(4, 6);
      if (w < 2) {
        f32x4 v[2];
#pragma unroll
        for (int h2 = 0; h2 < 2; ++h2) {
          v[h2] = (f32x4){0.f, 0.f, 0.f, 0.f};
#pragma unroll
          for (int ww = 0; ww < 4; ++ww) v[h2] += ldsf4(L_R + (ww * 4 + w * 2 + h2) * 1024 + l * 16);
          const int he = (et * 16 + fr) / 20;
#pragma unroll
          for (int r = 0; r < 4; ++r) { const int d = (2 * st + h2) * 16 + 4 * g + r; if (d / 20 != he) v[h2][r] = 0.f; }
        }
        st16(rws, WS_KVT + (unsigned)(((rj * 2 + w) * 9 + rf) * 1024) + l * 16,
             (u32x4){cvt_pk(v[0][0], v[0][1]), cvt_pk(v[0][2], v[0][3]), cvt_pk(v[1][0], v[1][1]), cvt_pk(v[1][2], v[1][3])});
      }
      run(6, 10);
      wait_vm<0>();
      if (w < 2 && l == 0) st8u(ws, WS_FK + (rj * 32 + w * 16 + rf) * 64, tag);
      run(10, 18);
    } else {
      run(0, 6);
      wait_vm<0>();
      run(6, 8);
      if (!isq && tid == 0) st8u(ws, WS_F1 + (panel1 * 24 + 16 + cb1) * 64, tag);
      run(8, 18);
    }
    if (isq) {
      const unsigned foff = (l & 15) < 9 ? WS_FK + (cb2 * 32 + (l & 31)) * 64 : WS_F1 + (panel1 * 24 + 16 + cb2) * 64;
      const u64 fv = l < 32 ? ld8(ws, foff) : tag;
      run(18, 22);
      if (!__all(fv == tag)) wait_words(ws, foff, l < 32, tag);
#pragma unroll
      for (int i = 0; i < 3; ++i) {
        int ins = w + 8 * i; if (ins >= 18) ins = 17;
        ins = __builtin_amdgcn_readfirstlane(ins);
        glds16c(ws, WS_KVT + cb2 * 18432 + ins * 1024 + l * 16, __builtin_amdgcn_readfirstlane(lds0 + L_KVT + ins * 1024));
      }
      const unsigned zo = WS_Z1 + (unsigned)(((panel1 * 8 + cb2) * 4 + 2 + (wt >> 1)) * 6) * 1024 + l * 16 + (wt & 1) * 8;
#pragma unroll
      for (int n = 0; n < 6; ++n) zreg[n] = __builtin_amdgcn_raw_buffer_load_b64(rws, zo + n * 1024, 0, 16);
      run(22, 24);
      wait_vm<0>();
      run(24, 26);
      u32x2 ov[5];
#pragma unroll
      for (int et = 0; et < 5; ++et) {
        f32x4 num = {0.f, 0.f, 0.f, 0.f}, den = {0.f, 0.f, 0.f, 0.f};
        const int st0 = et >= 3 ? 1 : 0, nst = et == 0 ? 1 : 2, f0 = et == 0 ? 0 : 1 + (et - 1) * 2;
#pragma unroll
        for (int s = 0; s < nst; ++s) {
          bf16x8 ka = lds_b128(lds0 + L_KVT + (f0 + s) * 1024 + l * 16), za = lds_b128(lds0 + L_KVT + (9 + f0 + s) * 1024 + l * 16);
          num = MFMA(ka, pf[st0 + s], num); den = MFMA(za, pf[st0 + s], den);
        }
        float v[4];
#pragma unroll
        for (int j = 0; j < 4; ++j) v[j] = num[j] * __builtin_amdgcn_rcpf(den[j] + 1e-6f);
        ov[et] = (u32x2){cvt_pk(v[0], v[1]), cvt_pk(v[2], v[3])};
      }
      const unsigned ob = WS_OH + (unsigned)(((2 * panel1 + kh) * 44 + ((cb2 * 5) >> 1)) * 4 + wt) * 1024 + l * 16;
      if ((cb2 & 1) == 0) {
        st16(rws, ob, (u32x4){ov[0][0], ov[0][1], ov[1][0], ov[1][1]});
        st16(rws, ob + 4096, (u32x4){ov[2][0], ov[2][1], ov[3][0], ov[3][1]});
        st8(ws, ob + 8192, ov[4]);
      } else {
        st8(ws, ob + 8, ov[0]);
        st16(rws, ob + 4096, (u32x4){ov[1][0], ov[1][1], ov[2][0], ov[2][1]});
        st16(rws, ob + 8192, (u32x4){ov[3][0], ov[3][1], ov[4][0], ov[4][1]});
      }
      run(26, 30);
      wait_vm<0>();
      run(30, 32);
      if (tid == 0) st8u(ws, WS_FO + (panel1 * 8 + cb2) * 64, tag);
    } else {
      run(18, 32);
#pragma unroll
      for (int n = 0; n < 6; ++n) zreg[n] = *(LAS u32x2*)(uintptr_t)(lds0 + L_KVT + 6144 + ((wt >> 1) * 6 + n) * 1024 + l * 16 + (wt & 1) * 8);
    }
#pragma unroll
    for (int n = 0; n < 6; ++n) acc[n] = MFMA(bfr[1][n], af[1], acc[n]);
    STAMP(4);
    bar();
    const unsigned L_RED = lds0 + 3 * P3_SLOT;
    if (kh == 1) {
#pragma unroll
      for (int n = 0; n < 6; ++n) stsf4(L_RED + (wt * 6 + n) * 1024 + l * 16, acc[n]);
    }
    bar();
    if (kh == 0) {
#pragma unroll
      for (int n = 0; n < 6; ++n) acc[n] += ldsf4(L_RED + (wt * 6 + n) * 1024 + l * 16);
      u32x2 hv[6];
#pragma unroll
      for (int n = 0; n < 6; ++n) {
        f32x4 gg = acc[n] + bs;
        hv[n] = (u32x2){cvt_pk(lo16(zreg[n][0]) * gg[0], hi16(zreg[n][0]) * gg[1]), cvt_pk(lo16(zreg[n][1]) * gg[2], hi16(zreg[n][1]) * gg[3])};
      }
      const unsigned hb = WS_OH + (unsigned)((panel2 * 44 + 20 + cb2 * 3) * 4 + wt) * 1024 + l * 16;
#pragma unroll
      for (int s = 0; s < 3; ++s) st16(rws, hb + s * 4096, (u32x4){hv[2 * s][0], hv[2 * s][1], hv[2 * s + 1][0], hv[2 * s + 1][1]});
      wait_vm<0>();
      if (l == 0) st8u(ws, WS_F2 + ((panel2 * 8 + cb2) * 4 + wt) * 64, tag);
    }
    bar();
    STAMP(5);
  }
  {
    const int wsr = w & 3, kh = w >> 2;
    const unsigned ao = (unsigned)(kh * 8192 + wsr * 1024 + l * 16);
    const unsigned bb = 16384u + kh * 10240 + l * 16;
    const bf16x8 zeros = {0, 0, 0, 0, 0, 0, 0, 0};
    f32x4 acc[5];
#pragma unroll
    for (int n = 0; n < 5; ++n) acc[n] = (f32x4){0.f, 0.f, 0.f, 0.f};
    bf16x8 af[2][2], bfr[2][2][5];
#pragma unroll
    for (int s2 = 0; s2 < 2; ++s2) {
      af[1][s2] = zeros;
#pragma unroll
      for (int n = 0; n < 5; ++n) bfr[1][s2][n] = zeros;
    }
    u32x4 xv[2];
    const int c0 = tid, c1 = tid + 512;
    const size_t xo0 = (size_t)(panel3 * 64 + c0 / 10) * D_ + cb3 * 80 + (c0 % 10) * 8;
    const size_t xo1 = (size_t)(panel3 * 64 + c1 / 10) * D_ + cb3 * 80 + (c1 % 10) * 8;
    xv[0] = *(const u32x4*)(P.x + xo0);
    if (tid < 128) xv[1] = *(const u32x4*)(P.x + xo1);
    const rsrc_t rout = __builtin_amdgcn_make_buffer_rsrc(P.out, 0, S_ * D_ * 2, 0x00020000);
    unsigned sb = lds0;
    auto mm = [&](int c) {
#pragma unroll
      for (int s2 = 0; s2 < 2; ++s2)
#pragma unroll
        for (int n = 0; n < 5; ++n) acc[n] = MFMA(bfr[c][s2][n], af[c][s2], acc[n]);
      SB0;
    };
    auto unit = [&](int c) {
      bar();
#pragma unroll
      for (int s2 = 0; s2 < 2; ++s2) {
        af[c][s2] = lds_b128(sb + ao + s2 * 4096);
#pragma unroll
        for (int n = 0; n < 5; ++n) bfr[c][s2][n] = lds_b128(sb + bb + s2 * 5120 + n * 1024);
      }
      SB0;
      mm(c ^ 1);
      sb += P3_SLOT; if (sb == lds0 + 4 * P3_SLOT) sb = lds0;
    };
#pragma unroll 1
    for (int u = 0; u < 5; ++u) { unit(0); unit(1); }
    unit(0);
    mm(0);
    STAMP(6);
    bar();
#pragma unroll
    for (int n = 0; n < 5; ++n) stsf4(lds0 + (unsigned)(((kh * 64 + wsr * 16 + fr) * 84 + n * 16 + 4 * g) * 4), acc[n]);
    bar();
#pragma unroll
    for (int i = 0; i < 2; ++i) {
      if (i == 0 || tid < 128) {
        const int c = tid + i * 512, r = c / 10, ch = c - r * 10;
        const unsigned a = lds0 + (unsigned)((r * 84 + ch * 8) * 4);
        f32x4 a0 = ldsf4(a) + ldsf4(a + 64 * 84 * 4), a1 = ldsf4(a + 16) + ldsf4(a + 64 * 84 * 4 + 16);
        u32x4 o = {cvt_pk(lo16(xv[i][0]) + a0[0], hi16(xv[i][0]) + a0[1]), cvt_pk(lo16(xv[i][1]) + a0[2], hi16(xv[i][1]) + a0[3]),
                   cvt_pk(lo16(xv[i][2]) + a1[0], hi16(xv[i][2]) + a1[1]), cvt_pk(lo16(xv[i][3]) + a1[2], hi16(xv[i][3]) + a1[3])};
        __builtin_amdgcn_raw_buffer_store_b128(o, rout, (unsigned)((i == 0 ? xo0 : xo1) * 2), 0, 16);
      }
    }
    STAMP(7);
  }
}

extern "C" void kernel_launch(void* const* d_in, const int* in_sizes, int n_in, void* d_out, int out_size,
                              void* d_ws, size_t ws_size, hipStream_t stream) {
  Params P;
  P.x = (const u16*)d_in[0];
  P.wqkv = (const u16*)d_in[1];
  P.wo = (const u16*)d_in[2];
  P.win = (const u16*)d_in[4];
  P.wsp = (const u16*)d_in[5];
  P.bsp = (const u16*)d_in[6];
  P.wout = (const u16*)d_in[7];
  P.out = (u16*)d_out;
  P.ws = (char*)d_ws;
  (void)hipFuncSetAttribute((const void*)mega_kernel, hipFuncAttributeMaxDynamicSharedMemorySize, LDS_TOTAL);
  mega_kernel<<<256, 768, LDS_TOTAL, stream>>>(P);
}
```

```cpp
#include <hip/hip_runtime.h>
#include <hip/hip_bf16.h>
#include <stdint.h>

typedef __attribute__((ext_vector_type(8))) short bf16x8;
typedef __attribute__((ext_vector_type(4))) short bf16x4;
typedef __attribute__((ext_vector_type(4))) float f32x4;
typedef __attribute__((ext_vector_type(2))) float f32x2;
typedef __attribute__((ext_vector_type(2))) __bf16 bf2;
typedef __attribute__((ext_vector_type(4))) unsigned u32x4;
typedef __attribute__((ext_vector_type(2))) unsigned u32x2;
typedef unsigned short u16;
typedef unsigned long long u64;
#define LAS __attribute__((address_space(3)))
#define GAS __attribute__((address_space(1)))
#define DI __device__ __forceinline__
#define RLX_AGENT __ATOMIC_RELAXED, __HIP_MEMORY_SCOPE_AGENT

constexpr int S_ = 2048, D_ = 640, NQKV = 1920, FIN = 1536, FH = 768;

constexpr unsigned WS_PQ  = 0;
constexpr unsigned WS_Z1  = WS_PQ + 16 * 8 * 8 * 3 * 1024;
constexpr unsigned WS_Z2N = WS_Z1 + 16 * 8 * 24 * 1024;
constexpr unsigned WS_OH  = WS_Z2N + 8 * 64 * 6 * 1024;
constexpr unsigned WS_WF  = WS_OH + 32 * 44 * 4 * 1024;
constexpr unsigned WS_KVT = WS_WF + 8 * 44 * 5 * 1024;
constexpr unsigned WS_KVP = WS_KVT + 8 * 18 * 1024;
constexpr unsigned WS_GR  = WS_KVP + 8 * 18 * 16 * 1024;
constexpr unsigned WS_GX  = WS_GR + 16 * 8 * 256 * 8;
constexpr unsigned WS_GC  = WS_GX + 2048 * 16;
constexpr unsigned WS_F1  = WS_GC + 24 * 10 * 2048;
constexpr unsigned WS_F2  = WS_F1 + 16 * 24 * 64;
constexpr unsigned WS_FO  = WS_F2 + 1024 * 64;
constexpr unsigned WS_FK  = WS_FO + 256 * 64;
constexpr unsigned WS_EP  = WS_FK + 256 * 64;
constexpr unsigned WS_END = WS_EP + 64;

#define STAMP(i)

DI float bf2f(u16 v) { return __uint_as_float(((unsigned)v) << 16); }
DI unsigned cvt_pk(float lo, float hi) {
  bf2 r; r[0] = (__bf16)lo; r[1] = (__bf16)hi; return __builtin_bit_cast(unsigned, r);
}
DI float rbf(float x) { return (float)((__bf16)x); }
DI float lo16(unsigned u) { return __uint_as_float(u << 16); }
DI float hi16(unsigned u) { return __uint_as_float(u & 0xffff0000u); }

DI void glds16(const void* gbase, unsigned voff, unsigned lds_dst) {
  unsigned keep;
  asm volatile("s_mov_b32 %0, m0\n\ts_mov_b32 m0, %3\n\ts_nop 0\n\tglobal_load_lds_dwordx4 %1, %2\n\ts_mov_b32 m0, %0"
               : "=&s"(keep) : "v"(voff), "s"(gbase), "s"(lds_dst) : "memory");
}
DI void glds16c(const void* gbase, unsigned voff, unsigned lds_dst) {
  unsigned keep;
  asm volatile("s_mov_b32 %0, m0\n\ts_mov_b32 m0, %3\n\ts_nop 0\n\tglobal_load_lds_dwordx4 %1, %2 sc1\n\ts_mov_b32 m0, %0"
               : "=&s"(keep) : "v"(voff), "s"(gbase), "s"(lds_dst) : "memory");
}
#define GL_PRE "s_mov_b32 %0, m0\n\ts_mov_b32 m0, %2\n\ts_nop 0\n\t"
#define GL_POST "s_mov_b32 m0, %0"
DI void gl2(const void* gb, unsigned lds, unsigned v0, unsigned v1) {
  unsigned keep;
  asm volatile(GL_PRE "global_load_lds_dwordx4 %3, %1\n\tglobal_load_lds_dwordx4 %4, %1 offset:1024\n\t" GL_POST
               : "=&s"(keep) : "s"(gb), "s"(lds), "v"(v0), "v"(v1) : "memory");
}
DI void gl3(const void* gb, unsigned lds, unsigned v0, unsigned v1, unsigned v2) {
  unsigned keep;
  asm volatile(GL_PRE "global_load_lds_dwordx4 %3, %1\n\tglobal_load_lds_dwordx4 %4, %1 offset:1024\n\tglobal_load_lds_dwordx4 %5, %1 offset:2048\n\t" GL_POST
               : "=&s"(keep) : "s"(gb), "s"(lds), "v"(v0), "v"(v1), "v"(v2) : "memory");
}
DI void gl4(const void* gb, unsigned lds, unsigned v0, unsigned v1, unsigned v2, unsigned v3) {
  unsigned keep;
  asm volatile(GL_PRE "global_load_lds_dwordx4 %3, %1\n\tglobal_load_lds_dwordx4 %4, %1 offset:1024\n\tglobal_load_lds_dwordx4 %5, %1 offset:2048\n\tglobal_load_lds_dwordx4 %6, %1 offset:3072\n\t" GL_POST
               : "=&s"(keep) : "s"(gb), "s"(lds), "v"(v0), "v"(v1), "v"(v2), "v"(v3) : "memory");
}
DI void gl3c(const void* gb, unsigned lds, unsigned v0) {
  unsigned keep;
  asm volatile(GL_PRE "global_load_lds_dwordx4 %3, %1 sc1\n\tglobal_load_lds_dwordx4 %3, %1 offset:1024 sc1\n\tglobal_load_lds_dwordx4 %3, %1 offset:2048 sc1\n\t" GL_POST
               : "=&s"(keep) : "s"(gb), "s"(lds), "v"(v0) : "memory");
}
DI void gl4c(const void* gb, unsigned lds, unsigned v0) {
  unsigned keep;
  asm volatile(GL_PRE "global_load_lds_dwordx4 %3, %1 sc1\n\tglobal_load_lds_dwordx4 %3, %1 offset:1024 sc1\n\tglobal_load_lds_dwordx4 %3, %1 offset:2048 sc1\n\tglobal_load_lds_dwordx4 %3, %1 offset:3072 sc1\n\t" GL_POST
               : "=&s"(keep) : "s"(gb), "s"(lds), "v"(v0) : "memory");
}
template <int N> DI void wait_vm() { asm volatile("s_waitcnt vmcnt(%0)" :: "n"(N) : "memory"); }

DI bf16x8 lds_b128(unsigned addr) { return *(LAS bf16x8*)(uintptr_t)addr; }
DI bf16x8 lds_2b64(unsigned addr0, unsigned addr1) {
  bf16x4 a = *(LAS bf16x4*)(uintptr_t)addr0, b = *(LAS bf16x4*)(uintptr_t)addr1;
  return __builtin_shufflevector(a, b, 0, 1, 2, 3, 4, 5, 6, 7);
}
DI bf16x8 lds_tr(unsigned addr0, unsigned addr1) {
  bf16x4 a = __builtin_amdgcn_ds_read_tr16_b64_v4i16((LAS bf16x4*)(uintptr_t)addr0);
  bf16x4 b = __builtin_amdgcn_ds_read_tr16_b64_v4i16((LAS bf16x4*)(uintptr_t)addr1);
  return __builtin_shufflevector(a, b, 0, 1, 2, 3, 4, 5, 6, 7);
}
DI float ldsf(unsigned a) { return *(LAS float*)(uintptr_t)a; }
DI void stsf(unsigned a, float v) { *(LAS float*)(uintptr_t)a = v; }
DI f32x4 ldsf4(unsigned a) { return *(LAS f32x4*)(uintptr_t)a; }
DI void stsf4(unsigned a, f32x4 v) { *(LAS f32x4*)(uintptr_t)a = v; }
#define SB0 __builtin_amdgcn_sched_barrier(0)
#define MFMA(a, b, c) __builtin_amdgcn_mfma_f32_16x16x32_bf16((a), (b), (c), 0, 0, 0)

template <int CTRL> DI float dpp(float v) {
  return __builtin_bit_cast(float, __builtin_amdgcn_update_dpp(0, __builtin_bit_cast(int, v), CTRL, 0xf, 0xf, true));
}
DI float sum8(float v) { v += dpp<0xB1>(v); v += dpp<0x4E>(v); v += dpp<0x141>(v); return v; }
DI float sum16(float v) { v = sum8(v); v += dpp<0x140>(v); return v; }

DI float gelu_t(float x) {
  float u = x * (0.7978845608f + 0.0356774081f * x * x);
  float e = __builtin_amdgcn_exp2f(-2.885390082f * u);
  return x * __builtin_amdgcn_rcpf(1.0f + e);
}
DI f32x4 gelu4(f32x4 x) {
  const f32x4 a = x * ((x * x) * -0.1029432397f + -2.302208198f);
  f32x4 e;
#pragma unroll
  for (int j = 0; j < 4; ++j) e[j] = __builtin_amdgcn_exp2f(a[j]);
  e += 1.0f;
#pragma unroll
  for (int j = 0; j < 4; ++j) e[j] = __builtin_amdgcn_rcpf(e[j]);
  return x * e;
}
DI f32x4 phi4(f32x4 t) {
  f32x4 a, m;
#pragma unroll
  for (int j = 0; j < 4; ++j) { a[j] = fminf(t[j], 0.f); m[j] = fmaxf(t[j], 0.f); }
  a *= 1.4426950409f;
#pragma unroll
  for (int j = 0; j < 4; ++j) a[j] = __builtin_amdgcn_exp2f(a[j]);
  return (m + 1e-6f) + a;
}
DI float phi_f(float t) {
  return (fmaxf(t, 0.f) + 1e-6f) + __builtin_amdgcn_exp2f(fminf(t, 0.f) * 1.4426950409f);
}

typedef __amdgpu_buffer_rsrc_t rsrc_t;
DI void st16(rsrc_t r, unsigned off, u32x4 v) { __builtin_amdgcn_raw_buffer_store_b128(v, r, off, 0, 16); }
DI u32x4 ld16(rsrc_t r, unsigned off) { return __builtin_amdgcn_raw_buffer_load_b128(r, off, 0, 16); }
DI void st8(char* ws, unsigned off, u32x2 v) { __hip_atomic_store((GAS u64*)(ws + off), ((u64)v[1] << 32) | v[0], RLX_AGENT); }
DI void st8u(char* ws, unsigned off, u64 v) { __hip_atomic_store((GAS u64*)(ws + off), v, RLX_AGENT); }
DI void st4(char* ws, unsigned off, float v) { __hip_atomic_store((GAS unsigned*)(ws + off), __float_as_uint(v), RLX_AGENT); }
DI u64 ld8(const char* ws, unsigned off) { return __hip_atomic_load((GAS u64*)(ws + off), RLX_AGENT); }
constexpr unsigned SPIN_CAP = 1u << 20;
DI void wait_words(const char* ws, unsigned off, bool act, u64 tag) {
#pragma unroll 1
  for (unsigned sp = 0; sp < SPIN_CAP; ++sp) {
    u64 v = act ? ld8(ws, off) : tag;
    if (__all(v == tag)) break;
    __builtin_amdgcn_s_sleep(1);
  }
  __builtin_amdgcn_fence(__ATOMIC_ACQUIRE, "wavefront");
}

struct Params { const u16 *x, *wqkv, *wo, *win, *wsp, *bsp, *wout; u16* out; char* ws; };

constexpr unsigned KBS = 4224;
constexpr unsigned K1_STAGE = 16384 + 8 * KBS;
constexpr unsigned L_MISC = 153600;
constexpr int LDS_TOTAL = 163840;
constexpr unsigned P2_SLOT = 20480;
constexpr unsigned L_KVT = 135168;
constexpr unsigned P3_SLOT = 36864;

DI void bar() { asm volatile("s_waitcnt lgkmcnt(0)" ::: "memory"); __builtin_amdgcn_s_barrier(); asm volatile("" ::: "memory"); }

struct Side { u32x4 r0, r1; unsigned wv[8]; };
DI void side_issue(const Params& P, Side& S, int row, int w) {
  const int l = threadIdx.x & 63;
  const u32x4* src = (const u32x4*)(P.x + (size_t)row * D_);
  S.r0 = src[l]; S.r1 = src[64 + (l & 15)];
  int item = blockIdx.x; if (item > 219) item = 219;
  const int st = item / 5, T = (item - st * 5) * 8 + w;
  const u16* wsrc = (st < 20 ? P.wo + (size_t)st * 32 * D_ : P.wout + (size_t)(st - 20) * 32 * D_) + (size_t)(l >> 4) * 4 * D_ + T * 16 + (l & 15);
#pragma unroll
  for (int e = 0; e < 8; ++e) S.wv[e] = wsrc[(size_t)(16 * (e >> 2) + (e & 3)) * D_];
}
DI float rdl(float v, int lane) { return __builtin_bit_cast(float, __builtin_amdgcn_readlane(__builtin_bit_cast(int, v), lane)); }
DI void side_finish(const Params& P, rsrc_t rws, const Side& S, int row, int w, unsigned tl, unsigned th) {
  const int l = threadIdx.x & 63;
  float s = 0.f, q = 0.f, s2 = 0.f, q2 = 0.f;
#pragma unroll
  for (int j = 0; j < 4; ++j) {
    float a = lo16(S.r0[j]), b = hi16(S.r0[j]), c = lo16(S.r1[j]), d = hi16(S.r1[j]);
    s += a + b; q += a * a + b * b; s2 += c + d; q2 += c * c + d * d;
  }
  s = sum16(s); q = sum16(q); s2 = sum16(s2); q2 = sum16(q2);
  s = (rdl(s, 0) + rdl(s, 16)) + (rdl(s, 32) + rdl(s, 48)) + s2;
  q = (rdl(q, 0) + rdl(q, 16)) + (rdl(q, 32) + rdl(q, 48)) + q2;
  const float m = s * (1.f / D_);
  const float v = rbf(fmaxf(q * (1.f / D_) - m * m, 0.f));
  const float r = 1.f / rbf(sqrtf(rbf(v + 1e-5f)));
  if (l < 2) st8u(P.ws, WS_GX + row * 16 + l * 8, l == 0 ? (((u64)tl << 32) | __float_as_uint(-rbf(m) * r)) : (((u64)th << 32) | __float_as_uint(r)));
  int item = blockIdx.x; if (item > 219) item = 219;
  const int st = item / 5, T = (item - st * 5) * 8 + w, cb = T / 5, nt = T - cb * 5;
  st16(rws, WS_WF + (unsigned)(((cb * 44 + st) * 5 + nt) * 1024) + l * 16,
       (u32x4){S.wv[0] | (S.wv[1] << 16), S.wv[2] | (S.wv[3] << 16), S.wv[4] | (S.wv[5] << 16), S.wv[6] | (S.wv[7] << 16)});
}
DI void xstat_gather(const char* ws, int panel, unsigned L_ST, int tid, unsigned tg, u64 gv) {
  if (tid < 256) {
#pragma unroll 1
    for (unsigned sp = 0; sp < SPIN_CAP; ++sp) {
      if (__all((unsigned)(gv >> 32) == tg)) break;
      __builtin_amdgcn_s_sleep(1);
      gv = ld8(ws, WS_GX + panel * 2048 + tid * 8);
    }
    stsf(L_ST + (tid & 1) * 512 + (tid >> 1) * 4, __uint_as_float((unsigned)gv));
  }
}

DI void csum_issue(const char* ws, int blk, int t, u64 (&cv)[10]) {
#pragma unroll
  for (int k = 0; k < 10; ++k) cv[k] = ld8(ws, WS_GC + (blk * 10 + k) * 2048 + t * 8);
}
DI void csum_gather(const char* ws, int blk, int t, unsigned tl, u64 (&cv)[10], unsigned dst) {
#pragma unroll 1
  for (unsigned sp = 0; sp < SPIN_CAP; ++sp) {
    bool ok = true;
#pragma unroll
    for (int k = 0; k < 10; ++k) ok &= (unsigned)(cv[k] >> 32) == tl;
    if (__all(ok)) break;
    __builtin_amdgcn_s_sleep(1);
    csum_issue(ws, blk, t, cv);
  }
  float a = 0.f;
#pragma unroll
  for (int k = 0; k < 10; ++k) a += __uint_as_float((unsigned)cv[k]);
  stsf(dst + t * 4, a);
}
DI void csum_store(char* ws, unsigned off, f32x4 d, int fr, int g, unsigned tl) {
  const float v = fr == 0 ? d[0] : fr == 1 ? d[1] : fr == 2 ? d[2] : d[3];
  if (fr < 4) st8u(ws, off + (4 * g + fr) * 8, ((u64)tl << 32) | __float_as_uint(v));
}

DI void p1q_consumer(const Params& P, rsrc_t rws, int w, int panel, int cbi, unsigned lds0, u64 tag, bf16x8 (&pf)[3]) {
  char* ws = P.ws;
  const int tid = threadIdx.x, l = tid & 63;
  const int wm = w >> 2, wc = (w + 2 * wm) & 3;
  const int g = l >> 4, fr = l & 15, q = (l & 15) >> 2, p = l & 3, g1 = g & 1;
  const unsigned aoff0 = (unsigned)((wm * 64 + fr) * 128 + ((g ^ (l & 7)) << 4));
  const unsigned ttab = (unsigned)(0x4498E765DC32BA10ull >> (16 * wc)) & 0xffffu;
  int tile[4];
  unsigned bo[4];
#pragma unroll
  for (int n = 0; n < 4; ++n) {
    tile[n] = (ttab >> (4 * n)) & 15;
    bo[n] = 16384u + g * KBS + q * 480 + p * 8 + tile[n] * 32;
  }
  const unsigned tl = (unsigned)tag, th = tl * 0x9E3779B1u ^ (unsigned)(tag >> 32);
  {
    Side S; side_issue(P, S, panel * 128 + cbi * 8 + w, w);
    side_finish(P, rws, S, panel * 128 + cbi * 8 + w, w, tl, th);
  }
  f32x4 acc[4][4];
#pragma unroll
  for (int m = 0; m < 4; ++m)
#pragma unroll
    for (int n = 0; n < 4; ++n) acc[m][n] = (f32x4){0.f, 0.f, 0.f, 0.f};
  const short one = 0x3F80;
  const bf16x8 ones = {one, one, one, one, one, one, one, one};
  const bf16x8 zeros = {0, 0, 0, 0, 0, 0, 0, 0};

  bf16x8 af[2][4], bfr[2][4];
#pragma unroll
  for (int m = 0; m < 4; ++m) { af[1][m] = zeros; bfr[1][m] = zeros; }
  auto mm = [&](int ks) {
#pragma unroll
    for (int n = 0; n < 3; ++n)
#pragma unroll
      for (int m = 0; m < 4; ++m) acc[m][n] = MFMA(bfr[ks][n], af[ks][m], acc[m][n]);
    if (wc < 3) {
#pragma unroll
      for (int m = 0; m < 4; ++m) acc[m][3] = MFMA(bfr[ks][3], af[ks][m], acc[m][3]);
    }
  };
  auto step = [&](unsigned sb, int ks) {
#pragma unroll
    for (int n = 0; n < 4; ++n) bfr[ks][n] = lds_tr(sb + ks * 4 * KBS + bo[n], sb + ks * 4 * KBS + bo[n] + 1920);
#pragma unroll
    for (int m = 0; m < 4; ++m) af[ks][m] = lds_b128(sb + ((aoff0 ^ (ks * 64)) + m * 2048));
    SB0;
    mm(ks ^ 1);
    SB0;
  };
  unsigned sb = lds0;
  auto run = [&](int n) {
#pragma unroll 1
    for (int kt = 0; kt < n; ++kt) {
      bar();
      step(sb, 0);
      step(sb, 1);
      sb += K1_STAGE; if (sb == lds0 + 3 * K1_STAGE) sb = lds0;
    }
  };
  run(1);
  if (panel < 10) {
#pragma unroll
    for (int h = 0; h < 2; ++h) {
      const bf16x8 b0 = wm == 0 ? bfr[0][h] : bfr[0][2 + h], b1 = wm == 0 ? bfr[1][h] : bfr[1][2 + h];
      const int tt = wm == 0 ? tile[h] : tile[2 + h];
      f32x4 d = MFMA(b0, ones, ((f32x4){0.f, 0.f, 0.f, 0.f}));
      d = MFMA(b1, ones, d);
      csum_store(ws, WS_GC + (cbi * 10 + panel) * 2048 + tt * 128, d, fr, g, tl);
    }
  }
  run(9);
  mm(1);
  STAMP(1);
  bar();
  STAMP(10);
  const unsigned L_ST = lds0 + L_MISC, L_CS = lds0 + L_MISC + 5120;
  const unsigned L_K = lds0, L_V = L_K + 20480, L_Q = lds0 + 122880;
  float mr[4], rinv[4];
#pragma unroll
  for (int m = 0; m < 4; ++m) { const int rl = wm * 64 + m * 16 + fr; mr[m] = ldsf(L_ST + rl * 4); rinv[m] = ldsf(L_ST + 512 + rl * 4); }
#pragma unroll
  for (int n = 0; n < 4; ++n) {
    if (n < 3 || wc < 3) {
      const int t = tile[n];
      const f32x4 cs = ldsf4(L_CS + (t * 16 + 4 * g) * 4);
      unsigned dst, dstep;
      if (t < 5) { dst = L_Q + (wm * 12 + (t >> 1)) * 1024 + l * 16 + (t & 1) * 8; dstep = 3072; }
      else {
        const int kb = wm * 8 + (fr >> 3), pos = (fr & 7) ^ ((fr >> 3) << 2);
        dst = (t < 10 ? L_K + (t - 5) * 256 : L_V + (t - 10) * 256) + kb * 1280 + pos * 32 + g * 8; dstep = 2560;
      }
      if (t < 10) {
#pragma unroll
        for (int m = 0; m < 4; ++m) {
          f32x4 v = acc[m][n] * rinv[m] + cs * mr[m];
          v = phi4(v);
          *(LAS u32x2*)(uintptr_t)(dst + m * dstep) = (u32x2){cvt_pk(v[0], v[1]), cvt_pk(v[2], v[3])};
        }
      } else {
#pragma unroll
        for (int m = 0; m < 4; ++m) {
          f32x4 v = acc[m][n] * rinv[m] + cs * mr[m];
          *(LAS u32x2*)(uintptr_t)(dst + m * dstep) = (u32x2){cvt_pk(v[0], v[1]), cvt_pk(v[2], v[3])};
        }
      }
    }
  }
  STAMP(8);
  bar();
  {
    const unsigned troff = (unsigned)(g * 5 * 256 + ((4 * g1 + q) * 32) + p * 8);
    f32x4 d[3];
    int jb[3];
#pragma unroll
    for (int r = 0; r < 3; ++r) {
      int job = w + 8 * r; if (job >= 18) job = w;
      jb[r] = job;
      const int i = job < 13 ? (job + 1) / 3 : job - 13;
      const int j = job < 13 ? job - 2 * i : 0;
      d[r] = (f32x4){0.f, 0.f, 0.f, 0.f};
#pragma unroll
      for (int ks = 0; ks < 4; ++ks) {
        const unsigned a0 = L_K + troff + (ks * 20 + i) * 256, b0 = L_V + troff + (ks * 20 + j) * 256;
        const bf16x8 ka = lds_tr(a0, a0 ^ 128);
        bf16x8 vb = lds_tr(b0, b0 ^ 128);
        if (job >= 13) vb = ones;
        d[r] = MFMA(ka, vb, d[r]);
      }
    }
#pragma unroll
    for (int r = 0; r < 3; ++r)
      st16(rws, WS_KVP + (unsigned)(((cbi * 18 + jb[r]) * 16 + panel) * 1024) + l * 16, __builtin_bit_cast(u32x4, d[r]));
    pf[0] = lds_b128(L_Q + w * 3072 + l * 16); pf[1] = lds_b128(L_Q + w * 3072 + 1024 + l * 16);
    const u32x2 q2 = *(LAS u32x2*)(uintptr_t)(L_Q + w * 3072 + 2048 + l * 16);
    pf[2] = __builtin_bit_cast(bf16x8, ((u32x4){q2[0], q2[1], 0u, 0u}));
  }
  STAMP(9);
  wait_vm<0>();
  bar();
  if (tid == 0) st8u(ws, WS_F1 + (panel * 24 + cbi) * 64, tag);
}

DI void p1z_consumer(const Params& P, rsrc_t rws, int w, int panel, int cbj, unsigned lds0, u64 tag) {
  char* ws = P.ws;
  const int tid = threadIdx.x, l = tid & 63;
  const int wm = w & 3, wn = w >> 2;
  const int g = l >> 4, fr = l & 15, q = (l & 15) >> 2, p = l & 3, g1 = g & 1;
  const unsigned aoff0 = (unsigned)((wm * 32 + fr) * 128 + ((g ^ (l & 7)) << 4));
  unsigned bo[3];
  int ca[3];
#pragma unroll
  for (int n = 0; n < 3; ++n) {
    const int c = wn * 3 + n;
    ca[n] = c;
    int pc = c + g1; if (pc == 6) pc = 0;
    bo[n] = 16384u + g * 1536 + q * 192 + pc * 32 + p * 8;
  }
  const short one = 0x3F80;
  const bf16x8 ones = {one, one, one, one, one, one, one, one};
  const bf16x8 zeros = {0, 0, 0, 0, 0, 0, 0, 0};
  f32x4 accA[2][3], accB[2][3];
#pragma unroll
  for (int m = 0; m < 2; ++m)
#pragma unroll
    for (int n = 0; n < 3; ++n) { accA[m][n] = (f32x4){0.f, 0.f, 0.f, 0.f}; accB[m][n] = (f32x4){0.f, 0.f, 0.f, 0.f}; }
  const unsigned tl = (unsigned)tag, th = tl * 0x9E3779B1u ^ (unsigned)(tag >> 32), tg = (tid & 1) ? th : tl;
  bf16x8 af[2][2], bfr[2][3];
  af[1][0] = af[1][1] = zeros; bfr[1][0] = bfr[1][1] = bfr[1][2] = zeros;
  auto rd = [&](unsigned sb, int ks) {
#pragma unroll
    for (int m = 0; m < 2; ++m) af[ks][m] = lds_b128(sb + ((aoff0 ^ (ks * 64)) + m * 2048));
#pragma unroll
    for (int n = 0; n < 3; ++n) { const unsigned o = sb + ks * 4 * 1536 + bo[n]; bfr[ks][n] = lds_tr(o, o + 768); }
    SB0;
  };
  auto mmA = [&](int ks) {
#pragma unroll
    for (int n = 0; n < 3; ++n)
#pragma unroll
      for (int m = 0; m < 2; ++m) accA[m][n] = MFMA(af[ks][m], bfr[ks][n], accA[m][n]);
    SB0;
  };
  auto mmB = [&](int ks) {
#pragma unroll
    for (int n = 0; n < 3; ++n)
#pragma unroll
      for (int m = 0; m < 2; ++m) accB[m][n] = MFMA(bfr[ks][n], af[ks][m], accB[m][n]);
    SB0;
  };
  const unsigned L_ST = lds0 + L_MISC, L_CS = lds0 + L_MISC + 5120;
  const unsigned L_RS = lds0 + L_MISC + 2048;
  const unsigned L_FS = lds0 + L_MISC + 4096;
  unsigned sb = lds0;
  auto nxt = [&]() { sb += K1_STAGE; if (sb == lds0 + 3 * K1_STAGE) sb = lds0; };
  auto runA = [&](int n) {
#pragma unroll 1
    for (int kt = 0; kt < n; ++kt) { bar(); rd(sb, 0); mmA(1); rd(sb, 1); mmA(0); nxt(); }
  };
  auto runB = [&](int n) {
#pragma unroll 1
    for (int kt = 0; kt < n; ++kt) { bar(); rd(sb, 0); mmB(1); rd(sb, 1); mmB(0); nxt(); }
  };
  auto cpub = [&](int blk) {
    if (panel < 10 && wm == 0) {
#pragma unroll
      for (int n = 0; n < 3; ++n) {
        f32x4 d = MFMA(bfr[0][n], ones, ((f32x4){0.f, 0.f, 0.f, 0.f}));
        d = MFMA(bfr[1][n], ones, d);
        csum_store(ws, WS_GC + (blk * 10 + panel) * 2048 + ca[n] * 128, d, fr, g, tl);
      }
    }
  };
  {
    Side S; side_issue(P, S, panel * 128 + 64 + cbj * 8 + w, w);
    side_finish(P, rws, S, panel * 128 + 64 + cbj * 8 + w, w, tl, th);
  }
  runA(1);
  cpub(8 + cbj);
  runA(6);
  const int tc = tid - 256;
  {
    u64 cv[10];
    if (tid < 256) cv[0] = ld8(ws, WS_GX + panel * 2048 + tid * 8);
    else if (tc < 96) csum_issue(ws, 8 + cbj, tc, cv);
    runA(3);
    if (tid < 256) xstat_gather(ws, panel, L_ST, tid, tg, cv[0]);
    else if (tc < 96) csum_gather(ws, 8 + cbj, tc, tl, cv, L_CS);
  }
  bar(); rd(sb, 0); mmA(1); rd(sb, 1);
  cpub(16 + cbj);
  STAMP(8);
  {
    float cn[3];
#pragma unroll
    for (int n = 0; n < 3; ++n) cn[n] = ldsf(L_CS + (ca[n] * 16 + fr) * 4);
#pragma unroll
    for (int m = 0; m < 2; ++m) {
      const int rl = wm * 32 + m * 16 + 4 * g;
      const f32x4 mr = ldsf4(L_ST + rl * 4), rinv = ldsf4(L_ST + 512 + rl * 4);
      f32x4 rs = {0.f, 0.f, 0.f, 0.f}, rq = {0.f, 0.f, 0.f, 0.f};
#pragma unroll
      for (int n = 0; n < 3; ++n) {
        f32x4 v = accA[m][n] * rinv + mr * cn[n];
        v = gelu4(v);
        rs += v; rq += v * v;
        accA[m][n] = v;
      }
#pragma unroll
      for (int j = 0; j < 4; ++j) { rs[j] = sum16(rs[j]); rq[j] = sum16(rq[j]); }
      if (fr == 0) {
        stsf4(L_RS + ((wn * 128 + rl) * 2) * 4, (f32x4){rs[0], rq[0], rs[1], rq[1]});
        stsf4(L_RS + ((wn * 128 + rl) * 2 + 4) * 4, (f32x4){rs[2], rq[2], rs[3], rq[3]});
      }
    }
  }
  mmB(0); nxt();
  bar(); rd(sb, 0);
  float part = 0.f;
  const unsigned gbase = WS_GR + (unsigned)(panel * 8) * 2048 + tid * 8;
  if (tid < 256) {
    part = ldsf(L_RS + tid * 4) + ldsf(L_RS + 1024 + tid * 4);
    st8u(ws, gbase + cbj * 2048, ((u64)tg << 32) | __float_as_uint(part));
  }
  mmB(1); rd(sb, 1); mmB(0); nxt();
  STAMP(9);
  runB(1);
  {
    u64 cv[10];
    if (tid < 256) {
#pragma unroll
      for (int i = 1; i < 8; ++i) cv[i] = ld8(ws, gbase + ((cbj + i) & 7) * 2048);
    }
    runB(1);
    if (tid < 256) {
      float tot;
#pragma unroll 1
      for (unsigned sp = 0; sp < SPIN_CAP; ++sp) {
        bool ok = true; tot = part;
#pragma unroll
        for (int i = 1; i < 8; ++i) { ok &= (unsigned)(cv[i] >> 32) == tg; tot += __uint_as_float((unsigned)cv[i]); }
        if (__all(ok)) break;
#pragma unroll
        for (int i = 1; i < 8; ++i) cv[i] = ld8(ws, gbase + ((cbj + i) & 7) * 2048);
      }
      float other = dpp<0xB1>(tot);
      if ((tid & 1) == 0) {
        float m = tot * (1.f / FH);
        float v = rbf(fmaxf(other * (1.f / FH) - m * m, 0.f));
        float r = 1.f / rbf(sqrtf(rbf(v + 1e-5f)));
        stsf(L_FS + (tid >> 1) * 4, -rbf(m) * r);
        stsf(L_FS + 512 + (tid >> 1) * 4, r);
      }
    }
  }
  bar(); rd(sb, 0);
  {
    f32x4 mr[2], ri[2];
#pragma unroll
    for (int m = 0; m < 2; ++m) { const int rl = wm * 32 + m * 16 + 4 * g; mr[m] = ldsf4(L_FS + rl * 4); ri[m] = ldsf4(L_FS + 512 + rl * 4); }
#pragma unroll
    for (int n = 0; n < 3; ++n) {
      f32x4 a = accA[0][n] * ri[0] + mr[0], b = accA[1][n] * ri[1] + mr[1];
      st16(rws, WS_Z2N + (unsigned)(((cbj * 64 + panel * 4 + wm) * 6 + ca[n]) * 1024) + l * 16,
           (u32x4){cvt_pk(a[0], a[1]), cvt_pk(a[2], a[3]), cvt_pk(b[0], b[1]), cvt_pk(b[2], b[3])});
    }
  }
  mmB(1); rd(sb, 1); mmB(0); nxt();
  STAMP(10);
  runB(2);
  wait_vm<0>();
  bar();
  if (tid == 0) st8u(ws, WS_F1 + (panel * 24 + 8 + cbj) * 64, tag);
  STAMP(11);
  rd(sb, 0); mmB(1); rd(sb, 1); mmB(0); nxt();
  {
    u64 cv[10];
    if (tid >= 256 && tc < 96) csum_issue(ws, 16 + cbj, tc, cv);
    runB(2);
    if (tid >= 256 && tc < 96) csum_gather(ws, 16 + cbj, tc, tl, cv, L_CS + 512);
  }
  mmB(1);
  STAMP(1);
  bar();
  {
    f32x4 cn[3];
#pragma unroll
    for (int n = 0; n < 3; ++n) { const unsigned a = L_CS + 512 + (ca[n] * 16 + 4 * g) * 4; cn[n] = ldsf4(a); }
    float mr[2], rinv[2];
#pragma unroll
    for (int m = 0; m < 2; ++m) { const int rl = wm * 32 + m * 16 + fr; mr[m] = ldsf(L_ST + rl * 4); rinv[m] = ldsf(L_ST + 512 + rl * 4); }
#pragma unroll
    for (int n = 0; n < 3; ++n) {
      u32x4 o;
#pragma unroll
      for (int m = 0; m < 2; ++m) {
        f32x4 v = accB[m][n] * rinv[m] + cn[n] * mr[m];
        v = gelu4(v);
        o[2 * m] = cvt_pk(v[0], v[1]); o[2 * m + 1] = cvt_pk(v[2], v[3]);
      }
      if (wm < 2) *(LAS u32x4*)(uintptr_t)(lds0 + L_KVT + 6144 + (wm * 6 + ca[n]) * 1024 + l * 16) = o;
      else st16(rws, WS_Z1 + (unsigned)(((panel * 8 + cbj) * 4 + wm) * 6 + ca[n]) * 1024 + l * 16, o);
    }
  }
}

__global__ __launch_bounds__(768, 3) void mega_kernel(Params P) {
  extern __shared__ __attribute__((aligned(16))) char smem[];
  const unsigned lds0 = (unsigned)(uintptr_t)(LAS char*)smem;
  char* ws = P.ws;
  const int tid = threadIdx.x, l = tid & 63;
  const int w = __builtin_amdgcn_readfirstlane(tid >> 6);
  const int g = l >> 4, fr = l & 15;
  const int b = blockIdx.x, xcd = b & 7, bi = b >> 3;
  const int panel1 = (xcd >> 1) * 4 + (bi & 3), cb1 = (xcd & 1) * 4 + ((bi >> 2) & 3);
  const bool isq = bi < 16;
  const int panel2 = 2 * panel1 + (isq ? 1 : 0), cb2 = cb1;
  const int panel3 = xcd * 4 + (bi & 3), cb3 = bi >> 2;
  STAMP(0);
  const u64 tag = ld8(ws, WS_EP) + 1;
  if (w == 7 && bi < 20) {
    u64 pc; asm volatile("s_getpc_b64 %0" : "=s"(pc));
    u32x4 d0, d1;
    asm volatile("global_load_dwordx4 %0, %2, %3\n\tglobal_load_dwordx4 %1, %2, %3 offset:1024\n\ts_waitcnt vmcnt(0)"
                 : "=&v"(d0), "=&v"(d1) : "v"(bi * 2048 + l * 16), "s"(pc & ~127ull) : "memory");
  }

  if (w >= 8) {
    const int lw = w - 8;
    const unsigned vA0 = (unsigned)(((l >> 3) * D_ + (((l & 7) ^ (l >> 3)) << 3)) * 2);
    const unsigned vA1 = vA0 + 8 * D_ * 2 - 1024, vA2 = vA0 + 16 * D_ * 2 - 2048, vA3 = vA0 + 24 * D_ * 2 - 3072;
    const char* gA = (const char*)(P.x + (size_t)(panel1 * 128 + lw * 32) * D_);
    const unsigned v20 = (unsigned)(((l >> 3) * S_ + (((l & 7) ^ (l >> 4)) << 3)) * 2);
    const unsigned v21 = (unsigned)((((l >> 3) + 8) * S_ + (((l & 7) ^ ((l >> 4) | 4)) << 3)) * 2) - 1024;
    const char* gA2 = (const char*)(P.wsp + (size_t)(panel2 * 64 + lw * 16) * S_);
    const int rot2 = 0;
    auto ldA = [&](int kt, unsigned sl) { gl2(gA2 + ((kt + rot2) & 31) * 128, sl + lw * 2048, v20, v21); };
    auto sl2 = [&](int kt) { return lds0 + ((kt + 4) % 6) * P2_SLOT; };
    const unsigned tl = (unsigned)tag, th = tl * 0x9E3779B1u ^ (unsigned)(tag >> 32);
    const int lt = tid - 512;
    auto gather = [&](int blk, int ncol, unsigned dst, bool xs) {
      u64 cv[10]; u64 gv = 0;
      const int tc = lt < ncol ? lt : 0;
      if (xs) gv = ld8(ws, WS_GX + panel1 * 2048 + lt * 8);
      csum_issue(ws, blk, tc, cv);
      if (xs) xstat_gather(ws, panel1, lds0 + L_MISC, lt, (lt & 1) ? th : tl, gv);
      csum_gather(ws, blk, tc, tl, cv, dst);
    };
    const unsigned l16 = l * 16;
    const char* gB2 = ws + WS_Z2N + (size_t)cb2 * 64 * 6144 + lw * 3072;
    auto ldB = [&](int kt, unsigned sl) { gl3c(gB2 + ((kt + rot2) & 31) * 12288, sl + 8192 + lw * 3072, l16); };
    const unsigned zfl = WS_F1 + ((l & 15) * 24 + 8 + cb2) * 64;
    auto try_z = [&]() {
      const u64 v = l < 16 ? ld8(ws, zfl) : tag;
      return (bool)__builtin_amdgcn_readfirstlane((int)__all(v == tag));
    };
    bool early = false;
    const int rot = panel1 < 10 ? panel1 : 0;
    auto krot = [&](int kt) { const int k = kt >= 10 ? kt - 10 : kt; return __builtin_amdgcn_readfirstlane(k == 0 ? rot : (k - 1 < rot ? k - 1 : k)); };
    if (isq) {
      gl4(gA + rot * 128, lds0 + lw * 4096, vA0, vA1, vA2, vA3);
      unsigned vB[4];
#pragma unroll
      for (int i = 0; i < 4; ++i) {
        int c = i * 64 + l; if (c > 239) c = 239;
        const int row = c / 30, cc = c - row * 30, seg = cc / 10, ch = cc - seg * 10;
        vB[i] = (unsigned)((row * NQKV + seg * 640 + cb1 * 80 + ch * 8) * 2) + 4096 - i * 1024;
      }
      const char* gB = (const char*)P.wqkv + (size_t)lw * 16 * NQKV * 2 - 4096;
      auto stageB = [&](int kt, unsigned sb) {
        const char* bp = gB + (size_t)krot(kt) * 64 * NQKV * 2;
        gl4(bp, sb + 16384 + lw * 2 * KBS, vB[0], vB[1], vB[2], vB[3]);
        gl4(bp + 8 * NQKV * 2, sb + 16384 + (lw * 2 + 1) * KBS, vB[0], vB[1], vB[2], vB[3]);
      };
      auto stage = [&](int kt, unsigned sb) { gl4(gA + krot(kt) * 128, sb + lw * 4096, vA0, vA1, vA2, vA3); stageB(kt, sb); };
      stageB(0, lds0); stage(1, lds0 + K1_STAGE);
      wait_vm<12>(); bar();
      stage(2, lds0 + 2 * K1_STAGE);
      unsigned sb = lds0, sc = lds0 + K1_STAGE;
#pragma unroll 1
      for (int kt = 1; kt < 8; ++kt) {
        wait_vm<12>(); bar();
        if (kt == 2) STAMP(10);
        if (kt == 7) STAMP(11);
        stage(kt + 2, sb);
        sb = sc; sc += K1_STAGE; if (sc == lds0 + 3 * K1_STAGE) sc = lds0;
      }
      wait_vm<12>(); bar();
      gather(cb1, 240, lds0 + L_MISC + 5120, true);
      bar();
      ldA(0, sl2(0)); ldA(1, sl2(1)); ldA(5, sl2(5));
      bar();
      ldA(4, sl2(4));
#pragma unroll 1
      for (int t = 0; t < 3 && !early; ++t) early = try_z();
      if (early) { ldB(0, sl2(0)); ldB(1, sl2(1)); ldB(4, sl2(4)); ldB(5, sl2(5)); }
      bar();
      if (!early) { early = try_z(); if (early) { ldB(0, sl2(0)); ldB(1, sl2(1)); ldB(4, sl2(4)); ldB(5, sl2(5)); } }
      bar();
    } else {
      gl4(gA + rot * 128, lds0 + lw * 4096, vA0, vA1, vA2, vA3);
      unsigned vB[3];
#pragma unroll
      for (int i = 0; i < 3; ++i) {
        const int c = i * 64 + l, row = c / 12, cc = c - row * 12;
        int pc = (cc >> 1) - (row >> 3); if (pc < 0) pc = 5;
        vB[i] = (unsigned)((row * FIN + cb1 * 96 + pc * 16 + (cc & 1) * 8) * 2) + 4096 - i * 1024;
      }
      const char* gB = (const char*)P.win + (size_t)lw * 16 * FIN * 2 - 4096;
      auto stageB = [&](int kt, unsigned sb) {
        const int k = krot(kt);
        gl3(gB + (kt < 10 ? FH * 2 : 0) + (size_t)k * 64 * FIN * 2, sb + 16384 + lw * 3072, vB[0], vB[1], vB[2]);
      };
      auto stage = [&](int kt, unsigned sb) {
        const int k = krot(kt);
        gl4(gA + k * 128, sb + lw * 4096, vA0, vA1, vA2, vA3); stageB(kt, sb);
      };
      stageB(0, lds0); stage(1, lds0 + K1_STAGE);
      wait_vm<7>(); bar();
      stage(2, lds0 + 2 * K1_STAGE);
      unsigned sb = lds0, sc = lds0 + K1_STAGE;
      auto runL = [&](int k0, int k1) {
#pragma unroll 1
        for (int kt = k0; kt < k1; ++kt) {
          wait_vm<7>(); bar();
          stage(kt + 2, sb);
          sb = sc; sc += K1_STAGE; if (sc == lds0 + 3 * K1_STAGE) sc = lds0;
        }
      };
      runL(1, 11);
      {
        const char* wp = (const char*)(P.wsp + (size_t)(panel1 * 128 + lw * 32) * S_) + (cb1 & 3) * 1024;
        const unsigned vo = (unsigned)((l >> 3) * S_ * 2 + (l & 7) * 128);
        unsigned d0, d1, d2, d3;
        asm volatile("global_load_dword %0, %4, %5\n\tglobal_load_dword %1, %4, %6\n\tglobal_load_dword %2, %4, %7\n\tglobal_load_dword %3, %4, %8"
                     : "=&v"(d0), "=&v"(d1), "=&v"(d2), "=&v"(d3)
                     : "v"(vo), "s"(wp), "s"(wp + 8 * S_ * 2), "s"(wp + 16 * S_ * 2), "s"(wp + 24 * S_ * 2) : "memory");
        runL(11, 13);
        asm volatile("" :: "v"(d0), "v"(d1), "v"(d2), "v"(d3));
      }
      runL(13, 18);
      wait_vm<7>(); bar();
      wait_vm<0>(); bar();
      bar();
      wait_words(ws, zfl, l < 16, tag);
      early = true;
      ldA(0, sl2(0)); ldB(0, sl2(0)); ldA(1, sl2(1)); ldB(1, sl2(1));
      ldA(4, sl2(4)); ldA(5, sl2(5)); ldB(4, sl2(4)); ldB(5, sl2(5));
    }
    STAMP(2);
    if (!early) {
      wait_words(ws, zfl, l < 16, tag);
      ldB(0, sl2(0)); ldB(1, sl2(1)); ldB(4, sl2(4)); ldB(5, sl2(5));
    }
    STAMP(3);
    ldA(2, sl2(2)); ldA(3, sl2(3)); ldB(2, sl2(2)); ldB(3, sl2(3));
    wait_vm<16>(); bar();
    {
      unsigned sl = sl2(0);
      auto ldU = [&](int u) {
        ldA(2 * u, sl); ldB(2 * u, sl); ldA(2 * u + 1, sl + P2_SLOT); ldB(2 * u + 1, sl + P2_SLOT);
        sl += 2 * P2_SLOT; if (sl == lds0 + 6 * P2_SLOT) sl = lds0;
      };
      wait_vm<0>(); bar(); ldU(3);
#pragma unroll 1
      for (int u = 2; u < 14; ++u) {
        wait_vm<10>(); bar();
        if (u == 4) STAMP(8);
        if (u == 10) STAMP(9);
        ldU(u + 2);
      }
    }
    wait_vm<10>(); bar();
    wait_words(ws, WS_F1 + (((lw * 64 + l) >> 4) * 24 + ((l & 8) ? 8 : 0) + (l & 15)) * 64, true, tag);
    wait_vm<0>(); bar();
    if (b == 0 && tid == 512) st8u(ws, WS_EP, tag);
    STAMP(4);
    const char* gA3 = ws + WS_OH + (size_t)panel3 * 44 * 4096 + lw * 4096;
    const char* gB3 = ws + WS_WF + (size_t)cb3 * 44 * 5120 + lw * 5120;
    int r5 = cb3 + 2 * (bi & 3); r5 = r5 >= 10 ? r5 - 10 : r5 >= 5 ? r5 - 5 : r5;
    int r6 = cb3 + 2 * (bi & 3); r6 = r6 >= 12 ? r6 - 12 : r6 >= 6 ? r6 - 6 : r6;
    auto ur = [&](int u) {
      int v;
      if (u < 5) { v = u + r5; if (v >= 5) v -= 5; } else { v = u + r6; if (v >= 11) v -= 6; }
      return __builtin_amdgcn_readfirstlane(v);
    };
    auto ldB3 = [&](int u, unsigned sl) {
      gl4c(gB3 + ur(u) * 20480, sl + 16384 + lw * 5120, l16);
      glds16c(gB3 + ur(u) * 20480 + 4096, l16, sl + 16384 + lw * 5120 + 4096);
    };
    auto ldA3 = [&](int u, unsigned sl) { gl4c(gA3 + ur(u) * 16384, sl + lw * 4096, l16); };
    bar(); bar();
    ldB3(0, lds0); ldB3(1, lds0 + P3_SLOT); ldB3(2, lds0 + 2 * P3_SLOT);
    wait_words(ws, WS_FO + ((panel3 >> 1) * 8 + (l & 7)) * 64, l < 8, tag);
    ldA3(0, lds0); ldA3(1, lds0 + P3_SLOT); ldA3(2, lds0 + 2 * P3_SLOT);
    bar();
    ldA3(3, lds0 + 3 * P3_SLOT); ldB3(3, lds0 + 3 * P3_SLOT);
    wait_vm<17>(); bar();
    wait_vm<13>(); bar(); ldA3(4, lds0); ldB3(4, lds0);
    wait_vm<18>(); bar(); ldB3(5, lds0 + P3_SLOT);
    wait_vm<14>(); bar(); ldB3(6, lds0 + 2 * P3_SLOT);
    wait_vm<10>(); bar(); ldB3(7, lds0 + 3 * P3_SLOT);
    wait_words(ws, WS_F2 + (panel3 * 32 + (l & 31)) * 64, l < 32, tag);
    STAMP(5);
    ldA3(5, lds0 + P3_SLOT); ldA3(6, lds0 + 2 * P3_SLOT); ldA3(7, lds0 + 3 * P3_SLOT);
    wait_vm<8>(); bar(); ldA3(8, lds0); ldB3(8, lds0);
    wait_vm<13>(); bar(); ldA3(9, lds0 + P3_SLOT); ldB3(9, lds0 + P3_SLOT);
    wait_vm<18>(); bar(); ldA3(10, lds0 + 2 * P3_SLOT); ldB3(10, lds0 + 2 * P3_SLOT);
    wait_vm<18>(); bar(); wait_vm<9>(); bar(); wait_vm<0>(); bar();
    bar(); bar();
    STAMP(6);
    return;
  }

  const rsrc_t rws = __builtin_amdgcn_make_buffer_rsrc(ws, 0, (int)WS_END, 0x00020000);
  bf16x8 pf[3];
  if (isq) p1q_consumer(P, rws, w, panel1, cb1, lds0, tag, pf);
  else p1z_consumer(P, rws, w, panel1, cb1, lds0, tag);
  STAMP(2);

  {
    const int wt = w & 3, kh = w >> 2;
    const int rsw = ((wt * 16 + fr) >> 1) & 7;
    const unsigned ao0 = (unsigned)((wt * 16 + fr) * 128 + (((4 * kh + (g >> 1)) ^ rsw) << 4) + (g & 1) * 8);
    const unsigned ao1 = (unsigned)((wt * 16 + fr) * 128 + (((4 * kh + 2 + (g >> 1)) ^ rsw) << 4) + (g & 1) * 8);
    const unsigned bo = (unsigned)(8192 + kh * 6144 + l * 16);
    const bf16x8 zeros = {0, 0, 0, 0, 0, 0, 0, 0};
    f32x4 acc[6];
#pragma unroll
    for (int n = 0; n < 6; ++n) acc[n] = (f32x4){0.f, 0.f, 0.f, 0.f};
    bf16x8 af[2], bfr[2][6];
    af[1] = zeros;
#pragma unroll
    for (int n = 0; n < 6; ++n) bfr[1][n] = zeros;
    const int zi = (bi & 15) * 8 + xcd;
    const bool reducer = !isq && zi < 72;
    const int rj = zi & 7, rf = zi >> 3;
    const int row = panel2 * 64 + wt * 16 + fr;
    u32x2 zreg[6];
    const float bs = bf2f(P.bsp[row]);
    unsigned sl = lds0 + 4 * P2_SLOT;
    auto step = [&](int cur, int kt) {
      if (cur == 0) bar();
      af[cur] = lds_2b64(sl + ao0, sl + ao1);
#pragma unroll
      for (int n = 0; n < 6; ++n) bfr[cur][n] = lds_b128(sl + bo + n * 1024);
      SB0;
#pragma unroll
      for (int n = 0; n < 6; ++n) acc[n] = MFMA(bfr[cur ^ 1][n], af[cur ^ 1], acc[n]);
      SB0;
      sl += P2_SLOT; if (sl == lds0 + 6 * P2_SLOT) sl = lds0;
    };
    auto run = [&](int k0, int k1) {
#pragma unroll 1
      for (int kt = k0; kt < k1; kt += 2) { step(0, kt); step(1, kt + 1); }
    };
    if (reducer) {
      const int et = (rf + 1) >> 1, st = rf == 0 ? 0 : ((rf + 1) & 1) + (et >= 3 ? 1 : 0);
      wait_words(ws, WS_F1 + ((l & 15) * 24 + rj) * 64, true, tag);
      f32x4 pv[8];
      bool okv[4];
#pragma unroll
      for (int t4 = 0; t4 < 4; ++t4) {
        const int i = 2 * st + (t4 & 1);
        okv[t4] = i < 5 && (t4 >= 2 || (et - i <= 1 && i - et <= 1));
        const int job = okv[t4] ? (t4 >= 2 ? 13 + i : 2 * i + et) : 0;
#pragma unroll
        for (int pp = 0; pp < 2; ++pp)
          pv[t4 * 2 + pp] = __builtin_bit_cast(f32x4, ld16(rws, WS_KVP + (unsigned)(((rj * 18 + job) * 16 + w * 2 + pp) * 1024) + l * 16));
      }
      run(0, 2);
      if (tid == 0) st8u(ws, WS_F1 + (panel1 * 24 + 16 + cb1) * 64, tag);
      const unsigned L_R = lds0 + 122880;
      f32x4 sv[4];
#pragma unroll
      for (int t4 = 0; t4 < 4; ++t4) { sv[t4] = pv[t4 * 2] + pv[t4 * 2 + 1]; if (!okv[t4]) sv[t4] = (f32x4){0.f, 0.f, 0.f, 0.f}; }
      if (w >= 4) {
#pragma unroll
        for (int t4 = 0; t4 < 4; ++t4) stsf4(L_R + ((w - 4) * 4 + t4) * 1024 + l * 16, sv[t4]);
      }
      run(2, 4);
      if (w < 4) {
#pragma unroll
        for (int t4 = 0; t4 < 4; ++t4) stsf4(L_R + (w * 4 + t4) * 1024 + l * 16, sv[t4] + ldsf4(L_R + (w * 4 + t4) * 1024 + l * 16));
      }
      run(4, 6);
      if (w < 2) {
        f32x4 v[2];
#pragma unroll
        for (int h2 = 0; h2 < 2; ++h2) {
          v[h2] = (f32x4){0.f, 0.f, 0.f, 0.f};
#pragma unroll
          for (int ww = 0; ww < 4; ++ww) v[h2] += ldsf4(L_R + (ww * 4 + w * 2 + h2) * 1024 + l * 16);
          const int he = (et * 16 + fr) / 20;
#pragma unroll
          for (int r = 0; r < 4; ++r) { const int d = (2 * st + h2) * 16 + 4 * g + r; if (d / 20 != he) v[h2][r] = 0.f; }
        }
        st16(rws, WS_KVT + (unsigned)(((rj * 2 + w) * 9 + rf) * 1024) + l * 16,
             (u32x4){cvt_pk(v[0][0], v[0][1]), cvt_pk(v[0][2], v[0][3]), cvt_pk(v[1][0], v[1][1]), cvt_pk(v[1][2], v[1][3])});
      }
      run(6, 10);
      wait_vm<0>();
      if (w < 2 && l == 0) st8u(ws, WS_FK + (rj * 32 + w * 16 + rf) * 64, tag);
      run(10, 18);
    } else {
      run(0, 6);
      wait_vm<0>();
      run(6, 8);
      if (!isq && tid == 0) st8u(ws, WS_F1 + (panel1 * 24 + 16 + cb1) * 64, tag);
      run(8, 18);
    }
    if (isq) {
      const unsigned foff = (l & 15) < 9 ? WS_FK + (cb2 * 32 + (l & 31)) * 64 : WS_F1 + (panel1 * 24 + 16 + cb2) * 64;
      const u64 fv = ld8(ws, foff);
      run(18, 22);
      if (!__all(fv == tag)) wait_words(ws, foff, l < 32, tag);
#pragma unroll
      for (int i = 0; i < 3; ++i) {
        int ins = w + 8 * i; if (ins >= 18) ins = 17;
        ins = __builtin_amdgcn_readfirstlane(ins);
        glds16c(ws, WS_KVT + cb2 * 18432 + ins * 1024 + l * 16, __builtin_amdgcn_readfirstlane(lds0 + L_KVT + ins * 1024));
      }
      const unsigned zo = WS_Z1 + (unsigned)(((panel1 * 8 + cb2) * 4 + 2 + (wt >> 1)) * 6) * 1024 + l * 16 + (wt & 1) * 8;
#pragma unroll
      for (int n = 0; n < 6; ++n) zreg[n] = __builtin_amdgcn_raw_buffer_load_b64(rws, zo + n * 1024, 0, 16);
      run(22, 24);
      wait_vm<0>();
      run(24, 26);
      u32x2 ov[5];
#pragma unroll
      for (int et = 0; et < 5; ++et) {
        f32x4 num = {0.f, 0.f, 0.f, 0.f}, den = {0.f, 0.f, 0.f, 0.f};
        const int st0 = et >= 3 ? 1 : 0, nst = et == 0 ? 1 : 2, f0 = et == 0 ? 0 : 1 + (et - 1) * 2;
#pragma unroll
        for (int s = 0; s < nst; ++s) {
          bf16x8 ka = lds_b128(lds0 + L_KVT + (f0 + s) * 1024 + l * 16), za = lds_b128(lds0 + L_KVT + (9 + f0 + s) * 1024 + l * 16);
          num = MFMA(ka, pf[st0 + s], num); den = MFMA(za, pf[st0 + s], den);
        }
        float v[4];
#pragma unroll
        for (int j = 0; j < 4; ++j) v[j] = num[j] * __builtin_amdgcn_rcpf(den[j] + 1e-6f);
        ov[et] = (u32x2){cvt_pk(v[0], v[1]), cvt_pk(v[2], v[3])};
      }
      const unsigned ob = WS_OH + (unsigned)(((2 * panel1 + kh) * 44 + ((cb2 * 5) >> 1)) * 4 + wt) * 1024 + l * 16;
      if ((cb2 & 1) == 0) {
        st16(rws, ob, (u32x4){ov[0][0], ov[0][1], ov[1][0], ov[1][1]});
        st16(rws, ob + 4096, (u32x4){ov[2][0], ov[2][1], ov[3][0], ov[3][1]});
        st8(ws, ob + 8192, ov[4]);
      } else {
        st8(ws, ob + 8, ov[0]);
        st16(rws, ob + 4096, (u32x4){ov[1][0], ov[1][1], ov[2][0], ov[2][1]});
        st16(rws, ob + 8192, (u32x4){ov[3][0], ov[3][1], ov[4][0], ov[4][1]});
      }
      run(26, 30);
      wait_vm<0>();
      run(30, 32);
      if (tid == 0) st8u(ws, WS_FO + (panel1 * 8 + cb2) * 64, tag);
    } else {
      run(18, 32);
#pragma unroll
      for (int n = 0; n < 6; ++n) zreg[n] = *(LAS u32x2*)(uintptr_t)(lds0 + L_KVT + 6144 + ((wt >> 1) * 6 + n) * 1024 + l * 16 + (wt & 1) * 8);
    }
#pragma unroll
    for (int n = 0; n < 6; ++n) acc[n] = MFMA(bfr[1][n], af[1], acc[n]);
    STAMP(4);
    bar();
    const unsigned L_RED = lds0 + 3 * P3_SLOT;
    if (kh == 1) {
#pragma unroll
      for (int n = 0; n < 6; ++n) stsf4(L_RED + (wt * 6 + n) * 1024 + l * 16, acc[n]);
    }
    bar();
    if (kh == 0) {
#pragma unroll
      for (int n = 0; n < 6; ++n) acc[n] += ldsf4(L_RED + (wt * 6 + n) * 1024 + l * 16);
      u32x2 hv[6];
#pragma unroll
      for (int n = 0; n < 6; ++n) {
        f32x4 gg = acc[n] + bs;
        hv[n] = (u32x2){cvt_pk(lo16(zreg[n][0]) * gg[0], hi16(zreg[n][0]) * gg[1]), cvt_pk(lo16(zreg[n][1]) * gg[2], hi16(zreg[n][1]) * gg[3])};
      }
      const unsigned hb = WS_OH + (unsigned)((panel2 * 44 + 20 + cb2 * 3) * 4 + wt) * 1024 + l * 16;
#pragma unroll
      for (int s = 0; s < 3; ++s) st16(rws, hb + s * 4096, (u32x4){hv[2 * s][0], hv[2 * s][1], hv[2 * s + 1][0], hv[2 * s + 1][1]});
      wait_vm<0>();
      if (l == 0) st8u(ws, WS_F2 + ((panel2 * 8 + cb2) * 4 + wt) * 64, tag);
    }
    bar();
    STAMP(5);
  }
  {
    const int wsr = w & 3, kh = w >> 2;
    const unsigned ao = (unsigned)(kh * 8192 + wsr * 1024 + l * 16);
    const unsigned bb = 16384u + kh * 10240 + l * 16;
    const bf16x8 zeros = {0, 0, 0, 0, 0, 0, 0, 0};
    f32x4 acc[5];
#pragma unroll
    for (int n = 0; n < 5; ++n) acc[n] = (f32x4){0.f, 0.f, 0.f, 0.f};
    bf16x8 af[2][2], bfr[2][2][5];
#pragma unroll
    for (int s2 = 0; s2 < 2; ++s2) {
      af[1][s2] = zeros;
#pragma unroll
      for (int n = 0; n < 5; ++n) bfr[1][s2][n] = zeros;
    }
    u32x4 xv[2];
    const int c0 = tid, c1 = tid + 512;
    const size_t xo0 = (size_t)(panel3 * 64 + c0 / 10) * D_ + cb3 * 80 + (c0 % 10) * 8;
    const size_t xo1 = (size_t)(panel3 * 64 + c1 / 10) * D_ + cb3 * 80 + (c1 % 10) * 8;
    xv[0] = *(const u32x4*)(P.x + xo0);
    if (tid < 128) xv[1] = *(const u32x4*)(P.x + xo1);
    unsigned sb = lds0;
    auto mm = [&](int c) {
#pragma unroll
      for (int s2 = 0; s2 < 2; ++s2)
#pragma unroll
        for (int n = 0; n < 5; ++n) acc[n] = MFMA(bfr[c][s2][n], af[c][s2], acc[n]);
      SB0;
    };
    auto unit = [&](int c) {
      bar();
#pragma unroll
      for (int s2 = 0; s2 < 2; ++s2) {
        af[c][s2] = lds_b128(sb + ao + s2 * 4096);
#pragma unroll
        for (int n = 0; n < 5; ++n) bfr[c][s2][n] = lds_b128(sb + bb + s2 * 5120 + n * 1024);
      }
      SB0;
      mm(c ^ 1);
      sb += P3_SLOT; if (sb == lds0 + 4 * P3_SLOT) sb = lds0;
    };
#pragma unroll 1
    for (int u = 0; u < 5; ++u) { unit(0); unit(1); }
    unit(0);
    mm(0);
    STAMP(6);
    bar();
#pragma unroll
    for (int n = 0; n < 5; ++n) stsf4(lds0 + (unsigned)(((kh * 64 + wsr * 16 + fr) * 84 + n * 16 + 4 * g) * 4), acc[n]);
    bar();
#pragma unroll
    for (int i = 0; i < 2; ++i) {
      if (i == 0 || tid < 128) {
        const int c = tid + i * 512, r = c / 10, ch = c - r * 10;
        const unsigned a = lds0 + (unsigned)((r * 84 + ch * 8) * 4);
        f32x4 a0 = ldsf4(a) + ldsf4(a + 64 * 84 * 4), a1 = ldsf4(a + 16) + ldsf4(a + 64 * 84 * 4 + 16);
        u32x4 o = {cvt_pk(lo16(xv[i][0]) + a0[0], hi16(xv[i][0]) + a0[1]), cvt_pk(lo16(xv[i][1]) + a0[2], hi16(xv[i][1]) + a0[3]),
                   cvt_pk(lo16(xv[i][2]) + a1[0], hi16(xv[i][2]) + a1[1]), cvt_pk(lo16(xv[i][3]) + a1[2], hi16(xv[i][3]) + a1[3])};
        *(u32x4*)(P.out + (i == 0 ? xo0 : xo1)) = o;
      }
    }
    STAMP(7);
  }
}

extern "C" void kernel_launch(void* const* d_in, const int* in_sizes, int n_in, void* d_out, int out_size,
                              void* d_ws, size_t ws_size, hipStream_t stream) {
  Params P;
  P.x = (const u16*)d_in[0];
  P.wqkv = (const u16*)d_in[1];
  P.wo = (const u16*)d_in[2];
  P.win = (const u16*)d_in[4];
  P.wsp = (const u16*)d_in[5];
  P.bsp = (const u16*)d_in[6];
  P.wout = (const u16*)d_in[7];
  P.out = (u16*)d_out;
  P.ws = (char*)d_ws;
  (void)hipFuncSetAttribute((const void*)mega_kernel, hipFuncAttributeMaxDynamicSharedMemorySize, LDS_TOTAL);
  mega_kernel<<<256, 768, LDS_TOTAL, stream>>>(P);
}
```

```cpp
#include <hip/hip_runtime.h>
#include <hip/hip_bf16.h>
#include <stdint.h>

typedef __attribute__((ext_vector_type(8))) short bf16x8;
typedef __attribute__((ext_vector_type(4))) short bf16x4;
typedef __attribute__((ext_vector_type(4))) float f32x4;
typedef __attribute__((ext_vector_type(2))) float f32x2;
typedef __attribute__((ext_vector_type(2))) __bf16 bf2;
typedef __attribute__((ext_vector_type(4))) unsigned u32x4;
typedef __attribute__((ext_vector_type(2))) unsigned u32x2;
typedef unsigned short u16;
typedef unsigned long long u64;
#define LAS __attribute__((address_space(3)))
#define GAS __attribute__((address_space(1)))
#define DI __device__ __forceinline__
#define RLX_AGENT __ATOMIC_RELAXED, __HIP_MEMORY_SCOPE_AGENT

constexpr int S_ = 2048, D_ = 640, NQKV = 1920, FIN = 1536, FH = 768;

constexpr unsigned WS_PQ  = 0;
constexpr unsigned WS_Z1  = WS_PQ + 16 * 8 * 8 * 3 * 1024;
constexpr unsigned WS_Z2N = WS_Z1 + 16 * 8 * 24 * 1024;
constexpr unsigned WS_OH  = WS_Z2N + 8 * 64 * 6 * 1024;
constexpr unsigned WS_WF  = WS_OH + 32 * 44 * 4 * 1024;
constexpr unsigned WS_KVT = WS_WF + 8 * 44 * 5 * 1024;
constexpr unsigned WS_KVP = WS_KVT + 8 * 18 * 1024;
constexpr unsigned WS_GR  = WS_KVP + 8 * 18 * 16 * 1024;
constexpr unsigned WS_GX  = WS_GR + 16 * 8 * 256 * 8;
constexpr unsigned WS_GC  = WS_GX + 2048 * 16;
constexpr unsigned WS_F1  = WS_GC + 24 * 10 * 2048;
constexpr unsigned WS_F2  = WS_F1 + 16 * 24 * 64;
constexpr unsigned WS_FO  = WS_F2 + 1024 * 64;
constexpr unsigned WS_FK  = WS_FO + 256 * 64;
constexpr unsigned WS_EP  = WS_FK + 256 * 64;
constexpr unsigned WS_END = WS_EP + 64;

#define STAMP(i)

DI float bf2f(u16 v) { return __uint_as_float(((unsigned)v) << 16); }
DI unsigned cvt_pk(float lo, float hi) {
  bf2 r; r[0] = (__bf16)lo; r[1] = (__bf16)hi; return __builtin_bit_cast(unsigned, r);
}
DI float rbf(float x) { return (float)((__bf16)x); }
DI float lo16(unsigned u) { return __uint_as_float(u << 16); }
DI float hi16(unsigned u) { return __uint_as_float(u & 0xffff0000u); }

DI void glds16(const void* gbase, unsigned voff, unsigned lds_dst) {
  unsigned keep;
  asm volatile("s_mov_b32 %0, m0\n\ts_mov_b32 m0, %3\n\ts_nop 0\n\tglobal_load_lds_dwordx4 %1, %2\n\ts_mov_b32 m0, %0"
               : "=&s"(keep) : "v"(voff), "s"(gbase), "s"(lds_dst) : "memory");
}
DI void glds16c(const void* gbase, unsigned voff, unsigned lds_dst) {
  unsigned keep;
  asm volatile("s_mov_b32 %0, m0\n\ts_mov_b32 m0, %3\n\ts_nop 0\n\tglobal_load_lds_dwordx4 %1, %2 sc1\n\ts_mov_b32 m0, %0"
               : "=&s"(keep) : "v"(voff), "s"(gbase), "s"(lds_dst) : "memory");
}
#define GL_PRE "s_mov_b32 %0, m0\n\ts_mov_b32 m0, %2\n\ts_nop 0\n\t"
#define GL_POST "s_mov_b32 m0, %0"
DI void gl2(const void* gb, unsigned lds, unsigned v0, unsigned v1) {
  unsigned keep;
  asm volatile(GL_PRE "global_load_lds_dwordx4 %3, %1\n\tglobal_load_lds_dwordx4 %4, %1 offset:1024\n\t" GL_POST
               : "=&s"(keep) : "s"(gb), "s"(lds), "v"(v0), "v"(v1) : "memory");
}
DI void gl3(const void* gb, unsigned lds, unsigned v0, unsigned v1, unsigned v2) {
  unsigned keep;
  asm volatile(GL_PRE "global_load_lds_dwordx4 %3, %1\n\tglobal_load_lds_dwordx4 %4, %1 offset:1024\n\tglobal_load_lds_dwordx4 %5, %1 offset:2048\n\t" GL_POST
               : "=&s"(keep) : "s"(gb), "s"(lds), "v"(v0), "v"(v1), "v"(v2) : "memory");
}
DI void gl4(const void* gb, unsigned lds, unsigned v0, unsigned v1, unsigned v2, unsigned v3) {
  unsigned keep;
  asm volatile(GL_PRE "global_load_lds_dwordx4 %3, %1\n\tglobal_load_lds_dwordx4 %4, %1 offset:1024\n\tglobal_load_lds_dwordx4 %5, %1 offset:2048\n\tglobal_load_lds_dwordx4 %6, %1 offset:3072\n\t" GL_POST
               : "=&s"(keep) : "s"(gb), "s"(lds), "v"(v0), "v"(v1), "v"(v2), "v"(v3) : "memory");
}
DI void gl3c(const void* gb, unsigned lds, unsigned v0) {
  unsigned keep;
  asm volatile(GL_PRE "global_load_lds_dwordx4 %3, %1 sc1\n\tglobal_load_lds_dwordx4 %3, %1 offset:1024 sc1\n\tglobal_load_lds_dwordx4 %3, %1 offset:2048 sc1\n\t" GL_POST
               : "=&s"(keep) : "s"(gb), "s"(lds), "v"(v0) : "memory");
}
DI void gl4c(const void* gb, unsigned lds, unsigned v0) {
  unsigned keep;
  asm volatile(GL_PRE "global_load_lds_dwordx4 %3, %1 sc1\n\tglobal_load_lds_dwordx4 %3, %1 offset:1024 sc1\n\tglobal_load_lds_dwordx4 %3, %1 offset:2048 sc1\n\tglobal_load_lds_dwordx4 %3, %1 offset:3072 sc1\n\t" GL_POST
               : "=&s"(keep) : "s"(gb), "s"(lds), "v"(v0) : "memory");
}
template <int N> DI void wait_vm() { asm volatile("s_waitcnt vmcnt(%0)" :: "n"(N) : "memory"); }

DI bf16x8 lds_b128(unsigned addr) { return *(LAS bf16x8*)(uintptr_t)addr; }
DI bf16x8 lds_2b64(unsigned addr0, unsigned addr1) {
  bf16x4 a = *(LAS bf16x4*)(uintptr_t)addr0, b = *(LAS bf16x4*)(uintptr_t)addr1;
  return __builtin_shufflevector(a, b, 0, 1, 2, 3, 4, 5, 6, 7);
}
DI bf16x8 lds_tr(unsigned addr0, unsigned addr1) {
  bf16x4 a = __builtin_amdgcn_ds_read_tr16_b64_v4i16((LAS bf16x4*)(uintptr_t)addr0);
  bf16x4 b = __builtin_amdgcn_ds_read_tr16_b64_v4i16((LAS bf16x4*)(uintptr_t)addr1);
  return __builtin_shufflevector(a, b, 0, 1, 2, 3, 4, 5, 6, 7);
}
DI float ldsf(unsigned a) { return *(LAS float*)(uintptr_t)a; }
DI void stsf(unsigned a, float v) { *(LAS float*)(uintptr_t)a = v; }
DI f32x4 ldsf4(unsigned a) { return *(LAS f32x4*)(uintptr_t)a; }
DI void stsf4(unsigned a, f32x4 v) { *(LAS f32x4*)(uintptr_t)a = v; }
#define SB0 __builtin_amdgcn_sched_barrier(0)
#define MFMA(a, b, c) __builtin_amdgcn_mfma_f32_16x16x32_bf16((a), (b), (c), 0, 0, 0)

template <int CTRL> DI float dpp(float v) {
  return __builtin_bit_cast(float, __builtin_amdgcn_update_dpp(0, __builtin_bit_cast(int, v), CTRL, 0xf, 0xf, true));
}
DI float sum8(float v) { v += dpp<0xB1>(v); v += dpp<0x4E>(v); v += dpp<0x141>(v); return v; }
DI float sum16(float v) { v = sum8(v); v += dpp<0x140>(v); return v; }

DI float gelu_t(float x) {
  float u = x * (0.7978845608f + 0.0356774081f * x * x);
  float e = __builtin_amdgcn_exp2f(-2.885390082f * u);
  return x * __builtin_amdgcn_rcpf(1.0f + e);
}
DI f32x4 gelu4(f32x4 x) {
  const f32x4 a = x * ((x * x) * -0.1029432397f + -2.302208198f);
  f32x4 e;
#pragma unroll
  for (int j = 0; j < 4; ++j) e[j] = __builtin_amdgcn_exp2f(a[j]);
  e += 1.0f;
#pragma unroll
  for (int j = 0; j < 4; ++j) e[j] = __builtin_amdgcn_rcpf(e[j]);
  return x * e;
}
DI f32x4 phi4(f32x4 t) {
  f32x4 a, m;
#pragma unroll
  for (int j = 0; j < 4; ++j) { a[j] = fminf(t[j], 0.f); m[j] = fmaxf(t[j], 0.f); }
  a *= 1.4426950409f;
#pragma unroll
  for (int j = 0; j < 4; ++j) a[j] = __builtin_amdgcn_exp2f(a[j]);
  return (m + 1e-6f) + a;
}
DI float phi_f(float t) {
  return (fmaxf(t, 0.f) + 1e-6f) + __builtin_amdgcn_exp2f(fminf(t, 0.f) * 1.4426950409f);
}

typedef __amdgpu_buffer_rsrc_t rsrc_t;
DI void st16(rsrc_t r, unsigned off, u32x4 v) { __builtin_amdgcn_raw_buffer_store_b128(v, r, off, 0, 16); }
DI u32x4 ld16(rsrc_t r, unsigned off) { return __builtin_amdgcn_raw_buffer_load_b128(r, off, 0, 16); }
DI void st8(char* ws, unsigned off, u32x2 v) { __hip_atomic_store((GAS u64*)(ws + off), ((u64)v[1] << 32) | v[0], RLX_AGENT); }
DI void st8u(char* ws, unsigned off, u64 v) { __hip_atomic_store((GAS u64*)(ws + off), v, RLX_AGENT); }
DI void st4(char* ws, unsigned off, float v) { __hip_atomic_store((GAS unsigned*)(ws + off), __float_as_uint(v), RLX_AGENT); }
DI u64 ld8(const char* ws, unsigned off) { return __hip_atomic_load((GAS u64*)(ws + off), RLX_AGENT); }
constexpr unsigned SPIN_CAP = 1u << 20;
DI void wait_words(const char* ws, unsigned off, bool act, u64 tag) {
#pragma unroll 1
  for (unsigned sp = 0; sp < SPIN_CAP; ++sp) {
    u64 v = act ? ld8(ws, off) : tag;
    if (__all(v == tag)) break;
    __builtin_amdgcn_s_sleep(1);
  }
  __builtin_amdgcn_fence(__ATOMIC_ACQUIRE, "wavefront");
}

struct Params { const u16 *x, *wqkv, *wo, *win, *wsp, *bsp, *wout; u16* out; char* ws; };

constexpr unsigned KBS = 4224;
constexpr unsigned K1_STAGE = 16384 + 8 * KBS;
constexpr unsigned L_MISC = 153600;
constexpr int LDS_TOTAL = 163840;
constexpr unsigned P2_SLOT = 20480;
constexpr unsigned L_KVT = 135168;
constexpr unsigned P3_SLOT = 36864;

DI void bar() { asm volatile("s_waitcnt lgkmcnt(0)" ::: "memory"); __builtin_amdgcn_s_barrier(); asm volatile("" ::: "memory"); }

struct Side { u32x4 r0, r1; unsigned wv[8]; };
DI void side_issue(const Params& P, Side& S, int row, int w) {
  const int l = threadIdx.x & 63;
  const u32x4* src = (const u32x4*)(P.x + (size_t)row * D_);
  S.r0 = src[l]; S.r1 = src[64 + (l & 15)];
  int item = blockIdx.x; if (item > 219) item = 219;
  const int st = item / 5, T = (item - st * 5) * 8 + w;
  const u16* wsrc = (st < 20 ? P.wo + (size_t)st * 32 * D_ : P.wout + (size_t)(st - 20) * 32 * D_) + (size_t)(l >> 4) * 4 * D_ + T * 16 + (l & 15);
#pragma unroll
  for (int e = 0; e < 8; ++e) S.wv[e] = wsrc[(size_t)(16 * (e >> 2) + (e & 3)) * D_];
}
DI float rdl(float v, int lane) { return __builtin_bit_cast(float, __builtin_amdgcn_readlane(__builtin_bit_cast(int, v), lane)); }
DI void side_finish(const Params& P, rsrc_t rws, const Side& S, int row, int w, unsigned tl, unsigned th) {
  const int l = threadIdx.x & 63;
  float s = 0.f, q = 0.f, s2 = 0.f, q2 = 0.f;
#pragma unroll
  for (int j = 0; j < 4; ++j) {
    float a = lo16(S.r0[j]), b = hi16(S.r0[j]), c = lo16(S.r1[j]), d = hi16(S.r1[j]);
    s += a + b; q += a * a + b * b; s2 += c + d; q2 += c * c + d * d;
  }
  s = sum16(s); q = sum16(q); s2 = sum16(s2); q2 = sum16(q2);
  s = (rdl(s, 0) + rdl(s, 16)) + (rdl(s, 32) + rdl(s, 48)) + s2;
  q = (rdl(q, 0) + rdl(q, 16)) + (rdl(q, 32) + rdl(q, 48)) + q2;
  const float m = s * (1.f / D_);
  const float v = rbf(fmaxf(q * (1.f / D_) - m * m, 0.f));
  const float r = 1.f / rbf(sqrtf(rbf(v + 1e-5f)));
  if (l < 2) st8u(P.ws, WS_GX + row * 16 + l * 8, l == 0 ? (((u64)tl << 32) | __float_as_uint(-rbf(m) * r)) : (((u64)th << 32) | __float_as_uint(r)));
  int item = blockIdx.x; if (item > 219) item = 219;
  const int st = item / 5, T = (item - st * 5) * 8 + w, cb = T / 5, nt = T - cb * 5;
  st16(rws, WS_WF + (unsigned)(((cb * 44 + st) * 5 + nt) * 1024) + l * 16,
       (u32x4){S.wv[0] | (S.wv[1] << 16), S.wv[2] | (S.wv[3] << 16), S.wv[4] | (S.wv[5] << 16), S.wv[6] | (S.wv[7] << 16)});
}
DI void xstat_gather(const char* ws, int panel, unsigned L_ST, int tid, unsigned tg, u64 gv) {
  if (tid < 256) {
#pragma unroll 1
    for (unsigned sp = 0; sp < SPIN_CAP; ++sp) {
      if (__all((unsigned)(gv >> 32) == tg)) break;
      __builtin_amdgcn_s_sleep(1);
      gv = ld8(ws, WS_GX + panel * 2048 + tid * 8);
    }
    stsf(L_ST + (tid & 1) * 512 + (tid >> 1) * 4, __uint_as_float((unsigned)gv));
  }
}

DI void csum_issue(const char* ws, int blk, int t, u64 (&cv)[10]) {
#pragma unroll
  for (int k = 0; k < 10; ++k) cv[k] = ld8(ws, WS_GC + (blk * 10 + k) * 2048 + t * 8);
}
DI void csum_gather(const char* ws, int blk, int t, unsigned tl, u64 (&cv)[10], unsigned dst) {
#pragma unroll 1
  for (unsigned sp = 0; sp < SPIN_CAP; ++sp) {
    bool ok = true;
#pragma unroll
    for (int k = 0; k < 10; ++k) ok &= (unsigned)(cv[k] >> 32) == tl;
    if (__all(ok)) break;
    __builtin_amdgcn_s_sleep(1);
    csum_issue(ws, blk, t, cv);
  }
  float a = 0.f;
#pragma unroll
  for (int k = 0; k < 10; ++k) a += __uint_as_float((unsigned)cv[k]);
  stsf(dst + t * 4, a);
}
DI void csum_store(char* ws, unsigned off, f32x4 d, int fr, int g, unsigned tl) {
  const float v = fr == 0 ? d[0] : fr == 1 ? d[1] : fr == 2 ? d[2] : d[3];
  if (fr < 4) st8u(ws, off + (4 * g + fr) * 8, ((u64)tl << 32) | __float_as_uint(v));
}

DI void p1q_consumer(const Params& P, rsrc_t rws, int w, int panel, int cbi, unsigned lds0, u64 tag, bf16x8 (&pf)[3]) {
  char* ws = P.ws;
  const int tid = threadIdx.x, l = tid & 63;
  const int wm = w >> 2, wc = (w + 2 * wm) & 3;
  const int g = l >> 4, fr = l & 15, q = (l & 15) >> 2, p = l & 3, g1 = g & 1;
  const unsigned aoff0 = (unsigned)((wm * 64 + fr) * 128 + ((g ^ (l & 7)) << 4));
  const unsigned ttab = (unsigned)(0x4498E765DC32BA10ull >> (16 * wc)) & 0xffffu;
  int tile[4];
  unsigned bo[4];
#pragma unroll
  for (int n = 0; n < 4; ++n) {
    tile[n] = (ttab >> (4 * n)) & 15;
    bo[n] = 16384u + g * KBS + q * 480 + p * 8 + tile[n] * 32;
  }
  const unsigned tl = (unsigned)tag, th = tl * 0x9E3779B1u ^ (unsigned)(tag >> 32);
  Side S; side_issue(P, S, panel * 128 + cbi * 8 + w, w);
  f32x4 acc[4][4];
#pragma unroll
  for (int m = 0; m < 4; ++m)
#pragma unroll
    for (int n = 0; n < 4; ++n) acc[m][n] = (f32x4){0.f, 0.f, 0.f, 0.f};
  const short one = 0x3F80;
  const bf16x8 ones = {one, one, one, one, one, one, one, one};
  const bf16x8 zeros = {0, 0, 0, 0, 0, 0, 0, 0};

  bf16x8 af[2][4], bfr[2][4];
#pragma unroll
  for (int m = 0; m < 4; ++m) { af[1][m] = zeros; bfr[1][m] = zeros; }
  auto mm = [&](int ks) {
#pragma unroll
    for (int n = 0; n < 3; ++n)
#pragma unroll
      for (int m = 0; m < 4; ++m) acc[m][n] = MFMA(bfr[ks][n], af[ks][m], acc[m][n]);
    if (wc < 3) {
#pragma unroll
      for (int m = 0; m < 4; ++m) acc[m][3] = MFMA(bfr[ks][3], af[ks][m], acc[m][3]);
    }
  };
  auto step = [&](unsigned sb, int ks) {
#pragma unroll
    for (int n = 0; n < 4; ++n) bfr[ks][n] = lds_tr(sb + ks * 4 * KBS + bo[n], sb + ks * 4 * KBS + bo[n] + 1920);
#pragma unroll
    for (int m = 0; m < 4; ++m) af[ks][m] = lds_b128(sb + ((aoff0 ^ (ks * 64)) + m * 2048));
    SB0;
    mm(ks ^ 1);
    SB0;
  };
  unsigned sb = lds0;
  auto run = [&](int n) {
#pragma unroll 1
    for (int kt = 0; kt < n; ++kt) {
      bar();
      step(sb, 0);
      step(sb, 1);
      sb += K1_STAGE; if (sb == lds0 + 3 * K1_STAGE) sb = lds0;
    }
  };
  run(1);
  side_finish(P, rws, S, panel * 128 + cbi * 8 + w, w, tl, th);
  if (panel < 10) {
#pragma unroll
    for (int h = 0; h < 2; ++h) {
      const bf16x8 b0 = wm == 0 ? bfr[0][h] : bfr[0][2 + h], b1 = wm == 0 ? bfr[1][h] : bfr[1][2 + h];
      const int tt = wm == 0 ? tile[h] : tile[2 + h];
      f32x4 d = MFMA(b0, ones, ((f32x4){0.f, 0.f, 0.f, 0.f}));
      d = MFMA(b1, ones, d);
      csum_store(ws, WS_GC + (cbi * 10 + panel) * 2048 + tt * 128, d, fr, g, tl);
    }
  }
  run(9);
  mm(1);
  STAMP(1);
  bar();
  STAMP(10);
  const unsigned L_ST = lds0 + L_MISC, L_CS = lds0 + L_MISC + 5120;
  const unsigned L_K = lds0, L_V = L_K + 20480, L_Q = lds0 + 122880;
  float mr[4], rinv[4];
#pragma unroll
  for (int m = 0; m < 4; ++m) { const int rl = wm * 64 + m * 16 + fr; mr[m] = ldsf(L_ST + rl * 4); rinv[m] = ldsf(L_ST + 512 + rl * 4); }
#pragma unroll
  for (int n = 0; n < 4; ++n) {
    if (n < 3 || wc < 3) {
      const int t = tile[n];
      const f32x4 cs = ldsf4(L_CS + (t * 16 + 4 * g) * 4);
      unsigned dst, dstep;
      if (t < 5) { dst = L_Q + (wm * 12 + (t >> 1)) * 1024 + l * 16 + (t & 1) * 8; dstep = 3072; }
      else {
        const int kb = wm * 8 + (fr >> 3), pos = (fr & 7) ^ ((fr >> 3) << 2);
        dst = (t < 10 ? L_K + (t - 5) * 256 : L_V + (t - 10) * 256) + kb * 1280 + pos * 32 + g * 8; dstep = 2560;
      }
      if (t < 10) {
#pragma unroll
        for (int m = 0; m < 4; ++m) {
          f32x4 v = acc[m][n] * rinv[m] + cs * mr[m];
          v = phi4(v);
          *(LAS u32x2*)(uintptr_t)(dst + m * dstep) = (u32x2){cvt_pk(v[0], v[1]), cvt_pk(v[2], v[3])};
        }
      } else {
#pragma unroll
        for (int m = 0; m < 4; ++m) {
          f32x4 v = acc[m][n] * rinv[m] + cs * mr[m];
          *(LAS u32x2*)(uintptr_t)(dst + m * dstep) = (u32x2){cvt_pk(v[0], v[1]), cvt_pk(v[2], v[3])};
        }
      }
    }
  }
  STAMP(8);
  bar();
  {
    const unsigned troff = (unsigned)(g * 5 * 256 + ((4 * g1 + q) * 32) + p * 8);
    f32x4 d[3];
    int jb[3];
#pragma unroll
    for (int r = 0; r < 3; ++r) {
      int job = w + 8 * r; if (job >= 18) job = w;
      jb[r] = job;
      const int i = job < 13 ? (job + 1) / 3 : job - 13;
      const int j = job < 13 ? job - 2 * i : 0;
      d[r] = (f32x4){0.f, 0.f, 0.f, 0.f};
#pragma unroll
      for (int ks = 0; ks < 4; ++ks) {
        const unsigned a0 = L_K + troff + (ks * 20 + i) * 256, b0 = L_V + troff + (ks * 20 + j) * 256;
        const bf16x8 ka = lds_tr(a0, a0 ^ 128);
        bf16x8 vb = lds_tr(b0, b0 ^ 128);
        if (job >= 13) vb = ones;
        d[r] = MFMA(ka, vb, d[r]);
      }
    }
#pragma unroll
    for (int r = 0; r < 3; ++r)
      st16(rws, WS_KVP + (unsigned)(((cbi * 18 + jb[r]) * 16 + panel) * 1024) + l * 16, __builtin_bit_cast(u32x4, d[r]));
    pf[0] = lds_b128(L_Q + w * 3072 + l * 16); pf[1] = lds_b128(L_Q + w * 3072 + 1024 + l * 16);
    const u32x2 q2 = *(LAS u32x2*)(uintptr_t)(L_Q + w * 3072 + 2048 + l * 16);
    pf[2] = __builtin_bit_cast(bf16x8, ((u32x4){q2[0], q2[1], 0u, 0u}));
  }
  STAMP(9);
  wait_vm<0>();
  bar();
  if (tid == 0) st8u(ws, WS_F1 + (panel * 24 + cbi) * 64, tag);
}

DI void p1z_consumer(const Params& P, rsrc_t rws, int w, int panel, int cbj, unsigned lds0, u64 tag) {
  char* ws = P.ws;
  const int tid = threadIdx.x, l = tid & 63;
  const int wm = w & 3, wn = w >> 2;
  const int g = l >> 4, fr = l & 15, q = (l & 15) >> 2, p = l & 3, g1 = g & 1;
  const unsigned aoff0 = (unsigned)((wm * 32 + fr) * 128 + ((g ^ (l & 7)) << 4));
  unsigned bo[3];
  int ca[3];
#pragma unroll
  for (int n = 0; n < 3; ++n) {
    const int c = wn * 3 + n;
    ca[n] = c;
    int pc = c + g1; if (pc == 6) pc = 0;
    bo[n] = 16384u + g * 1536 + q * 192 + pc * 32 + p * 8;
  }
  const short one = 0x3F80;
  const bf16x8 ones = {one, one, one, one, one, one, one, one};
  const bf16x8 zeros = {0, 0, 0, 0, 0, 0, 0, 0};
  f32x4 accA[2][3], accB[2][3];
#pragma unroll
  for (int m = 0; m < 2; ++m)
#pragma unroll
    for (int n = 0; n < 3; ++n) { accA[m][n] = (f32x4){0.f, 0.f, 0.f, 0.f}; accB[m][n] = (f32x4){0.f, 0.f, 0.f, 0.f}; }
  const unsigned tl = (unsigned)tag, th = tl * 0x9E3779B1u ^ (unsigned)(tag >> 32), tg = (tid & 1) ? th : tl;
  bf16x8 af[2][2], bfr[2][3];
  af[1][0] = af[1][1] = zeros; bfr[1][0] = bfr[1][1] = bfr[1][2] = zeros;
  auto rd = [&](unsigned sb, int ks) {
#pragma unroll
    for (int m = 0; m < 2; ++m) af[ks][m] = lds_b128(sb + ((aoff0 ^ (ks * 64)) + m * 2048));
#pragma unroll
    for (int n = 0; n < 3; ++n) { const unsigned o = sb + ks * 4 * 1536 + bo[n]; bfr[ks][n] = lds_tr(o, o + 768); }
    SB0;
  };
  auto mmA = [&](int ks) {
#pragma unroll
    for (int n = 0; n < 3; ++n)
#pragma unroll
      for (int m = 0; m < 2; ++m) accA[m][n] = MFMA(af[ks][m], bfr[ks][n], accA[m][n]);
    SB0;
  };
  auto mmB = [&](int ks) {
#pragma unroll
    for (int n = 0; n < 3; ++n)
#pragma unroll
      for (int m = 0; m < 2; ++m) accB[m][n] = MFMA(bfr[ks][n], af[ks][m], accB[m][n]);
    SB0;
  };
  const unsigned L_ST = lds0 + L_MISC, L_CS = lds0 + L_MISC + 5120;
  const unsigned L_RS = lds0 + L_MISC + 2048;
  const unsigned L_FS = lds0 + L_MISC + 4096;
  unsigned sb = lds0;
  auto nxt = [&]() { sb += K1_STAGE; if (sb == lds0 + 3 * K1_STAGE) sb = lds0; };
  auto runA = [&](int n) {
#pragma unroll 1
    for (int kt = 0; kt < n; ++kt) { bar(); rd(sb, 0); mmA(1); rd(sb, 1); mmA(0); nxt(); }
  };
  auto runB = [&](int n) {
#pragma unroll 1
    for (int kt = 0; kt < n; ++kt) { bar(); rd(sb, 0); mmB(1); rd(sb, 1); mmB(0); nxt(); }
  };
  auto cpub = [&](int blk) {
    if (panel < 10 && wm == 0) {
#pragma unroll
      for (int n = 0; n < 3; ++n) {
        f32x4 d = MFMA(bfr[0][n], ones, ((f32x4){0.f, 0.f, 0.f, 0.f}));
        d = MFMA(bfr[1][n], ones, d);
        csum_store(ws, WS_GC + (blk * 10 + panel) * 2048 + ca[n] * 128, d, fr, g, tl);
      }
    }
  };
  Side S; side_issue(P, S, panel * 128 + 64 + cbj * 8 + w, w);
  runA(1);
  side_finish(P, rws, S, panel * 128 + 64 + cbj * 8 + w, w, tl, th);
  cpub(8 + cbj);
  runA(6);
  const int tc = tid - 256;
  {
    u64 cv[10];
    if (tid < 256) cv[0] = ld8(ws, WS_GX + panel * 2048 + tid * 8);
    else if (tc < 96) csum_issue(ws, 8 + cbj, tc, cv);
    runA(3);
    if (tid < 256) xstat_gather(ws, panel, L_ST, tid, tg, cv[0]);
    else if (tc < 96) csum_gather(ws, 8 + cbj, tc, tl, cv, L_CS);
  }
  bar(); rd(sb, 0); mmA(1); rd(sb, 1);
  cpub(16 + cbj);
  STAMP(8);
  {
    float cn[3];
#pragma unroll
    for (int n = 0; n < 3; ++n) cn[n] = ldsf(L_CS + (ca[n] * 16 + fr) * 4);
#pragma unroll
    for (int m = 0; m < 2; ++m) {
      const int rl = wm * 32 + m * 16 + 4 * g;
      const f32x4 mr = ldsf4(L_ST + rl * 4), rinv = ldsf4(L_ST + 512 + rl * 4);
      f32x4 rs = {0.f, 0.f, 0.f, 0.f}, rq = {0.f, 0.f, 0.f, 0.f};
#pragma unroll
      for (int n = 0; n < 3; ++n) {
        f32x4 v = accA[m][n] * rinv + mr * cn[n];
        v = gelu4(v);
        rs += v; rq += v * v;
        accA[m][n] = v;
      }
#pragma unroll
      for (int j = 0; j < 4; ++j) { rs[j] = sum16(rs[j]); rq[j] = sum16(rq[j]); }
      if (fr == 0) {
        stsf4(L_RS + ((wn * 128 + rl) * 2) * 4, (f32x4){rs[0], rq[0], rs[1], rq[1]});
        stsf4(L_RS + ((wn * 128 + rl) * 2 + 4) * 4, (f32x4){rs[2], rq[2], rs[3], rq[3]});
      }
    }
  }
  mmB(0); nxt();
  bar(); rd(sb, 0);
  float part = 0.f;
  const unsigned gbase = WS_GR + (unsigned)(panel * 8) * 2048 + tid * 8;
  if (tid < 256) {
    part = ldsf(L_RS + tid * 4) + ldsf(L_RS + 1024 + tid * 4);
    st8u(ws, gbase + cbj * 2048, ((u64)tg << 32) | __float_as_uint(part));
  }
  mmB(1); rd(sb, 1); mmB(0); nxt();
  STAMP(9);
  runB(1);
  {
    u64 cv[10];
    if (tid < 256) {
#pragma unroll
      for (int i = 1; i < 8; ++i) cv[i] = ld8(ws, gbase + ((cbj + i) & 7) * 2048);
    }
    runB(1);
    if (tid < 256) {
      float tot;
#pragma unroll 1
      for (unsigned sp = 0; sp < SPIN_CAP; ++sp) {
        bool ok = true; tot = part;
#pragma unroll
        for (int i = 1; i < 8; ++i) { ok &= (unsigned)(cv[i] >> 32) == tg; tot += __uint_as_float((unsigned)cv[i]); }
        if (__all(ok)) break;
#pragma unroll
        for (int i = 1; i < 8; ++i) cv[i] = ld8(ws, gbase + ((cbj + i) & 7) * 2048);
      }
      float other = dpp<0xB1>(tot);
      if ((tid & 1) == 0) {
        float m = tot * (1.f / FH);
        float v = rbf(fmaxf(other * (1.f / FH) - m * m, 0.f));
        float r = 1.f / rbf(sqrtf(rbf(v + 1e-5f)));
        stsf(L_FS + (tid >> 1) * 4, -rbf(m) * r);
        stsf(L_FS + 512 + (tid >> 1) * 4, r);
      }
    }
  }
  bar(); rd(sb, 0);
  {
    f32x4 mr[2], ri[2];
#pragma unroll
    for (int m = 0; m < 2; ++m) { const int rl = wm * 32 + m * 16 + 4 * g; mr[m] = ldsf4(L_FS + rl * 4); ri[m] = ldsf4(L_FS + 512 + rl * 4); }
#pragma unroll
    for (int n = 0; n < 3; ++n) {
      f32x4 a = accA[0][n] * ri[0] + mr[0], b = accA[1][n] * ri[1] + mr[1];
      st16(rws, WS_Z2N + (unsigned)(((cbj * 64 + panel * 4 + wm) * 6 + ca[n]) * 1024) + l * 16,
           (u32x4){cvt_pk(a[0], a[1]), cvt_pk(a[2], a[3]), cvt_pk(b[0], b[1]), cvt_pk(b[2], b[3])});
    }
  }
  mmB(1); rd(sb, 1); mmB(0); nxt();
  STAMP(10);
  runB(2);
  wait_vm<0>();
  bar();
  if (tid == 0) st8u(ws, WS_F1 + (panel * 24 + 8 + cbj) * 64, tag);
  STAMP(11);
  rd(sb, 0); mmB(1); rd(sb, 1); mmB(0); nxt();
  {
    u64 cv[10];
    if (tid >= 256 && tc < 96) csum_issue(ws, 16 + cbj, tc, cv);
    runB(2);
    if (tid >= 256 && tc < 96) csum_gather(ws, 16 + cbj, tc, tl, cv, L_CS + 512);
  }
  mmB(1);
  STAMP(1);
  bar();
  {
    f32x4 cn[3];
#pragma unroll
    for (int n = 0; n < 3; ++n) { const unsigned a = L_CS + 512 + (ca[n] * 16 + 4 * g) * 4; cn[n] = ldsf4(a); }
    float mr[2], rinv[2];
#pragma unroll
    for (int m = 0; m < 2; ++m) { const int rl = wm * 32 + m * 16 + fr; mr[m] = ldsf(L_ST + rl * 4); rinv[m] = ldsf(L_ST + 512 + rl * 4); }
#pragma unroll
    for (int n = 0; n < 3; ++n) {
      u32x4 o;
#pragma unroll
      for (int m = 0; m < 2; ++m) {
        f32x4 v = accB[m][n] * rinv[m] + cn[n] * mr[m];
        v = gelu4(v);
        o[2 * m] = cvt_pk(v[0], v[1]); o[2 * m + 1] = cvt_pk(v[2], v[3]);
      }
      if (wm < 2) *(LAS u32x4*)(uintptr_t)(lds0 + L_KVT + 6144 + (wm * 6 + ca[n]) * 1024 + l * 16) = o;
      else st16(rws, WS_Z1 + (unsigned)(((panel * 8 + cbj) * 4 + wm) * 6 + ca[n]) * 1024 + l * 16, o);
    }
  }
}

__global__ __launch_bounds__(768, 3) void mega_kernel(Params P) {
  extern __shared__ __attribute__((aligned(16))) char smem[];
  const unsigned lds0 = (unsigned)(uintptr_t)(LAS char*)smem;
  char* ws = P.ws;
  const int tid = threadIdx.x, l = tid & 63;
  const int w = __builtin_amdgcn_readfirstlane(tid >> 6);
  const int g = l >> 4, fr = l & 15;
  const int b = blockIdx.x, xcd = b & 7, bi = b >> 3;
  const int panel1 = (xcd >> 1) * 4 + (bi & 3), cb1 = (xcd & 1) * 4 + ((bi >> 2) & 3);
  const bool isq = bi < 16;
  const int panel2 = 2 * panel1 + (isq ? 1 : 0), cb2 = cb1;
  const int panel3 = xcd * 4 + (bi & 3), cb3 = bi >> 2;
  STAMP(0);
  const u64 tag = ld8(ws, WS_EP) + 1;
  if (w == 7 && bi < 21) {
    u64 pc; asm volatile("s_getpc_b64 %0" : "=s"(pc));
    gl2((const char*)(pc & ~127ull) + bi * 2048, lds0 + 3 * K1_STAGE, l * 16, l * 16);
  }

  if (w >= 8) {
    const int lw = w - 8;
    const unsigned vA0 = (unsigned)(((l >> 3) * D_ + (((l & 7) ^ (l >> 3)) << 3)) * 2);
    const unsigned vA1 = vA0 + 8 * D_ * 2 - 1024, vA2 = vA0 + 16 * D_ * 2 - 2048, vA3 = vA0 + 24 * D_ * 2 - 3072;
    const char* gA = (const char*)(P.x + (size_t)(panel1 * 128 + lw * 32) * D_);
    const unsigned v20 = (unsigned)(((l >> 3) * S_ + (((l & 7) ^ (l >> 4)) << 3)) * 2);
    const unsigned v21 = (unsigned)((((l >> 3) + 8) * S_ + (((l & 7) ^ ((l >> 4) | 4)) << 3)) * 2) - 1024;
    const char* gA2 = (const char*)(P.wsp + (size_t)(panel2 * 64 + lw * 16) * S_);
    const int rot2 = 0;
    auto ldA = [&](int kt, unsigned sl) { gl2(gA2 + ((kt + rot2) & 31) * 128, sl + lw * 2048, v20, v21); };
    auto sl2 = [&](int kt) { return lds0 + ((kt + 4) % 6) * P2_SLOT; };
    const unsigned tl = (unsigned)tag, th = tl * 0x9E3779B1u ^ (unsigned)(tag >> 32);
    const int lt = tid - 512;
    auto gather = [&](int blk, int ncol, unsigned dst, bool xs) {
      u64 cv[10]; u64 gv = 0;
      const int tc = lt < ncol ? lt : 0;
      if (xs) gv = ld8(ws, WS_GX + panel1 * 2048 + lt * 8);
      csum_issue(ws, blk, tc, cv);
      if (xs) xstat_gather(ws, panel1, lds0 + L_MISC, lt, (lt & 1) ? th : tl, gv);
      csum_gather(ws, blk, tc, tl, cv, dst);
    };
    const unsigned l16 = l * 16;
    const char* gB2 = ws + WS_Z2N + (size_t)cb2 * 64 * 6144 + lw * 3072;
    auto ldB = [&](int kt, unsigned sl) { gl3c(gB2 + ((kt + rot2) & 31) * 12288, sl + 8192 + lw * 3072, l16); };
    const unsigned zfl = WS_F1 + ((l & 15) * 24 + 8 + cb2) * 64;
    auto try_z = [&]() {
      const u64 v = l < 16 ? ld8(ws, zfl) : tag;
      return (bool)__builtin_amdgcn_readfirstlane((int)__all(v == tag));
    };
    bool early = false;
    const int rot = panel1 < 10 ? panel1 : 0;
    auto krot = [&](int kt) { const int k = kt >= 10 ? kt - 10 : kt; return __builtin_amdgcn_readfirstlane(k == 0 ? rot : (k - 1 < rot ? k - 1 : k)); };
    if (isq) {
      gl4(gA + rot * 128, lds0 + lw * 4096, vA0, vA1, vA2, vA3);
      unsigned vB[4];
#pragma unroll
      for (int i = 0; i < 4; ++i) {
        int c = i * 64 + l; if (c > 239) c = 239;
        const int row = c / 30, cc = c - row * 30, seg = cc / 10, ch = cc - seg * 10;
        vB[i] = (unsigned)((row * NQKV + seg * 640 + cb1 * 80 + ch * 8) * 2) + 4096 - i * 1024;
      }
      const char* gB = (const char*)P.wqkv + (size_t)lw * 16 * NQKV * 2 - 4096;
      auto stageB = [&](int kt, unsigned sb) {
        const char* bp = gB + (size_t)krot(kt) * 64 * NQKV * 2;
        gl4(bp, sb + 16384 + lw * 2 * KBS, vB[0], vB[1], vB[2], vB[3]);
        gl4(bp + 8 * NQKV * 2, sb + 16384 + (lw * 2 + 1) * KBS, vB[0], vB[1], vB[2], vB[3]);
      };
      auto stage = [&](int kt, unsigned sb) { gl4(gA + krot(kt) * 128, sb + lw * 4096, vA0, vA1, vA2, vA3); stageB(kt, sb); };
      stageB(0, lds0); stage(1, lds0 + K1_STAGE);
      wait_vm<12>(); bar();
      stage(2, lds0 + 2 * K1_STAGE);
      unsigned sb = lds0, sc = lds0 + K1_STAGE;
#pragma unroll 1
      for (int kt = 1; kt < 8; ++kt) {
        wait_vm<12>(); bar();
        if (kt == 2) STAMP(10);
        if (kt == 7) STAMP(11);
        stage(kt + 2, sb);
        sb = sc; sc += K1_STAGE; if (sc == lds0 + 3 * K1_STAGE) sc = lds0;
      }
      wait_vm<12>(); bar();
      gather(cb1, 240, lds0 + L_MISC + 5120, true);
      bar();
      ldA(0, sl2(0)); ldA(1, sl2(1)); ldA(5, sl2(5));
      bar();
      ldA(4, sl2(4));
#pragma unroll 1
      for (int t = 0; t < 3 && !early; ++t) early = try_z();
      if (early) { ldB(0, sl2(0)); ldB(1, sl2(1)); ldB(4, sl2(4)); ldB(5, sl2(5)); }
      bar();
      if (!early) { early = try_z(); if (early) { ldB(0, sl2(0)); ldB(1, sl2(1)); ldB(4, sl2(4)); ldB(5, sl2(5)); } }
      bar();
    } else {
      gl4(gA + rot * 128, lds0 + lw * 4096, vA0, vA1, vA2, vA3);
      unsigned vB[3];
#pragma unroll
      for (int i = 0; i < 3; ++i) {
        const int c = i * 64 + l, row = c / 12, cc = c - row * 12;
        int pc = (cc >> 1) - (row >> 3); if (pc < 0) pc = 5;
        vB[i] = (unsigned)((row * FIN + cb1 * 96 + pc * 16 + (cc & 1) * 8) * 2) + 4096 - i * 1024;
      }
      const char* gB = (const char*)P.win + (size_t)lw * 16 * FIN * 2 - 4096;
      auto stageB = [&](int kt, unsigned sb) {
        const int k = krot(kt);
        gl3(gB + (kt < 10 ? FH * 2 : 0) + (size_t)k * 64 * FIN * 2, sb + 16384 + lw * 3072, vB[0], vB[1], vB[2]);
      };
      auto stage = [&](int kt, unsigned sb) {
        const int k = krot(kt);
        gl4(gA + k * 128, sb + lw * 4096, vA0, vA1, vA2, vA3); stageB(kt, sb);
      };
      stageB(0, lds0); stage(1, lds0 + K1_STAGE);
      wait_vm<7>(); bar();
      stage(2, lds0 + 2 * K1_STAGE);
      unsigned sb = lds0, sc = lds0 + K1_STAGE;
      auto runL = [&](int k0, int k1) {
#pragma unroll 1
        for (int kt = k0; kt < k1; ++kt) {
          wait_vm<7>(); bar();
          stage(kt + 2, sb);
          sb = sc; sc += K1_STAGE; if (sc == lds0 + 3 * K1_STAGE) sc = lds0;
        }
      };
      runL(1, 11);
      {
        const char* wp = (const char*)(P.wsp + (size_t)(panel1 * 128 + lw * 32) * S_) + (cb1 & 3) * 1024;
        const unsigned vo = (unsigned)((l >> 3) * S_ * 2 + (l & 7) * 128);
        unsigned d0, d1, d2, d3;
        asm volatile("global_load_dword %0, %4, %5\n\tglobal_load_dword %1, %4, %6\n\tglobal_load_dword %2, %4, %7\n\tglobal_load_dword %3, %4, %8"
                     : "=&v"(d0), "=&v"(d1), "=&v"(d2), "=&v"(d3)
                     : "v"(vo), "s"(wp), "s"(wp + 8 * S_ * 2), "s"(wp + 16 * S_ * 2), "s"(wp + 24 * S_ * 2) : "memory");
        runL(11, 13);
        asm volatile("" :: "v"(d0), "v"(d1), "v"(d2), "v"(d3));
      }
      runL(13, 18);
      wait_vm<7>(); bar();
      wait_vm<0>(); bar();
      bar();
      wait_words(ws, zfl, l < 16, tag);
      early = true;
      ldA(0, sl2(0)); ldB(0, sl2(0)); ldA(1, sl2(1)); ldB(1, sl2(1));
      ldA(4, sl2(4)); ldA(5, sl2(5)); ldB(4, sl2(4)); ldB(5, sl2(5));
    }
    STAMP(2);
    if (!early) {
      wait_words(ws, zfl, l < 16, tag);
      ldB(0, sl2(0)); ldB(1, sl2(1)); ldB(4, sl2(4)); ldB(5, sl2(5));
    }
    STAMP(3);
    ldA(2, sl2(2)); ldA(3, sl2(3)); ldB(2, sl2(2)); ldB(3, sl2(3));
    wait_vm<16>(); bar();
    {
      unsigned sl = sl2(0);
      auto ldU = [&](int u) {
        ldA(2 * u, sl); ldB(2 * u, sl); ldA(2 * u + 1, sl + P2_SLOT); ldB(2 * u + 1, sl + P2_SLOT);
        sl += 2 * P2_SLOT; if (sl == lds0 + 6 * P2_SLOT) sl = lds0;
      };
      wait_vm<0>(); bar(); ldU(3);
#pragma unroll 1
      for (int u = 2; u < 14; ++u) {
        wait_vm<10>(); bar();
        if (u == 4) STAMP(8);
        if (u == 10) STAMP(9);
        ldU(u + 2);
      }
    }
    wait_vm<10>(); bar();
    wait_words(ws, WS_F1 + (((lw * 64 + l) >> 4) * 24 + ((l & 8) ? 8 : 0) + (l & 15)) * 64, true, tag);
    wait_vm<0>(); bar();
    if (b == 0 && tid == 512) st8u(ws, WS_EP, tag);
    STAMP(4);
    const char* gA3 = ws + WS_OH + (size_t)panel3 * 44 * 4096 + lw * 4096;
    const char* gB3 = ws + WS_WF + (size_t)cb3 * 44 * 5120 + lw * 5120;
    int r5 = cb3 + 2 * (bi & 3); r5 = r5 >= 10 ? r5 - 10 : r5 >= 5 ? r5 - 5 : r5;
    int r6 = cb3 + 2 * (bi & 3); r6 = r6 >= 12 ? r6 - 12 : r6 >= 6 ? r6 - 6 : r6;
    auto ur = [&](int u) {
      int v;
      if (u < 5) { v = u + r5; if (v >= 5) v -= 5; } else { v = u + r6; if (v >= 11) v -= 6; }
      return __builtin_amdgcn_readfirstlane(v);
    };
    auto ldB3 = [&](int u, unsigned sl) {
      gl4c(gB3 + ur(u) * 20480, sl + 16384 + lw * 5120, l16);
      glds16c(gB3 + ur(u) * 20480 + 4096, l16, sl + 16384 + lw * 5120 + 4096);
    };
    auto ldA3 = [&](int u, unsigned sl) { gl4c(gA3 + ur(u) * 16384, sl + lw * 4096, l16); };
    bar(); bar();
    ldB3(0, lds0); ldB3(1, lds0 + P3_SLOT); ldB3(2, lds0 + 2 * P3_SLOT);
    wait_words(ws, WS_FO + ((panel3 >> 1) * 8 + (l & 7)) * 64, l < 8, tag);
    ldA3(0, lds0); ldA3(1, lds0 + P3_SLOT); ldA3(2, lds0 + 2 * P3_SLOT);
    bar();
    ldA3(3, lds0 + 3 * P3_SLOT); ldB3(3, lds0 + 3 * P3_SLOT);
    wait_vm<17>(); bar();
    wait_vm<13>(); bar(); ldA3(4, lds0); ldB3(4, lds0);
    wait_vm<18>(); bar(); ldB3(5, lds0 + P3_SLOT);
    wait_vm<14>(); bar(); ldB3(6, lds0 + 2 * P3_SLOT);
    wait_vm<10>(); bar(); ldB3(7, lds0 + 3 * P3_SLOT);
    wait_words(ws, WS_F2 + (panel3 * 32 + (l & 31)) * 64, l < 32, tag);
    STAMP(5);
    ldA3(5, lds0 + P3_SLOT); ldA3(6, lds0 + 2 * P3_SLOT); ldA3(7, lds0 + 3 * P3_SLOT);
    wait_vm<8>(); bar(); ldA3(8, lds0); ldB3(8, lds0);
    wait_vm<13>(); bar(); ldA3(9, lds0 + P3_SLOT); ldB3(9, lds0 + P3_SLOT);
    wait_vm<18>(); bar(); ldA3(10, lds0 + 2 * P3_SLOT); ldB3(10, lds0 + 2 * P3_SLOT);
    wait_vm<18>(); bar(); wait_vm<9>(); bar(); wait_vm<0>(); bar();
    bar(); bar();
    STAMP(6);
    return;
  }

  const rsrc_t rws = __builtin_amdgcn_make_buffer_rsrc(ws, 0, (int)WS_END, 0x00020000);
  bf16x8 pf[3];
  if (isq) p1q_consumer(P, rws, w, panel1, cb1, lds0, tag, pf);
  else p1z_consumer(P, rws, w, panel1, cb1, lds0, tag);
  STAMP(2);

  u32x4 xv[2];
  unsigned xo0, xo1, la0, la1;
  {
    const int wt = w & 3, kh = w >> 2;
    const int rsw = ((wt * 16 + fr) >> 1) & 7;
    const unsigned ao0 = (unsigned)((wt * 16 + fr) * 128 + (((4 * kh + (g >> 1)) ^ rsw) << 4) + (g & 1) * 8);
    const unsigned ao1 = (unsigned)((wt * 16 + fr) * 128 + (((4 * kh + 2 + (g >> 1)) ^ rsw) << 4) + (g & 1) * 8);
    const unsigned bo = (unsigned)(8192 + kh * 6144 + l * 16);
    const bf16x8 zeros = {0, 0, 0, 0, 0, 0, 0, 0};
    f32x4 acc[6];
#pragma unroll
    for (int n = 0; n < 6; ++n) acc[n] = (f32x4){0.f, 0.f, 0.f, 0.f};
    bf16x8 af[2], bfr[2][6];
    af[1] = zeros;
#pragma unroll
    for (int n = 0; n < 6; ++n) bfr[1][n] = zeros;
    const int zi = (bi & 15) * 8 + xcd;
    const bool reducer = !isq && zi < 72;
    const int rj = zi & 7, rf = zi >> 3;
    const int row = panel2 * 64 + wt * 16 + fr;
    u32x2 zreg[6];
    const float bs = bf2f(P.bsp[row]);
    unsigned sl = lds0 + 4 * P2_SLOT;
    auto step = [&](int cur, int kt) {
      if (cur == 0) bar();
      af[cur] = lds_2b64(sl + ao0, sl + ao1);
#pragma unroll
      for (int n = 0; n < 6; ++n) bfr[cur][n] = lds_b128(sl + bo + n * 1024);
      SB0;
#pragma unroll
      for (int n = 0; n < 6; ++n) acc[n] = MFMA(bfr[cur ^ 1][n], af[cur ^ 1], acc[n]);
      SB0;
      sl += P2_SLOT; if (sl == lds0 + 6 * P2_SLOT) sl = lds0;
    };
    auto run = [&](int k0, int k1) {
#pragma unroll 1
      for (int kt = k0; kt < k1; kt += 2) { step(0, kt); step(1, kt + 1); }
    };
    if (reducer) {
      const int et = (rf + 1) >> 1, st = rf == 0 ? 0 : ((rf + 1) & 1) + (et >= 3 ? 1 : 0);
      wait_words(ws, WS_F1 + ((l & 15) * 24 + rj) * 64, true, tag);
      f32x4 pv[8];
      bool okv[4];
#pragma unroll
      for (int t4 = 0; t4 < 4; ++t4) {
        const int i = 2 * st + (t4 & 1);
        okv[t4] = i < 5 && (t4 >= 2 || (et - i <= 1 && i - et <= 1));
        const int job = okv[t4] ? (t4 >= 2 ? 13 + i : 2 * i + et) : 0;
#pragma unroll
        for (int pp = 0; pp < 2; ++pp)
          pv[t4 * 2 + pp] = __builtin_bit_cast(f32x4, ld16(rws, WS_KVP + (unsigned)(((rj * 18 + job) * 16 + w * 2 + pp) * 1024) + l * 16));
      }
      run(0, 2);
      if (tid == 0) st8u(ws, WS_F1 + (panel1 * 24 + 16 + cb1) * 64, tag);
      const unsigned L_R = lds0 + 122880;
      f32x4 sv[4];
#pragma unroll
      for (int t4 = 0; t4 < 4; ++t4) { sv[t4] = pv[t4 * 2] + pv[t4 * 2 + 1]; if (!okv[t4]) sv[t4] = (f32x4){0.f, 0.f, 0.f, 0.f}; }
      if (w >= 4) {
#pragma unroll
        for (int t4 = 0; t4 < 4; ++t4) stsf4(L_R + ((w - 4) * 4 + t4) * 1024 + l * 16, sv[t4]);
      }
      run(2, 4);
      if (w < 4) {
#pragma unroll
        for (int t4 = 0; t4 < 4; ++t4) stsf4(L_R + (w * 4 + t4) * 1024 + l * 16, sv[t4] + ldsf4(L_R + (w * 4 + t4) * 1024 + l * 16));
      }
      run(4, 6);
      if (w < 2) {
        f32x4 v[2];
#pragma unroll
        for (int h2 = 0; h2 < 2; ++h2) {
          v[h2] = (f32x4){0.f, 0.f, 0.f, 0.f};
#pragma unroll
          for (int ww = 0; ww < 4; ++ww) v[h2] += ldsf4(L_R + (ww * 4 + w * 2 + h2) * 1024 + l * 16);
          const int he = (et * 16 + fr) / 20;
#pragma unroll
          for (int r = 0; r < 4; ++r) { const int d = (2 * st + h2) * 16 + 4 * g + r; if (d / 20 != he) v[h2][r] = 0.f; }
        }
        st16(rws, WS_KVT + (unsigned)(((rj * 2 + w) * 9 + rf) * 1024) + l * 16,
             (u32x4){cvt_pk(v[0][0], v[0][1]), cvt_pk(v[0][2], v[0][3]), cvt_pk(v[1][0], v[1][1]), cvt_pk(v[1][2], v[1][3])});
      }
      run(6, 10);
      wait_vm<0>();
      if (w < 2 && l == 0) st8u(ws, WS_FK + (rj * 32 + w * 16 + rf) * 64, tag);
      run(10, 18);
    } else {
      run(0, 6);
      wait_vm<0>();
      run(6, 8);
      if (!isq && tid == 0) st8u(ws, WS_F1 + (panel1 * 24 + 16 + cb1) * 64, tag);
      run(8, 18);
    }
    if (isq) {
      const unsigned foff = (l & 15) < 9 ? WS_FK + (cb2 * 32 + (l & 31)) * 64 : WS_F1 + (panel1 * 24 + 16 + cb2) * 64;
      const u64 fv = ld8(ws, foff);
      run(18, 22);
      if (!__all(fv == tag)) wait_words(ws, foff, l < 32, tag);
#pragma unroll
      for (int i = 0; i < 3; ++i) {
        int ins = w + 8 * i; if (ins >= 18) ins = 17;
        ins = __builtin_amdgcn_readfirstlane(ins);
        glds16c(ws, WS_KVT + cb2 * 18432 + ins * 1024 + l * 16, __builtin_amdgcn_readfirstlane(lds0 + L_KVT + ins * 1024));
      }
      const unsigned zo = WS_Z1 + (unsigned)(((panel1 * 8 + cb2) * 4 + 2 + (wt >> 1)) * 6) * 1024 + l * 16 + (wt & 1) * 8;
#pragma unroll
      for (int n = 0; n < 6; ++n) zreg[n] = __builtin_amdgcn_raw_buffer_load_b64(rws, zo + n * 1024, 0, 16);
      run(22, 24);
      wait_vm<0>();
      run(24, 26);
      u32x2 ov[5];
#pragma unroll
      for (int et = 0; et < 5; ++et) {
        f32x4 num = {0.f, 0.f, 0.f, 0.f}, den = {0.f, 0.f, 0.f, 0.f};
        const int st0 = et >= 3 ? 1 : 0, nst = et == 0 ? 1 : 2, f0 = et == 0 ? 0 : 1 + (et - 1) * 2;
#pragma unroll
        for (int s = 0; s < nst; ++s) {
          bf16x8 ka = lds_b128(lds0 + L_KVT + (f0 + s) * 1024 + l * 16), za = lds_b128(lds0 + L_KVT + (9 + f0 + s) * 1024 + l * 16);
          num = MFMA(ka, pf[st0 + s], num); den = MFMA(za, pf[st0 + s], den);
        }
        float v[4];
#pragma unroll
        for (int j = 0; j < 4; ++j) v[j] = num[j] * __builtin_amdgcn_rcpf(den[j] + 1e-6f);
        ov[et] = (u32x2){cvt_pk(v[0], v[1]), cvt_pk(v[2], v[3])};
      }
      const unsigned ob = WS_OH + (unsigned)(((2 * panel1 + kh) * 44 + ((cb2 * 5) >> 1)) * 4 + wt) * 1024 + l * 16;
      if ((cb2 & 1) == 0) {
        st16(rws, ob, (u32x4){ov[0][0], ov[0][1], ov[1][0], ov[1][1]});
        st16(rws, ob + 4096, (u32x4){ov[2][0], ov[2][1], ov[3][0], ov[3][1]});
        st8(ws, ob + 8192, ov[4]);
      } else {
        st8(ws, ob + 8, ov[0]);
        st16(rws, ob + 4096, (u32x4){ov[1][0], ov[1][1], ov[2][0], ov[2][1]});
        st16(rws, ob + 8192, (u32x4){ov[3][0], ov[3][1], ov[4][0], ov[4][1]});
      }
      run(26, 30);
      wait_vm<0>();
      run(30, 32);
      if (tid == 0) st8u(ws, WS_FO + (panel1 * 8 + cb2) * 64, tag);
    } else {
      run(18, 32);
#pragma unroll
      for (int n = 0; n < 6; ++n) zreg[n] = *(LAS u32x2*)(uintptr_t)(lds0 + L_KVT + 6144 + ((wt >> 1) * 6 + n) * 1024 + l * 16 + (wt & 1) * 8);
    }
#pragma unroll
    for (int n = 0; n < 6; ++n) acc[n] = MFMA(bfr[1][n], af[1], acc[n]);
    STAMP(4);
    {
      const int r0 = tid / 10, h0 = tid - r0 * 10, c1 = (tid & 127) + 512, r1 = c1 / 10, h1 = c1 - r1 * 10;
      xo0 = (unsigned)((panel3 * 64 + r0) * D_ + cb3 * 80 + h0 * 8); xo1 = (unsigned)((panel3 * 64 + r1) * D_ + cb3 * 80 + h1 * 8);
      la0 = lds0 + (unsigned)((r0 * 84 + h0 * 8) * 4); la1 = lds0 + (unsigned)((r1 * 84 + h1 * 8) * 4);
      xv[0] = *(const u32x4*)(P.x + xo0); xv[1] = *(const u32x4*)(P.x + xo1);
    }
    bar();
    const unsigned L_RED = lds0 + 3 * P3_SLOT;
    if (kh == 1) {
#pragma unroll
      for (int n = 0; n < 6; ++n) stsf4(L_RED + (wt * 6 + n) * 1024 + l * 16, acc[n]);
    }
    bar();
    if (kh == 0) {
#pragma unroll
      for (int n = 0; n < 6; ++n) acc[n] += ldsf4(L_RED + (wt * 6 + n) * 1024 + l * 16);
      u32x2 hv[6];
#pragma unroll
      for (int n = 0; n < 6; ++n) {
        f32x4 gg = acc[n] + bs;
        hv[n] = (u32x2){cvt_pk(lo16(zreg[n][0]) * gg[0], hi16(zreg[n][0]) * gg[1]), cvt_pk(lo16(zreg[n][1]) * gg[2], hi16(zreg[n][1]) * gg[3])};
      }
      const unsigned hb = WS_OH + (unsigned)((panel2 * 44 + 20 + cb2 * 3) * 4 + wt) * 1024 + l * 16;
#pragma unroll
      for (int s = 0; s < 3; ++s) st16(rws, hb + s * 4096, (u32x4){hv[2 * s][0], hv[2 * s][1], hv[2 * s + 1][0], hv[2 * s + 1][1]});
      wait_vm<0>();
      if (l == 0) st8u(ws, WS_F2 + ((panel2 * 8 + cb2) * 4 + wt) * 64, tag);
    }
    bar();
    STAMP(5);
  }
  {
    const int wsr = w & 3, kh = w >> 2;
    const unsigned ao = (unsigned)(kh * 8192 + wsr * 1024 + l * 16);
    const unsigned bb = 16384u + kh * 10240 + l * 16;
    const bf16x8 zeros = {0, 0, 0, 0, 0, 0, 0, 0};
    f32x4 acc[5];
#pragma unroll
    for (int n = 0; n < 5; ++n) acc[n] = (f32x4){0.f, 0.f, 0.f, 0.f};
    bf16x8 af[2][2], bfr[2][2][5];
    unsigned sb = lds0;
    auto mm = [&](int c) {
#pragma unroll
      for (int s2 = 0; s2 < 2; ++s2)
#pragma unroll
        for (int n = 0; n < 5; ++n) acc[n] = MFMA(bfr[c][s2][n], af[c][s2], acc[n]);
      SB0;
    };
    auto rdu = [&](int c) {
      bar();
#pragma unroll
      for (int s2 = 0; s2 < 2; ++s2) {
        af[c][s2] = lds_b128(sb + ao + s2 * 4096);
#pragma unroll
        for (int n = 0; n < 5; ++n) bfr[c][s2][n] = lds_b128(sb + bb + s2 * 5120 + n * 1024);
      }
      SB0;
      sb += P3_SLOT; if (sb == lds0 + 4 * P3_SLOT) sb = lds0;
    };
    rdu(0);
#pragma unroll 1
    for (int u = 0; u < 5; ++u) { rdu(1); mm(0); rdu(0); mm(1); }
    mm(0);
    STAMP(6);
    bar();
#pragma unroll
    for (int n = 0; n < 5; ++n) stsf4(lds0 + (unsigned)(((kh * 64 + wsr * 16 + fr) * 84 + n * 16 + 4 * g) * 4), acc[n]);
    bar();
#pragma unroll
    for (int i = 0; i < 2; ++i) {
      if (i == 0 || tid < 128) {
        const unsigned a = i == 0 ? la0 : la1;
        f32x4 a0 = ldsf4(a) + ldsf4(a + 64 * 84 * 4), a1 = ldsf4(a + 16) + ldsf4(a + 64 * 84 * 4 + 16);
        u32x4 o = {cvt_pk(lo16(xv[i][0]) + a0[0], hi16(xv[i][0]) + a0[1]), cvt_pk(lo16(xv[i][1]) + a0[2], hi16(xv[i][1]) + a0[3]),
                   cvt_pk(lo16(xv[i][2]) + a1[0], hi16(xv[i][2]) + a1[1]), cvt_pk(lo16(xv[i][3]) + a1[2], hi16(xv[i][3]) + a1[3])};
        *(u32x4*)(P.out + (i == 0 ? xo0 : xo1)) = o;
      }
    }
    STAMP(7);
  }
}

extern "C" void kernel_launch(void* const* d_in, const int* in_sizes, int n_in, void* d_out, int out_size,
                              void* d_ws, size_t ws_size, hipStream_t stream) {
  Params P;
  P.x = (const u16*)d_in[0];
  P.wqkv = (const u16*)d_in[1];
  P.wo = (const u16*)d_in[2];
  P.win = (const u16*)d_in[4];
  P.wsp = (const u16*)d_in[5];
  P.bsp = (const u16*)d_in[6];
  P.wout = (const u16*)d_in[7];
  P.out = (u16*)d_out;
  P.ws = (char*)d_ws;
  (void)hipFuncSetAttribute((const void*)mega_kernel, hipFuncAttributeMaxDynamicSharedMemorySize, LDS_TOTAL);
  mega_kernel<<<256, 768, LDS_TOTAL, stream>>>(P);
}
```

```cpp
#include <hip/hip_runtime.h>
#include <hip/hip_bf16.h>
#include <stdint.h>

typedef __attribute__((ext_vector_type(8))) short bf16x8;
typedef __attribute__((ext_vector_type(4))) short bf16x4;
typedef __attribute__((ext_vector_type(4))) float f32x4;
typedef __attribute__((ext_vector_type(2))) float f32x2;
typedef __attribute__((ext_vector_type(2))) __bf16 bf2;
typedef __attribute__((ext_vector_type(4))) unsigned u32x4;
typedef __attribute__((ext_vector_type(2))) unsigned u32x2;
typedef unsigned short u16;
typedef unsigned long long u64;
#define LAS __attribute__((address_space(3)))
#define GAS __attribute__((address_space(1)))
#define DI __device__ __forceinline__
#define RLX_AGENT __ATOMIC_RELAXED, __HIP_MEMORY_SCOPE_AGENT

constexpr int S_ = 2048, D_ = 640, NQKV = 1920, FIN = 1536, FH = 768;

constexpr unsigned WS_PQ  = 0;
constexpr unsigned WS_Z1  = WS_PQ + 16 * 8 * 8 * 3 * 1024;
constexpr unsigned WS_Z2N = WS_Z1 + 16 * 8 * 24 * 1024;
constexpr unsigned WS_OH  = WS_Z2N + 8 * 64 * 6 * 1024;
constexpr unsigned WS_WF  = WS_OH + 32 * 44 * 4 * 1024;
constexpr unsigned WS_KVT = WS_WF + 8 * 44 * 5 * 1024;
constexpr unsigned WS_KVP = WS_KVT + 8 * 18 * 1024;
constexpr unsigned WS_GR  = WS_KVP + 8 * 18 * 16 * 1024;
constexpr unsigned WS_GX  = WS_GR + 16 * 8 * 256 * 8;
constexpr unsigned WS_GC  = WS_GX + 2048 * 16;
constexpr unsigned WS_F1  = WS_GC + 24 * 10 * 2048;
constexpr unsigned WS_F2  = WS_F1 + 16 * 32 * 64;
constexpr unsigned WS_FO  = WS_F2 + 1024 * 64;
constexpr unsigned WS_FK  = WS_FO + 256 * 64;
constexpr unsigned WS_EP  = WS_FK + 256 * 64;
constexpr unsigned WS_END = WS_EP + 64;

#define STAMP(i)

DI float bf2f(u16 v) { return __uint_as_float(((unsigned)v) << 16); }
DI unsigned cvt_pk(float lo, float hi) {
  bf2 r; r[0] = (__bf16)lo; r[1] = (__bf16)hi; return __builtin_bit_cast(unsigned, r);
}
DI float rbf(float x) { return (float)((__bf16)x); }
DI float lo16(unsigned u) { return __uint_as_float(u << 16); }
DI float hi16(unsigned u) { return __uint_as_float(u & 0xffff0000u); }

DI void glds16(const void* gbase, unsigned voff, unsigned lds_dst) {
  unsigned keep;
  asm volatile("s_mov_b32 %0, m0\n\ts_mov_b32 m0, %3\n\ts_nop 0\n\tglobal_load_lds_dwordx4 %1, %2\n\ts_mov_b32 m0, %0"
               : "=&s"(keep) : "v"(voff), "s"(gbase), "s"(lds_dst) : "memory");
}
DI void glds16c(const void* gbase, unsigned voff, unsigned lds_dst) {
  unsigned keep;
  asm volatile("s_mov_b32 %0, m0\n\ts_mov_b32 m0, %3\n\ts_nop 0\n\tglobal_load_lds_dwordx4 %1, %2 sc1\n\ts_mov_b32 m0, %0"
               : "=&s"(keep) : "v"(voff), "s"(gbase), "s"(lds_dst) : "memory");
}
#define GL_PRE "s_mov_b32 %0, m0\n\ts_mov_b32 m0, %2\n\ts_nop 0\n\t"
#define GL_POST "s_mov_b32 m0, %0"
DI void gl2(const void* gb, unsigned lds, unsigned v0, unsigned v1) {
  unsigned keep;
  asm volatile(GL_PRE "global_load_lds_dwordx4 %3, %1\n\tglobal_load_lds_dwordx4 %4, %1 offset:1024\n\t" GL_POST
               : "=&s"(keep) : "s"(gb), "s"(lds), "v"(v0), "v"(v1) : "memory");
}
DI void gl3(const void* gb, unsigned lds, unsigned v0, unsigned v1, unsigned v2) {
  unsigned keep;
  asm volatile(GL_PRE "global_load_lds_dwordx4 %3, %1\n\tglobal_load_lds_dwordx4 %4, %1 offset:1024\n\tglobal_load_lds_dwordx4 %5, %1 offset:2048\n\t" GL_POST
               : "=&s"(keep) : "s"(gb), "s"(lds), "v"(v0), "v"(v1), "v"(v2) : "memory");
}
DI void gl4(const void* gb, unsigned lds, unsigned v0, unsigned v1, unsigned v2, unsigned v3) {
  unsigned keep;
  asm volatile(GL_PRE "global_load_lds_dwordx4 %3, %1\n\tglobal_load_lds_dwordx4 %4, %1 offset:1024\n\tglobal_load_lds_dwordx4 %5, %1 offset:2048\n\tglobal_load_lds_dwordx4 %6, %1 offset:3072\n\t" GL_POST
               : "=&s"(keep) : "s"(gb), "s"(lds), "v"(v0), "v"(v1), "v"(v2), "v"(v3) : "memory");
}
DI void gl3c(const void* gb, unsigned lds, unsigned v0) {
  unsigned keep;
  asm volatile(GL_PRE "global_load_lds_dwordx4 %3, %1 sc1\n\tglobal_load_lds_dwordx4 %3, %1 offset:1024 sc1\n\tglobal_load_lds_dwordx4 %3, %1 offset:2048 sc1\n\t" GL_POST
               : "=&s"(keep) : "s"(gb), "s"(lds), "v"(v0) : "memory");
}
DI void gl4c(const void* gb, unsigned lds, unsigned v0) {
  unsigned keep;
  asm volatile(GL_PRE "global_load_lds_dwordx4 %3, %1 sc1\n\tglobal_load_lds_dwordx4 %3, %1 offset:1024 sc1\n\tglobal_load_lds_dwordx4 %3, %1 offset:2048 sc1\n\tglobal_load_lds_dwordx4 %3, %1 offset:3072 sc1\n\t" GL_POST
               : "=&s"(keep) : "s"(gb), "s"(lds), "v"(v0) : "memory");
}
template <int N> DI void wait_vm() { asm volatile("s_waitcnt vmcnt(%0)" :: "n"(N) : "memory"); }

DI bf16x8 lds_b128(unsigned addr) { return *(LAS bf16x8*)(uintptr_t)addr; }
DI bf16x8 lds_2b64(unsigned addr0, unsigned addr1) {
  bf16x4 a = *(LAS bf16x4*)(uintptr_t)addr0, b = *(LAS bf16x4*)(uintptr_t)addr1;
  return __builtin_shufflevector(a, b, 0, 1, 2, 3, 4, 5, 6, 7);
}
DI bf16x8 lds_tr(unsigned addr0, unsigned addr1) {
  bf16x4 a = __builtin_amdgcn_ds_read_tr16_b64_v4i16((LAS bf16x4*)(uintptr_t)addr0);
  bf16x4 b = __builtin_amdgcn_ds_read_tr16_b64_v4i16((LAS bf16x4*)(uintptr_t)addr1);
  return __builtin_shufflevector(a, b, 0, 1, 2, 3, 4, 5, 6, 7);
}
DI float ldsf(unsigned a) { return *(LAS float*)(uintptr_t)a; }
DI void stsf(unsigned a, float v) { *(LAS float*)(uintptr_t)a = v; }
DI f32x4 ldsf4(unsigned a) { return *(LAS f32x4*)(uintptr_t)a; }
DI void stsf4(unsigned a, f32x4 v) { *(LAS f32x4*)(uintptr_t)a = v; }
#define SB0 __builtin_amdgcn_sched_barrier(0)
#define MFMA(a, b, c) __builtin_amdgcn_mfma_f32_16x16x32_bf16((a), (b), (c), 0, 0, 0)

template <int CTRL> DI float dpp(float v) {
  return __builtin_bit_cast(float, __builtin_amdgcn_update_dpp(0, __builtin_bit_cast(int, v), CTRL, 0xf, 0xf, true));
}
DI float sum8(float v) { v += dpp<0xB1>(v); v += dpp<0x4E>(v); v += dpp<0x141>(v); return v; }
DI float sum16(float v) { v = sum8(v); v += dpp<0x140>(v); return v; }

DI float gelu_t(float x) {
  float u = x * (0.7978845608f + 0.0356774081f * x * x);
  float e = __builtin_amdgcn_exp2f(-2.885390082f * u);
  return x * __builtin_amdgcn_rcpf(1.0f + e);
}
DI f32x4 gelu4(f32x4 x) {
  const f32x4 a = x * ((x * x) * -0.1029432397f + -2.302208198f);
  f32x4 e;
#pragma unroll
  for (int j = 0; j < 4; ++j) e[j] = __builtin_amdgcn_exp2f(a[j]);
  e += 1.0f;
#pragma unroll
  for (int j = 0; j < 4; ++j) e[j] = __builtin_amdgcn_rcpf(e[j]);
  return x * e;
}
DI f32x4 phi4(f32x4 t) {
  f32x4 a, m;
#pragma unroll
  for (int j = 0; j < 4; ++j) { a[j] = fminf(t[j], 0.f); m[j] = fmaxf(t[j], 0.f); }
  a *= 1.4426950409f;
#pragma unroll
  for (int j = 0; j < 4; ++j) a[j] = __builtin_amdgcn_exp2f(a[j]);
  return (m + 1e-6f) + a;
}
DI float phi_f(float t) {
  return (fmaxf(t, 0.f) + 1e-6f) + __builtin_amdgcn_exp2f(fminf(t, 0.f) * 1.4426950409f);
}

typedef __amdgpu_buffer_rsrc_t rsrc_t;
DI void st16(rsrc_t r, unsigned off, u32x4 v) { __builtin_amdgcn_raw_buffer_store_b128(v, r, off, 0, 16); }
DI u32x4 ld16(rsrc_t r, unsigned off) { return __builtin_amdgcn_raw_buffer_load_b128(r, off, 0, 16); }
DI void st8(char* ws, unsigned off, u32x2 v) { __hip_atomic_store((GAS u64*)(ws + off), ((u64)v[1] << 32) | v[0], RLX_AGENT); }
DI void st8u(char* ws, unsigned off, u64 v) { __hip_atomic_store((GAS u64*)(ws + off), v, RLX_AGENT); }
DI void st4(char* ws, unsigned off, float v) { __hip_atomic_store((GAS unsigned*)(ws + off), __float_as_uint(v), RLX_AGENT); }
DI u64 ld8(const char* ws, unsigned off) { return __hip_atomic_load((GAS u64*)(ws + off), RLX_AGENT); }
constexpr unsigned SPIN_CAP = 1u << 20;
DI void wait_words(const char* ws, unsigned off, bool act, u64 tag) {
#pragma unroll 1
  for (unsigned sp = 0; sp < SPIN_CAP; ++sp) {
    u64 v = act ? ld8(ws, off) : tag;
    if (__all(v == tag)) break;
    __builtin_amdgcn_s_sleep(1);
  }
  __builtin_amdgcn_fence(__ATOMIC_ACQUIRE, "wavefront");
}

struct Params { const u16 *x, *wqkv, *wo, *win, *wsp, *bsp, *wout; u16* out; char* ws; };

constexpr unsigned KBS = 4224;
constexpr unsigned K1_STAGE = 16384 + 8 * KBS;
constexpr unsigned L_MISC = 153600;
constexpr int LDS_TOTAL = 163840;
constexpr unsigned P2_SLOT = 20480;
constexpr unsigned L_KVT = 135168;
constexpr unsigned P3_SLOT = 36864;

DI void bar() { asm volatile("s_waitcnt lgkmcnt(0)" ::: "memory"); __builtin_amdgcn_s_barrier(); asm volatile("" ::: "memory"); }

struct Side { u32x4 r0, r1; };
DI void side_issue(const Params& P, Side& S, int row, int w) {
  const int l = threadIdx.x & 63;
  const u32x4* src = (const u32x4*)(P.x + (size_t)row * D_);
  S.r0 = src[l]; S.r1 = src[64 + (l & 15)];
}
DI float rdl(float v, int lane) { return __builtin_bit_cast(float, __builtin_amdgcn_readlane(__builtin_bit_cast(int, v), lane)); }
DI void side_finish(const Params& P, rsrc_t rws, const Side& S, int row, int w, unsigned tl, unsigned th) {
  const int l = threadIdx.x & 63;
  float s = 0.f, q = 0.f, s2 = 0.f, q2 = 0.f;
#pragma unroll
  for (int j = 0; j < 4; ++j) {
    float a = lo16(S.r0[j]), b = hi16(S.r0[j]), c = lo16(S.r1[j]), d = hi16(S.r1[j]);
    s += a + b; q += a * a + b * b; s2 += c + d; q2 += c * c + d * d;
  }
  s = sum16(s); q = sum16(q); s2 = sum16(s2); q2 = sum16(q2);
  s = (rdl(s, 0) + rdl(s, 16)) + (rdl(s, 32) + rdl(s, 48)) + s2;
  q = (rdl(q, 0) + rdl(q, 16)) + (rdl(q, 32) + rdl(q, 48)) + q2;
  const float m = s * (1.f / D_);
  const float v = rbf(fmaxf(q * (1.f / D_) - m * m, 0.f));
  const float r = 1.f / rbf(sqrtf(rbf(v + 1e-5f)));
  if (l < 2) st8u(P.ws, WS_GX + row * 16 + l * 8, l == 0 ? (((u64)tl << 32) | __float_as_uint(-rbf(m) * r)) : (((u64)th << 32) | __float_as_uint(r)));
}
DI void wf_job(const Params& P, rsrc_t rws, int w) {
  const int l = threadIdx.x & 63;
  int item = blockIdx.x; if (item > 219) item = 219;
  const int st = item / 5, T = (item - st * 5) * 8 + w, cb = T / 5, nt = T - cb * 5;
  const u16* wsrc = (st < 20 ? P.wo + (size_t)st * 32 * D_ : P.wout + (size_t)(st - 20) * 32 * D_) + (size_t)(l >> 4) * 4 * D_ + T * 16 + (l & 15);
  unsigned wv[8];
#pragma unroll
  for (int e = 0; e < 8; ++e) wv[e] = wsrc[(size_t)(16 * (e >> 2) + (e & 3)) * D_];
  st16(rws, WS_WF + (unsigned)(((cb * 44 + st) * 5 + nt) * 1024) + l * 16,
       (u32x4){wv[0] | (wv[1] << 16), wv[2] | (wv[3] << 16), wv[4] | (wv[5] << 16), wv[6] | (wv[7] << 16)});
}
DI void xstat_gather(const char* ws, int panel, unsigned L_ST, int tid, unsigned tg, u64 gv) {
  if (tid < 256) {
#pragma unroll 1
    for (unsigned sp = 0; sp < SPIN_CAP; ++sp) {
      if (__all((unsigned)(gv >> 32) == tg)) break;
      __builtin_amdgcn_s_sleep(1);
      gv = ld8(ws, WS_GX + panel * 2048 + tid * 8);
    }
    stsf(L_ST + (tid & 1) * 512 + (tid >> 1) * 4, __uint_as_float((unsigned)gv));
  }
}

DI void csum_issue(const char* ws, int blk, int t, u64 (&cv)[10]) {
#pragma unroll
  for (int k = 0; k < 10; ++k) cv[k] = ld8(ws, WS_GC + (blk * 10 + k) * 2048 + t * 8);
}
DI void csum_gather(const char* ws, int blk, int t, unsigned tl, u64 (&cv)[10], unsigned dst) {
#pragma unroll 1
  for (unsigned sp = 0; sp < SPIN_CAP; ++sp) {
    bool ok = true;
#pragma unroll
    for (int k = 0; k < 10; ++k) ok &= (unsigned)(cv[k] >> 32) == tl;
    if (__all(ok)) break;
    __builtin_amdgcn_s_sleep(1);
    csum_issue(ws, blk, t, cv);
  }
  float a = 0.f;
#pragma unroll
  for (int k = 0; k < 10; ++k) a += __uint_as_float((unsigned)cv[k]);
  stsf(dst + t * 4, a);
}
DI void csum_store(char* ws, unsigned off, f32x4 d, int fr, int g, unsigned tl) {
  const float v = fr == 0 ? d[0] : fr == 1 ? d[1] : fr == 2 ? d[2] : d[3];
  if (fr < 4) st8u(ws, off + (4 * g + fr) * 8, ((u64)tl << 32) | __float_as_uint(v));
}

DI void p1q_consumer(const Params& P, rsrc_t rws, int w, int panel, int cbi, unsigned lds0, u64 tag, bf16x8 (&pf)[3]) {
  char* ws = P.ws;
  const int tid = threadIdx.x, l = tid & 63;
  const int wm = w >> 2, wc = (w + 2 * wm) & 3;
  const int g = l >> 4, fr = l & 15, q = (l & 15) >> 2, p = l & 3, g1 = g & 1;
  const unsigned aoff0 = (unsigned)((wm * 64 + fr) * 128 + ((g ^ (l & 7)) << 4));
  const unsigned ttab = (unsigned)(0x4498E765DC32BA10ull >> (16 * wc)) & 0xffffu;
  int tile[4];
  unsigned bo[4];
#pragma unroll
  for (int n = 0; n < 4; ++n) {
    tile[n] = (ttab >> (4 * n)) & 15;
    bo[n] = 16384u + g * KBS + q * 480 + p * 8 + tile[n] * 32;
  }
  const unsigned tl = (unsigned)tag, th = tl * 0x9E3779B1u ^ (unsigned)(tag >> 32);
  Side S; side_issue(P, S, panel * 128 + cbi * 8 + w, w);
  f32x4 acc[4][4];
#pragma unroll
  for (int m = 0; m < 4; ++m)
#pragma unroll
    for (int n = 0; n < 4; ++n) acc[m][n] = (f32x4){0.f, 0.f, 0.f, 0.f};
  const short one = 0x3F80;
  const bf16x8 ones = {one, one, one, one, one, one, one, one};
  const bf16x8 zeros = {0, 0, 0, 0, 0, 0, 0, 0};

  bf16x8 af[2][4], bfr[2][4];
#pragma unroll
  for (int m = 0; m < 4; ++m) { af[1][m] = zeros; bfr[1][m] = zeros; }
  auto mm = [&](int ks) {
#pragma unroll
    for (int n = 0; n < 3; ++n)
#pragma unroll
      for (int m = 0; m < 4; ++m) acc[m][n] = MFMA(bfr[ks][n], af[ks][m], acc[m][n]);
    if (wc < 3) {
#pragma unroll
      for (int m = 0; m < 4; ++m) acc[m][3] = MFMA(bfr[ks][3], af[ks][m], acc[m][3]);
    }
  };
  auto step = [&](unsigned sb, int ks) {
#pragma unroll
    for (int n = 0; n < 4; ++n) bfr[ks][n] = lds_tr(sb + ks * 4 * KBS + bo[n], sb + ks * 4 * KBS + bo[n] + 1920);
#pragma unroll
    for (int m = 0; m < 4; ++m) af[ks][m] = lds_b128(sb + ((aoff0 ^ (ks * 64)) + m * 2048));
    SB0;
    mm(ks ^ 1);
    SB0;
  };
  unsigned sb = lds0;
  auto run = [&](int n) {
#pragma unroll 1
    for (int kt = 0; kt < n; ++kt) {
      bar();
      step(sb, 0);
      step(sb, 1);
      sb += K1_STAGE; if (sb == lds0 + 3 * K1_STAGE) sb = lds0;
    }
  };
  run(1);
  side_finish(P, rws, S, panel * 128 + cbi * 8 + w, w, tl, th);
  if (panel < 10) {
#pragma unroll
    for (int h = 0; h < 2; ++h) {
      const bf16x8 b0 = wm == 0 ? bfr[0][h] : bfr[0][2 + h], b1 = wm == 0 ? bfr[1][h] : bfr[1][2 + h];
      const int tt = wm == 0 ? tile[h] : tile[2 + h];
      f32x4 d = MFMA(b0, ones, ((f32x4){0.f, 0.f, 0.f, 0.f}));
      d = MFMA(b1, ones, d);
      csum_store(ws, WS_GC + (cbi * 10 + panel) * 2048 + tt * 128, d, fr, g, tl);
    }
  }
  run(9);
  mm(1);
  STAMP(1);
  bar();
  STAMP(10);
  const unsigned L_ST = lds0 + L_MISC, L_CS = lds0 + L_MISC + 5120;
  const unsigned L_K = lds0, L_V = L_K + 20480, L_Q = lds0 + 122880;
  float mr[4], rinv[4];
#pragma unroll
  for (int m = 0; m < 4; ++m) { const int rl = wm * 64 + m * 16 + fr; mr[m] = ldsf(L_ST + rl * 4); rinv[m] = ldsf(L_ST + 512 + rl * 4); }
#pragma unroll
  for (int n = 0; n < 4; ++n) {
    if (n < 3 || wc < 3) {
      const int t = tile[n];
      const f32x4 cs = ldsf4(L_CS + (t * 16 + 4 * g) * 4);
      unsigned dst, dstep;
      if (t < 5) { dst = L_Q + (wm * 12 + (t >> 1)) * 1024 + l * 16 + (t & 1) * 8; dstep = 3072; }
      else {
        const int kb = wm * 8 + (fr >> 3), pos = (fr & 7) ^ ((fr >> 3) << 2);
        dst = (t < 10 ? L_K + (t - 5) * 256 : L_V + (t - 10) * 256) + kb * 1280 + pos * 32 + g * 8; dstep = 2560;
      }
      if (t < 10) {
#pragma unroll
        for (int m = 0; m < 4; ++m) {
          f32x4 v = acc[m][n] * rinv[m] + cs * mr[m];
          v = phi4(v);
          *(LAS u32x2*)(uintptr_t)(dst + m * dstep) = (u32x2){cvt_pk(v[0], v[1]), cvt_pk(v[2], v[3])};
        }
      } else {
#pragma unroll
        for (int m = 0; m < 4; ++m) {
          f32x4 v = acc[m][n] * rinv[m] + cs * mr[m];
          *(LAS u32x2*)(uintptr_t)(dst + m * dstep) = (u32x2){cvt_pk(v[0], v[1]), cvt_pk(v[2], v[3])};
        }
      }
    }
  }
  STAMP(8);
  bar();
  {
    const unsigned troff = (unsigned)(g * 5 * 256 + ((4 * g1 + q) * 32) + p * 8);
    f32x4 d[3];
    int jb[3];
#pragma unroll
    for (int r = 0; r < 3; ++r) {
      int job = w + 8 * r; if (job >= 18) job = w;
      jb[r] = job;
      const int i = job < 13 ? (job + 1) / 3 : job - 13;
      const int j = job < 13 ? job - 2 * i : 0;
      d[r] = (f32x4){0.f, 0.f, 0.f, 0.f};
#pragma unroll
      for (int ks = 0; ks < 4; ++ks) {
        const unsigned a0 = L_K + troff + (ks * 20 + i) * 256, b0 = L_V + troff + (ks * 20 + j) * 256;
        const bf16x8 ka = lds_tr(a0, a0 ^ 128);
        bf16x8 vb = lds_tr(b0, b0 ^ 128);
        if (job >= 13) vb = ones;
        d[r] = MFMA(ka, vb, d[r]);
      }
    }
#pragma unroll
    for (int r = 0; r < 3; ++r)
      st16(rws, WS_KVP + (unsigned)(((cbi * 18 + jb[r]) * 16 + panel) * 1024) + l * 16, __builtin_bit_cast(u32x4, d[r]));
    pf[0] = lds_b128(L_Q + w * 3072 + l * 16); pf[1] = lds_b128(L_Q + w * 3072 + 1024 + l * 16);
    const u32x2 q2 = *(LAS u32x2*)(uintptr_t)(L_Q + w * 3072 + 2048 + l * 16);
    pf[2] = __builtin_bit_cast(bf16x8, ((u32x4){q2[0], q2[1], 0u, 0u}));
  }
  STAMP(9);
  wait_vm<0>();
  bar();
  if (tid == 0) st8u(ws, WS_F1 + (panel * 32 + cbi) * 64, tag);
}

DI void p1z_consumer(const Params& P, rsrc_t rws, int w, int panel, int cbj, unsigned lds0, u64 tag) {
  char* ws = P.ws;
  const int tid = threadIdx.x, l = tid & 63;
  const int wm = w & 3, wn = w >> 2;
  const int g = l >> 4, fr = l & 15, q = (l & 15) >> 2, p = l & 3, g1 = g & 1;
  const unsigned aoff0 = (unsigned)((wm * 32 + fr) * 128 + ((g ^ (l & 7)) << 4));
  unsigned bo[3];
  int ca[3];
#pragma unroll
  for (int n = 0; n < 3; ++n) {
    const int c = wn * 3 + n;
    ca[n] = c;
    int pc = c + g1; if (pc == 6) pc = 0;
    bo[n] = 16384u + g * 1536 + q * 192 + pc * 32 + p * 8;
  }
  const short one = 0x3F80;
  const bf16x8 ones = {one, one, one, one, one, one, one, one};
  const bf16x8 zeros = {0, 0, 0, 0, 0, 0, 0, 0};
  f32x4 accA[2][3], accB[2][3];
#pragma unroll
  for (int m = 0; m < 2; ++m)
#pragma unroll
    for (int n = 0; n < 3; ++n) { accA[m][n] = (f32x4){0.f, 0.f, 0.f, 0.f}; accB[m][n] = (f32x4){0.f, 0.f, 0.f, 0.f}; }
  const unsigned tl = (unsigned)tag, th = tl * 0x9E3779B1u ^ (unsigned)(tag >> 32), tg = (tid & 1) ? th : tl;
  bf16x8 af[2][2], bfr[2][3];
  af[1][0] = af[1][1] = zeros; bfr[1][0] = bfr[1][1] = bfr[1][2] = zeros;
  auto rd = [&](unsigned sb, int ks) {
#pragma unroll
    for (int m = 0; m < 2; ++m) af[ks][m] = lds_b128(sb + ((aoff0 ^ (ks * 64)) + m * 2048));
#pragma unroll
    for (int n = 0; n < 3; ++n) { const unsigned o = sb + ks * 4 * 1536 + bo[n]; bfr[ks][n] = lds_tr(o, o + 768); }
    SB0;
  };
  auto mmA = [&](int ks) {
#pragma unroll
    for (int n = 0; n < 3; ++n)
#pragma unroll
      for (int m = 0; m < 2; ++m) accA[m][n] = MFMA(af[ks][m], bfr[ks][n], accA[m][n]);
    SB0;
  };
  auto mmB = [&](int ks) {
#pragma unroll
    for (int n = 0; n < 3; ++n)
#pragma unroll
      for (int m = 0; m < 2; ++m) accB[m][n] = MFMA(bfr[ks][n], af[ks][m], accB[m][n]);
    SB0;
  };
  const unsigned L_ST = lds0 + L_MISC, L_CS = lds0 + L_MISC + 5120;
  const unsigned L_RS = lds0 + L_MISC + 2048;
  const unsigned L_FS = lds0 + L_MISC + 4096;
  unsigned sb = lds0;
  auto nxt = [&]() { sb += K1_STAGE; if (sb == lds0 + 3 * K1_STAGE) sb = lds0; };
  auto runA = [&](int n) {
#pragma unroll 1
    for (int kt = 0; kt < n; ++kt) { bar(); rd(sb, 0); mmA(1); rd(sb, 1); mmA(0); nxt(); }
  };
  auto runB = [&](int n) {
#pragma unroll 1
    for (int kt = 0; kt < n; ++kt) { bar(); rd(sb, 0); mmB(1); rd(sb, 1); mmB(0); nxt(); }
  };
  auto cpub = [&](int blk) {
    if (panel < 10 && wm == 0) {
#pragma unroll
      for (int n = 0; n < 3; ++n) {
        f32x4 d = MFMA(bfr[0][n], ones, ((f32x4){0.f, 0.f, 0.f, 0.f}));
        d = MFMA(bfr[1][n], ones, d);
        csum_store(ws, WS_GC + (blk * 10 + panel) * 2048 + ca[n] * 128, d, fr, g, tl);
      }
    }
  };
  Side S; side_issue(P, S, panel * 128 + 64 + cbj * 8 + w, w);
  runA(1);
  side_finish(P, rws, S, panel * 128 + 64 + cbj * 8 + w, w, tl, th);
  cpub(8 + cbj);
  runA(6);
  const int tc = tid - 256;
  {
    u64 cv[10];
    if (tid < 256) cv[0] = ld8(ws, WS_GX + panel * 2048 + tid * 8);
    else if (tc < 96) csum_issue(ws, 8 + cbj, tc, cv);
    runA(3);
    if (tid < 256) xstat_gather(ws, panel, L_ST, tid, tg, cv[0]);
    else if (tc < 96) csum_gather(ws, 8 + cbj, tc, tl, cv, L_CS);
  }
  bar(); rd(sb, 0); mmA(1); rd(sb, 1);
  cpub(16 + cbj);
  STAMP(8);
  {
    float cn[3];
#pragma unroll
    for (int n = 0; n < 3; ++n) cn[n] = ldsf(L_CS + (ca[n] * 16 + fr) * 4);
#pragma unroll
    for (int m = 0; m < 2; ++m) {
      const int rl = wm * 32 + m * 16 + 4 * g;
      const f32x4 mr = ldsf4(L_ST + rl * 4), rinv = ldsf4(L_ST + 512 + rl * 4);
      f32x4 rs = {0.f, 0.f, 0.f, 0.f}, rq = {0.f, 0.f, 0.f, 0.f};
#pragma unroll
      for (int n = 0; n < 3; ++n) {
        f32x4 v = accA[m][n] * rinv + mr * cn[n];
        v = gelu4(v);
        rs += v; rq += v * v;
        accA[m][n] = v;
      }
#pragma unroll
      for (int j = 0; j < 4; ++j) { rs[j] = sum16(rs[j]); rq[j] = sum16(rq[j]); }
      if (fr == 0) {
        stsf4(L_RS + ((wn * 128 + rl) * 2) * 4, (f32x4){rs[0], rq[0], rs[1], rq[1]});
        stsf4(L_RS + ((wn * 128 + rl) * 2 + 4) * 4, (f32x4){rs[2], rq[2], rs[3], rq[3]});
      }
    }
  }
  mmB(0); nxt();
  bar(); rd(sb, 0);
  float part = 0.f;
  const unsigned gbase = WS_GR + (unsigned)(panel * 8) * 2048 + tid * 8;
  if (tid < 256) {
    part = ldsf(L_RS + tid * 4) + ldsf(L_RS + 1024 + tid * 4);
    st8u(ws, gbase + cbj * 2048, ((u64)tg << 32) | __float_as_uint(part));
  }
  mmB(1); rd(sb, 1); mmB(0); nxt();
  STAMP(9);
  runB(1);
  {
    u64 cv[10];
    if (tid < 256) {
#pragma unroll
      for (int i = 1; i < 8; ++i) cv[i] = ld8(ws, gbase + ((cbj + i) & 7) * 2048);
    }
    runB(1);
    if (tid < 256) {
      float tot;
#pragma unroll 1
      for (unsigned sp = 0; sp < SPIN_CAP; ++sp) {
        bool ok = true; tot = part;
#pragma unroll
        for (int i = 1; i < 8; ++i) { ok &= (unsigned)(cv[i] >> 32) == tg; tot += __uint_as_float((unsigned)cv[i]); }
        if (__all(ok)) break;
#pragma unroll
        for (int i = 1; i < 8; ++i) cv[i] = ld8(ws, gbase + ((cbj + i) & 7) * 2048);
      }
      float other = dpp<0xB1>(tot);
      if ((tid & 1) == 0) {
        float m = tot * (1.f / FH);
        float v = rbf(fmaxf(other * (1.f / FH) - m * m, 0.f));
        float r = 1.f / rbf(sqrtf(rbf(v + 1e-5f)));
        stsf(L_FS + (tid >> 1) * 4, -rbf(m) * r);
        stsf(L_FS + 512 + (tid >> 1) * 4, r);
      }
    }
  }
  bar(); rd(sb, 0);
  {
    f32x4 mr[2], ri[2];
#pragma unroll
    for (int m = 0; m < 2; ++m) { const int rl = wm * 32 + m * 16 + 4 * g; mr[m] = ldsf4(L_FS + rl * 4); ri[m] = ldsf4(L_FS + 512 + rl * 4); }
#pragma unroll
    for (int n = 0; n < 3; ++n) {
      f32x4 a = accA[0][n] * ri[0] + mr[0], b = accA[1][n] * ri[1] + mr[1];
      st16(rws, WS_Z2N + (unsigned)(((cbj * 64 + panel * 4 + wm) * 6 + ca[n]) * 1024) + l * 16,
           (u32x4){cvt_pk(a[0], a[1]), cvt_pk(a[2], a[3]), cvt_pk(b[0], b[1]), cvt_pk(b[2], b[3])});
    }
  }
  mmB(1); rd(sb, 1); mmB(0); nxt();
  STAMP(10);
  runB(2);
  wait_vm<0>();
  bar();
  if (tid == 0) st8u(ws, WS_F1 + (panel * 32 + 8 + cbj) * 64, tag);
  STAMP(11);
  rd(sb, 0); mmB(1); rd(sb, 1); mmB(0); nxt();
  {
    u64 cv[10];
    if (tid >= 256 && tc < 96) csum_issue(ws, 16 + cbj, tc, cv);
    runB(2);
    if (tid >= 256 && tc < 96) csum_gather(ws, 16 + cbj, tc, tl, cv, L_CS + 512);
  }
  mmB(1);
  STAMP(1);
  bar();
  {
    f32x4 cn[3];
#pragma unroll
    for (int n = 0; n < 3; ++n) { const unsigned a = L_CS + 512 + (ca[n] * 16 + 4 * g) * 4; cn[n] = ldsf4(a); }
    float mr[2], rinv[2];
#pragma unroll
    for (int m = 0; m < 2; ++m) { const int rl = wm * 32 + m * 16 + fr; mr[m] = ldsf(L_ST + rl * 4); rinv[m] = ldsf(L_ST + 512 + rl * 4); }
#pragma unroll
    for (int n = 0; n < 3; ++n) {
      u32x4 o;
#pragma unroll
      for (int m = 0; m < 2; ++m) {
        f32x4 v = accB[m][n] * rinv[m] + cn[n] * mr[m];
        v = gelu4(v);
        o[2 * m] = cvt_pk(v[0], v[1]); o[2 * m + 1] = cvt_pk(v[2], v[3]);
      }
      if (wm < 2) *(LAS u32x4*)(uintptr_t)(lds0 + L_KVT + 6144 + (wm * 6 + ca[n]) * 1024 + l * 16) = o;
      else st16(rws, WS_Z1 + (unsigned)(((panel * 8 + cbj) * 4 + wm) * 6 + ca[n]) * 1024 + l * 16, o);
    }
  }
}

__global__ __launch_bounds__(768, 3) void mega_kernel(Params P) {
  extern __shared__ __attribute__((aligned(16))) char smem[];
  const unsigned lds0 = (unsigned)(uintptr_t)(LAS char*)smem;
  char* ws = P.ws;
  const int tid = threadIdx.x, l = tid & 63;
  const int w = __builtin_amdgcn_readfirstlane(tid >> 6);
  const int g = l >> 4, fr = l & 15;
  const int b = blockIdx.x, xcd = b & 7, bi = b >> 3;
  const int panel1 = (xcd >> 1) * 4 + (bi & 3), cb1 = (xcd & 1) * 4 + ((bi >> 2) & 3);
  const bool isq = bi < 16;
  const int panel2 = 2 * panel1 + (isq ? 1 : 0), cb2 = cb1;
  const int panel3 = xcd * 4 + (bi & 3), cb3 = bi >> 2;
  STAMP(0);
  const u64 tag = ld8(ws, WS_EP) + 1;
  if (w == 7 && bi < 20) {
    u64 pc; asm volatile("s_getpc_b64 %0" : "=s"(pc));
    gl2((const char*)(pc & ~127ull) + bi * 2048, lds0 + 3 * K1_STAGE, l * 16, l * 16);
  }

  if (w >= 8) {
    const int lw = w - 8;
    const unsigned vA0 = (unsigned)(((l >> 3) * D_ + (((l & 7) ^ (l >> 3)) << 3)) * 2);
    const unsigned vA1 = vA0 + 8 * D_ * 2 - 1024, vA2 = vA0 + 16 * D_ * 2 - 2048, vA3 = vA0 + 24 * D_ * 2 - 3072;
    const char* gA = (const char*)(P.x + (size_t)(panel1 * 128 + lw * 32) * D_);
    const unsigned v20 = (unsigned)(((l >> 3) * S_ + (((l & 7) ^ (l >> 4)) << 3)) * 2);
    const unsigned v21 = (unsigned)((((l >> 3) + 8) * S_ + (((l & 7) ^ ((l >> 4) | 4)) << 3)) * 2) - 1024;
    const char* gA2 = (const char*)(P.wsp + (size_t)(panel2 * 64 + lw * 16) * S_);
    const int rot2 = 0;
    auto ldA = [&](int kt, unsigned sl) { gl2(gA2 + ((kt + rot2) & 31) * 128, sl + lw * 2048, v20, v21); };
    auto sl2 = [&](int kt) { return lds0 + ((kt + 4) % 6) * P2_SLOT; };
    const unsigned tl = (unsigned)tag, th = tl * 0x9E3779B1u ^ (unsigned)(tag >> 32);
    const int lt = tid - 512;
    auto gather = [&](int blk, int ncol, unsigned dst, bool xs) {
      u64 cv[10]; u64 gv = 0;
      const int tc = lt < ncol ? lt : 0;
      if (xs) gv = ld8(ws, WS_GX + panel1 * 2048 + lt * 8);
      csum_issue(ws, blk, tc, cv);
      if (xs) xstat_gather(ws, panel1, lds0 + L_MISC, lt, (lt & 1) ? th : tl, gv);
      csum_gather(ws, blk, tc, tl, cv, dst);
    };
    const unsigned l16 = l * 16;
    const char* gB2 = ws + WS_Z2N + (size_t)cb2 * 64 * 6144 + lw * 3072;
    auto ldB = [&](int kt, unsigned sl) { gl3c(gB2 + ((kt + rot2) & 31) * 12288, sl + 8192 + lw * 3072, l16); };
    const unsigned zfl = WS_F1 + ((l & 15) * 32 + 8 + cb2) * 64;
    auto try_z = [&]() {
      const u64 v = l < 16 ? ld8(ws, zfl) : tag;
      return (bool)__builtin_amdgcn_readfirstlane((int)__all(v == tag));
    };
    bool early = false;
    const int rot = panel1 < 10 ? panel1 : 0;
    auto krot = [&](int kt) { const int k = kt >= 10 ? kt - 10 : kt; return __builtin_amdgcn_readfirstlane(k == 0 ? rot : (k - 1 < rot ? k - 1 : k)); };
    if (isq) {
      gl4(gA + rot * 128, lds0 + lw * 4096, vA0, vA1, vA2, vA3);
      unsigned vB[4];
#pragma unroll
      for (int i = 0; i < 4; ++i) {
        int c = i * 64 + l; if (c > 239) c = 239;
        const int row = c / 30, cc = c - row * 30, seg = cc / 10, ch = cc - seg * 10;
        vB[i] = (unsigned)((row * NQKV + seg * 640 + cb1 * 80 + ch * 8) * 2) + 4096 - i * 1024;
      }
      const char* gB = (const char*)P.wqkv + (size_t)lw * 16 * NQKV * 2 - 4096;
      auto stageB = [&](int kt, unsigned sb) {
        const char* bp = gB + (size_t)krot(kt) * 64 * NQKV * 2;
        gl4(bp, sb + 16384 + lw * 2 * KBS, vB[0], vB[1], vB[2], vB[3]);
        gl4(bp + 8 * NQKV * 2, sb + 16384 + (lw * 2 + 1) * KBS, vB[0], vB[1], vB[2], vB[3]);
      };
      auto stage = [&](int kt, unsigned sb) { gl4(gA + krot(kt) * 128, sb + lw * 4096, vA0, vA1, vA2, vA3); stageB(kt, sb); };
      stageB(0, lds0); stage(1, lds0 + K1_STAGE);
      wait_vm<12>(); bar();
      stage(2, lds0 + 2 * K1_STAGE);
      unsigned sb = lds0, sc = lds0 + K1_STAGE;
#pragma unroll 1
      for (int kt = 1; kt < 8; ++kt) {
        wait_vm<12>(); bar();
        if (kt == 2) STAMP(10);
        if (kt == 7) STAMP(11);
        stage(kt + 2, sb);
        sb = sc; sc += K1_STAGE; if (sc == lds0 + 3 * K1_STAGE) sc = lds0;
      }
      wait_vm<12>(); bar();
      gather(cb1, 240, lds0 + L_MISC + 5120, true);
      bar();
      ldA(0, sl2(0)); ldA(1, sl2(1)); ldA(5, sl2(5));
      bar();
      ldA(4, sl2(4));
#pragma unroll 1
      for (int t = 0; t < 3 && !early; ++t) early = try_z();
      if (early) { ldB(0, sl2(0)); ldB(1, sl2(1)); ldB(4, sl2(4)); ldB(5, sl2(5)); }
      bar();
      if (!early) { early = try_z(); if (early) { ldB(0, sl2(0)); ldB(1, sl2(1)); ldB(4, sl2(4)); ldB(5, sl2(5)); } }
      bar();
    } else {
      gl4(gA + rot * 128, lds0 + lw * 4096, vA0, vA1, vA2, vA3);
      unsigned vB[3];
#pragma unroll
      for (int i = 0; i < 3; ++i) {
        const int c = i * 64 + l, row = c / 12, cc = c - row * 12;
        int pc = (cc >> 1) - (row >> 3); if (pc < 0) pc = 5;
        vB[i] = (unsigned)((row * FIN + cb1 * 96 + pc * 16 + (cc & 1) * 8) * 2) + 4096 - i * 1024;
      }
      const char* gB = (const char*)P.win + (size_t)lw * 16 * FIN * 2 - 4096;
      auto stageB = [&](int kt, unsigned sb) {
        const int k = krot(kt);
        gl3(gB + (kt < 10 ? FH * 2 : 0) + (size_t)k * 64 * FIN * 2, sb + 16384 + lw * 3072, vB[0], vB[1], vB[2]);
      };
      auto stage = [&](int kt, unsigned sb) {
        const int k = krot(kt);
        gl4(gA + k * 128, sb + lw * 4096, vA0, vA1, vA2, vA3); stageB(kt, sb);
      };
      stageB(0, lds0); stage(1, lds0 + K1_STAGE);
      wait_vm<7>(); bar();
      stage(2, lds0 + 2 * K1_STAGE);
      unsigned sb = lds0, sc = lds0 + K1_STAGE;
      auto runL = [&](int k0, int k1) {
#pragma unroll 1
        for (int kt = k0; kt < k1; ++kt) {
          wait_vm<7>(); bar();
          stage(kt + 2, sb);
          sb = sc; sc += K1_STAGE; if (sc == lds0 + 3 * K1_STAGE) sc = lds0;
        }
      };
      runL(1, 11);
      {
        const char* wp = (const char*)(P.wsp + (size_t)(panel1 * 128 + lw * 32) * S_) + (cb1 & 3) * 1024;
        const unsigned vo = (unsigned)((l >> 3) * S_ * 2 + (l & 7) * 128);
        unsigned d0, d1, d2, d3;
        asm volatile("global_load_dword %0, %4, %5\n\tglobal_load_dword %1, %4, %6\n\tglobal_load_dword %2, %4, %7\n\tglobal_load_dword %3, %4, %8"
                     : "=&v"(d0), "=&v"(d1), "=&v"(d2), "=&v"(d3)
                     : "v"(vo), "s"(wp), "s"(wp + 8 * S_ * 2), "s"(wp + 16 * S_ * 2), "s"(wp + 24 * S_ * 2) : "memory");
        runL(11, 13);
        asm volatile("" :: "v"(d0), "v"(d1), "v"(d2), "v"(d3));
      }
      runL(13, 18);
      wait_vm<7>(); bar();
      wait_vm<0>(); bar();
      bar();
      wait_words(ws, zfl, l < 16, tag);
      early = true;
      ldA(0, sl2(0)); ldB(0, sl2(0)); ldA(1, sl2(1)); ldB(1, sl2(1));
      ldA(4, sl2(4)); ldA(5, sl2(5)); ldB(4, sl2(4)); ldB(5, sl2(5));
    }
    STAMP(2);
    if (!early) {
      wait_words(ws, zfl, l < 16, tag);
      ldB(0, sl2(0)); ldB(1, sl2(1)); ldB(4, sl2(4)); ldB(5, sl2(5));
    }
    STAMP(3);
    ldA(2, sl2(2)); ldA(3, sl2(3)); ldB(2, sl2(2)); ldB(3, sl2(3));
    wait_vm<16>(); bar();
    {
      unsigned sl = sl2(0);
      auto ldU = [&](int u) {
        ldA(2 * u, sl); ldB(2 * u, sl); ldA(2 * u + 1, sl + P2_SLOT); ldB(2 * u + 1, sl + P2_SLOT);
        sl += 2 * P2_SLOT; if (sl == lds0 + 6 * P2_SLOT) sl = lds0;
      };
      wait_vm<0>(); bar(); ldU(3);
#pragma unroll 1
      for (int u = 2; u < 14; ++u) {
        wait_vm<10>(); bar();
        if (u == 4) STAMP(8);
        if (u == 10) STAMP(9);
        ldU(u + 2);
      }
    }
    wait_vm<10>(); bar();
    wait_words(ws, WS_F1 + (((lw * 64 + l) >> 4) * 32 + 16 + (l & 15)) * 64, true, tag);
    wait_vm<0>(); bar();
    if (b == 0 && tid == 512) st8u(ws, WS_EP, tag);
    STAMP(4);
    const char* gA3 = ws + WS_OH + (size_t)panel3 * 44 * 4096 + lw * 4096;
    const char* gB3 = ws + WS_WF + (size_t)cb3 * 44 * 5120 + lw * 5120;
    int r5 = cb3 + 2 * (bi & 3); r5 = r5 >= 10 ? r5 - 10 : r5 >= 5 ? r5 - 5 : r5;
    int r6 = cb3 + 2 * (bi & 3); r6 = r6 >= 12 ? r6 - 12 : r6 >= 6 ? r6 - 6 : r6;
    auto ur = [&](int u) {
      int v;
      if (u < 5) { v = u + r5; if (v >= 5) v -= 5; } else { v = u + r6; if (v >= 11) v -= 6; }
      return __builtin_amdgcn_readfirstlane(v);
    };
    auto ldB3 = [&](int u, unsigned sl) {
      gl4c(gB3 + ur(u) * 20480, sl + 16384 + lw * 5120, l16);
      glds16c(gB3 + ur(u) * 20480 + 4096, l16, sl + 16384 + lw * 5120 + 4096);
    };
    auto ldA3 = [&](int u, unsigned sl) { gl4c(gA3 + ur(u) * 16384, sl + lw * 4096, l16); };
    bar(); bar();
    ldB3(0, lds0); ldB3(1, lds0 + P3_SLOT); ldB3(2, lds0 + 2 * P3_SLOT);
    wait_words(ws, WS_FO + ((panel3 >> 1) * 8 + (l & 7)) * 64, l < 8, tag);
    ldA3(0, lds0); ldA3(1, lds0 + P3_SLOT); ldA3(2, lds0 + 2 * P3_SLOT);
    bar();
    ldA3(3, lds0 + 3 * P3_SLOT); ldB3(3, lds0 + 3 * P3_SLOT);
    wait_vm<17>(); bar();
    wait_vm<13>(); bar(); ldA3(4, lds0); ldB3(4, lds0);
    wait_vm<18>(); bar(); ldB3(5, lds0 + P3_SLOT);
    wait_vm<14>(); bar(); ldB3(6, lds0 + 2 * P3_SLOT);
    wait_vm<10>(); bar(); ldB3(7, lds0 + 3 * P3_SLOT);
    wait_words(ws, WS_F2 + (panel3 * 32 + (l & 31)) * 64, l < 32, tag);
    STAMP(5);
    ldA3(5, lds0 + P3_SLOT); ldA3(6, lds0 + 2 * P3_SLOT); ldA3(7, lds0 + 3 * P3_SLOT);
    wait_vm<8>(); bar(); ldA3(8, lds0); ldB3(8, lds0);
    wait_vm<13>(); bar(); ldA3(9, lds0 + P3_SLOT); ldB3(9, lds0 + P3_SLOT);
    wait_vm<18>(); bar(); ldA3(10, lds0 + 2 * P3_SLOT); ldB3(10, lds0 + 2 * P3_SLOT);
    wait_vm<18>(); bar(); wait_vm<9>(); bar(); wait_vm<0>(); bar();
    bar(); bar();
    STAMP(6);
    return;
  }

  const rsrc_t rws = __builtin_amdgcn_make_buffer_rsrc(ws, 0, (int)WS_END, 0x00020000);
  bf16x8 pf[3];
  if (isq) p1q_consumer(P, rws, w, panel1, cb1, lds0, tag, pf);
  else p1z_consumer(P, rws, w, panel1, cb1, lds0, tag);
  STAMP(2);
  wf_job(P, rws, w);

  u32x4 xv[2];
  unsigned xo0, xo1, la0, la1;
  {
    const int wt = w & 3, kh = w >> 2;
    const int rsw = ((wt * 16 + fr) >> 1) & 7;
    const unsigned ao0 = (unsigned)((wt * 16 + fr) * 128 + (((4 * kh + (g >> 1)) ^ rsw) << 4) + (g & 1) * 8);
    const unsigned ao1 = (unsigned)((wt * 16 + fr) * 128 + (((4 * kh + 2 + (g >> 1)) ^ rsw) << 4) + (g & 1) * 8);
    const unsigned bo = (unsigned)(8192 + kh * 6144 + l * 16);
    const bf16x8 zeros = {0, 0, 0, 0, 0, 0, 0, 0};
    f32x4 acc[6];
#pragma unroll
    for (int n = 0; n < 6; ++n) acc[n] = (f32x4){0.f, 0.f, 0.f, 0.f};
    bf16x8 af[2], bfr[2][6];
    af[1] = zeros;
#pragma unroll
    for (int n = 0; n < 6; ++n) bfr[1][n] = zeros;
    const int zi = (bi & 15) * 8 + xcd;
    const bool reducer = !isq && zi < 72;
    const int rj = zi & 7, rf = zi >> 3;
    const int row = panel2 * 64 + wt * 16 + fr;
    u32x2 zreg[6];
    const float bs = bf2f(P.bsp[row]);
    unsigned sl = lds0 + 4 * P2_SLOT;
    auto step = [&](int cur, int kt) {
      if (cur == 0) bar();
      af[cur] = lds_2b64(sl + ao0, sl + ao1);
#pragma unroll
      for (int n = 0; n < 6; ++n) bfr[cur][n] = lds_b128(sl + bo + n * 1024);
      SB0;
#pragma unroll
      for (int n = 0; n < 6; ++n) acc[n] = MFMA(bfr[cur ^ 1][n], af[cur ^ 1], acc[n]);
      SB0;
      sl += P2_SLOT; if (sl == lds0 + 6 * P2_SLOT) sl = lds0;
    };
    auto run = [&](int k0, int k1) {
#pragma unroll 1
      for (int kt = k0; kt < k1; kt += 2) { step(0, kt); step(1, kt + 1); }
    };
    if (reducer) {
      const int et = (rf + 1) >> 1, st = rf == 0 ? 0 : ((rf + 1) & 1) + (et >= 3 ? 1 : 0);
      wait_words(ws, WS_F1 + ((l & 15) * 32 + rj) * 64, true, tag);
      f32x4 pv[8];
      bool okv[4];
#pragma unroll
      for (int t4 = 0; t4 < 4; ++t4) {
        const int i = 2 * st + (t4 & 1);
        okv[t4] = i < 5 && (t4 >= 2 || (et - i <= 1 && i - et <= 1));
        const int job = okv[t4] ? (t4 >= 2 ? 13 + i : 2 * i + et) : 0;
#pragma unroll
        for (int pp = 0; pp < 2; ++pp)
          pv[t4 * 2 + pp] = __builtin_bit_cast(f32x4, ld16(rws, WS_KVP + (unsigned)(((rj * 18 + job) * 16 + w * 2 + pp) * 1024) + l * 16));
      }
      run(0, 2);
      if (tid == 0) st8u(ws, WS_F1 + (panel1 * 32 + 16 + cb1) * 64, tag);
      const unsigned L_R = lds0 + 122880;
      f32x4 sv[4];
#pragma unroll
      for (int t4 = 0; t4 < 4; ++t4) { sv[t4] = pv[t4 * 2] + pv[t4 * 2 + 1]; if (!okv[t4]) sv[t4] = (f32x4){0.f, 0.f, 0.f, 0.f}; }
      if (w >= 4) {
#pragma unroll
        for (int t4 = 0; t4 < 4; ++t4) stsf4(L_R + ((w - 4) * 4 + t4) * 1024 + l * 16, sv[t4]);
      }
      run(2, 4);
      if (w < 4) {
#pragma unroll
        for (int t4 = 0; t4 < 4; ++t4) stsf4(L_R + (w * 4 + t4) * 1024 + l * 16, sv[t4] + ldsf4(L_R + (w * 4 + t4) * 1024 + l * 16));
      }
      run(4, 6);
      if (w < 2) {
        f32x4 v[2];
#pragma unroll
        for (int h2 = 0; h2 < 2; ++h2) {
          v[h2] = (f32x4){0.f, 0.f, 0.f, 0.f};
#pragma unroll
          for (int ww = 0; ww < 4; ++ww) v[h2] += ldsf4(L_R + (ww * 4 + w * 2 + h2) * 1024 + l * 16);
          const int he = (et * 16 + fr) / 20;
#pragma unroll
          for (int r = 0; r < 4; ++r) { const int d = (2 * st + h2) * 16 + 4 * g + r; if (d / 20 != he) v[h2][r] = 0.f; }
        }
        st16(rws, WS_KVT + (unsigned)(((rj * 2 + w) * 9 + rf) * 1024) + l * 16,
             (u32x4){cvt_pk(v[0][0], v[0][1]), cvt_pk(v[0][2], v[0][3]), cvt_pk(v[1][0], v[1][1]), cvt_pk(v[1][2], v[1][3])});
      }
      run(6, 10);
      wait_vm<0>();
      if (w < 2 && l == 0) st8u(ws, WS_FK + (rj * 32 + w * 16 + rf) * 64, tag);
      run(10, 18);
    } else {
      run(0, 6);
      wait_vm<0>();
      run(6, 8);
      if (tid == 0) st8u(ws, WS_F1 + (panel1 * 32 + (isq ? 24 : 16) + cb1) * 64, tag);
      run(8, 18);
    }
    if (isq) {
      const unsigned foff = (l & 15) < 9 ? WS_FK + (cb2 * 32 + (l & 31)) * 64 : WS_F1 + (panel1 * 32 + 16 + cb2) * 64;
      const u64 fv = ld8(ws, foff);
      run(18, 22);
      if (!__all(fv == tag)) wait_words(ws, foff, l < 32, tag);
#pragma unroll
      for (int i = 0; i < 3; ++i) {
        int ins = w + 8 * i; if (ins >= 18) ins = 17;
        ins = __builtin_amdgcn_readfirstlane(ins);
        glds16c(ws, WS_KVT + cb2 * 18432 + ins * 1024 + l * 16, __builtin_amdgcn_readfirstlane(lds0 + L_KVT + ins * 1024));
      }
      const unsigned zo = WS_Z1 + (unsigned)(((panel1 * 8 + cb2) * 4 + 2 + (wt >> 1)) * 6) * 1024 + l * 16 + (wt & 1) * 8;
#pragma unroll
      for (int n = 0; n < 6; ++n) zreg[n] = __builtin_amdgcn_raw_buffer_load_b64(rws, zo + n * 1024, 0, 16);
      run(22, 24);
      wait_vm<0>();
      run(24, 26);
      u32x2 ov[5];
#pragma unroll
      for (int et = 0; et < 5; ++et) {
        f32x4 num = {0.f, 0.f, 0.f, 0.f}, den = {0.f, 0.f, 0.f, 0.f};
        const int st0 = et >= 3 ? 1 : 0, nst = et == 0 ? 1 : 2, f0 = et == 0 ? 0 : 1 + (et - 1) * 2;
#pragma unroll
        for (int s = 0; s < nst; ++s) {
          bf16x8 ka = lds_b128(lds0 + L_KVT + (f0 + s) * 1024 + l * 16), za = lds_b128(lds0 + L_KVT + (9 + f0 + s) * 1024 + l * 16);
          num = MFMA(ka, pf[st0 + s], num); den = MFMA(za, pf[st0 + s], den);
        }
        float v[4];
#pragma unroll
        for (int j = 0; j < 4; ++j) v[j] = num[j] * __builtin_amdgcn_rcpf(den[j] + 1e-6f);
        ov[et] = (u32x2){cvt_pk(v[0], v[1]), cvt_pk(v[2], v[3])};
      }
      const unsigned ob = WS_OH + (unsigned)(((2 * panel1 + kh) * 44 + ((cb2 * 5) >> 1)) * 4 + wt) * 1024 + l * 16;
      if ((cb2 & 1) == 0) {
        st16(rws, ob, (u32x4){ov[0][0], ov[0][1], ov[1][0], ov[1][1]});
        st16(rws, ob + 4096, (u32x4){ov[2][0], ov[2][1], ov[3][0], ov[3][1]});
        st8(ws, ob + 8192, ov[4]);
      } else {
        st8(ws, ob + 8, ov[0]);
        st16(rws, ob + 4096, (u32x4){ov[1][0], ov[1][1], ov[2][0], ov[2][1]});
        st16(rws, ob + 8192, (u32x4){ov[3][0], ov[3][1], ov[4][0], ov[4][1]});
      }
      run(26, 30);
      wait_vm<0>();
      run(30, 32);
      if (tid == 0) st8u(ws, WS_FO + (panel1 * 8 + cb2) * 64, tag);
    } else {
      run(18, 32);
#pragma unroll
      for (int n = 0; n < 6; ++n) zreg[n] = *(LAS u32x2*)(uintptr_t)(lds0 + L_KVT + 6144 + ((wt >> 1) * 6 + n) * 1024 + l * 16 + (wt & 1) * 8);
    }
#pragma unroll
    for (int n = 0; n < 6; ++n) acc[n] = MFMA(bfr[1][n], af[1], acc[n]);
    STAMP(4);
    {
      const int r0 = tid / 10, h0 = tid - r0 * 10, c1 = (tid & 127) + 512, r1 = c1 / 10, h1 = c1 - r1 * 10;
      xo0 = (unsigned)((panel3 * 64 + r0) * D_ + cb3 * 80 + h0 * 8); xo1 = (unsigned)((panel3 * 64 + r1) * D_ + cb3 * 80 + h1 * 8);
      la0 = lds0 + (unsigned)((r0 * 84 + h0 * 8) * 4); la1 = lds0 + (unsigned)((r1 * 84 + h1 * 8) * 4);
      xv[0] = *(const u32x4*)(P.x + xo0); xv[1] = *(const u32x4*)(P.x + xo1);
    }
    bar();
    const unsigned L_RED = lds0 + 3 * P3_SLOT;
    if (kh == 1) {
#pragma unroll
      for (int n = 0; n < 6; ++n) stsf4(L_RED + (wt * 6 + n) * 1024 + l * 16, acc[n]);
    }
    bar();
    if (kh == 0) {
#pragma unroll
      for (int n = 0; n < 6; ++n) acc[n] += ldsf4(L_RED + (wt * 6 + n) * 1024 + l * 16);
      u32x2 hv[6];
#pragma unroll
      for (int n = 0; n < 6; ++n) {
        f32x4 gg = acc[n] + bs;
        hv[n] = (u32x2){cvt_pk(lo16(zreg[n][0]) * gg[0], hi16(zreg[n][0]) * gg[1]), cvt_pk(lo16(zreg[n][1]) * gg[2], hi16(zreg[n][1]) * gg[3])};
      }
      const unsigned hb = WS_OH + (unsigned)((panel2 * 44 + 20 + cb2 * 3) * 4 + wt) * 1024 + l * 16;
#pragma unroll
      for (int s = 0; s < 3; ++s) st16(rws, hb + s * 4096, (u32x4){hv[2 * s][0], hv[2 * s][1], hv[2 * s + 1][0], hv[2 * s + 1][1]});
      wait_vm<0>();
      if (l == 0) st8u(ws, WS_F2 + ((panel2 * 8 + cb2) * 4 + wt) * 64, tag);
    }
    bar();
    STAMP(5);
  }
  {
    const int wsr = w & 3, kh = w >> 2;
    const unsigned ao = (unsigned)(kh * 8192 + wsr * 1024 + l * 16);
    const unsigned bb = 16384u + kh * 10240 + l * 16;
    const bf16x8 zeros = {0, 0, 0, 0, 0, 0, 0, 0};
    f32x4 acc[5];
#pragma unroll
    for (int n = 0; n < 5; ++n) acc[n] = (f32x4){0.f, 0.f, 0.f, 0.f};
    bf16x8 af[2][2], bfr[2][2][5];
    unsigned sb = lds0;
    auto mm = [&](int c) {
#pragma unroll
      for (int s2 = 0; s2 < 2; ++s2)
#pragma unroll
        for (int n = 0; n < 5; ++n) acc[n] = MFMA(bfr[c][s2][n], af[c][s2], acc[n]);
      SB0;
    };
    auto rdu = [&](int c) {
      bar();
#pragma unroll
      for (int s2 = 0; s2 < 2; ++s2) {
        af[c][s2] = lds_b128(sb + ao + s2 * 4096);
#pragma unroll
        for (int n = 0; n < 5; ++n) bfr[c][s2][n] = lds_b128(sb + bb + s2 * 5120 + n * 1024);
      }
      SB0;
      sb += P3_SLOT; if (sb == lds0 + 4 * P3_SLOT) sb = lds0;
    };
    rdu(0);
#pragma unroll 1
    for (int u = 0; u < 5; ++u) { rdu(1); mm(0); rdu(0); mm(1); }
    mm(0);
    STAMP(6);
    bar();
#pragma unroll
    for (int n = 0; n < 5; ++n) stsf4(lds0 + (unsigned)(((kh * 64 + wsr * 16 + fr) * 84 + n * 16 + 4 * g) * 4), acc[n]);
    bar();
#pragma unroll
    for (int i = 0; i < 2; ++i) {
      if (i == 0 || tid < 128) {
        const unsigned a = i == 0 ? la0 : la1;
        f32x4 a0 = ldsf4(a) + ldsf4(a + 64 * 84 * 4), a1 = ldsf4(a + 16) + ldsf4(a + 64 * 84 * 4 + 16);
        u32x4 o = {cvt_pk(lo16(xv[i][0]) + a0[0], hi16(xv[i][0]) + a0[1]), cvt_pk(lo16(xv[i][1]) + a0[2], hi16(xv[i][1]) + a0[3]),
                   cvt_pk(lo16(xv[i][2]) + a1[0], hi16(xv[i][2]) + a1[1]), cvt_pk(lo16(xv[i][3]) + a1[2], hi16(xv[i][3]) + a1[3])};
        *(u32x4*)(P.out + (i == 0 ? xo0 : xo1)) = o;
      }
    }
    STAMP(7);
  }
}

extern "C" void kernel_launch(void* const* d_in, const int* in_sizes, int n_in, void* d_out, int out_size,
                              void* d_ws, size_t ws_size, hipStream_t stream) {
  Params P;
  P.x = (const u16*)d_in[0];
  P.wqkv = (const u16*)d_in[1];
  P.wo = (const u16*)d_in[2];
  P.win = (const u16*)d_in[4];
  P.wsp = (const u16*)d_in[5];
  P.bsp = (const u16*)d_in[6];
  P.wout = (const u16*)d_in[7];
  P.out = (u16*)d_out;
  P.ws = (char*)d_ws;
  (void)hipFuncSetAttribute((const void*)mega_kernel, hipFuncAttributeMaxDynamicSharedMemorySize, LDS_TOTAL);
  mega_kernel<<<256, 768, LDS_TOTAL, stream>>>(P);
}
```

```cpp
#include <hip/hip_runtime.h>
#include <hip/hip_bf16.h>
#include <stdint.h>

typedef __attribute__((ext_vector_type(8))) short bf16x8;
typedef __attribute__((ext_vector_type(4))) short bf16x4;
typedef __attribute__((ext_vector_type(4))) float f32x4;
typedef __attribute__((ext_vector_type(2))) float f32x2;
typedef __attribute__((ext_vector_type(2))) __bf16 bf2;
typedef __attribute__((ext_vector_type(4))) unsigned u32x4;
typedef __attribute__((ext_vector_type(2))) unsigned u32x2;
typedef unsigned short u16;
typedef unsigned long long u64;
#define LAS __attribute__((address_space(3)))
#define GAS __attribute__((address_space(1)))
#define DI __device__ __forceinline__
#define RLX_AGENT __ATOMIC_RELAXED, __HIP_MEMORY_SCOPE_AGENT

constexpr int S_ = 2048, D_ = 640, NQKV = 1920, FIN = 1536, FH = 768;

constexpr unsigned WS_PQ  = 0;
constexpr unsigned WS_Z1  = WS_PQ + 16 * 8 * 8 * 3 * 1024;
constexpr unsigned WS_Z2N = WS_Z1 + 16 * 8 * 24 * 1024;
constexpr unsigned WS_OH  = WS_Z2N + 8 * 64 * 6 * 1024;
constexpr unsigned WS_WF  = WS_OH + 32 * 44 * 4 * 1024;
constexpr unsigned WS_KVT = WS_WF + 8 * 44 * 5 * 1024;
constexpr unsigned WS_KVP = WS_KVT + 8 * 18 * 1024;
constexpr unsigned WS_GR  = WS_KVP + 8 * 18 * 16 * 1024;
constexpr unsigned WS_GX  = WS_GR + 16 * 8 * 256 * 8;
constexpr unsigned WS_GC  = WS_GX + 2048 * 16;
constexpr unsigned WS_F1  = WS_GC + 24 * 10 * 2048;
constexpr unsigned WS_F2  = WS_F1 + 16 * 32 * 64;
constexpr unsigned WS_FO  = WS_F2 + 1024 * 64;
constexpr unsigned WS_FK  = WS_FO + 256 * 64;
constexpr unsigned WS_EP  = WS_FK + 256 * 64;
constexpr unsigned WS_END = WS_EP + 64;

DI float bf2f(u16 v) { return __uint_as_float(((unsigned)v) << 16); }
DI unsigned cvt_pk(float lo, float hi) {
  bf2 r; r[0] = (__bf16)lo; r[1] = (__bf16)hi; return __builtin_bit_cast(unsigned, r);
}
DI float rbf(float x) { return (float)((__bf16)x); }
DI float lo16(unsigned u) { return __uint_as_float(u << 16); }
DI float hi16(unsigned u) { return __uint_as_float(u & 0xffff0000u); }

DI void glds16(const void* gbase, unsigned voff, unsigned lds_dst) {
  unsigned keep;
  asm volatile("s_mov_b32 %0, m0\n\ts_mov_b32 m0, %3\n\ts_nop 0\n\tglobal_load_lds_dwordx4 %1, %2\n\ts_mov_b32 m0, %0"
               : "=&s"(keep) : "v"(voff), "s"(gbase), "s"(lds_dst) : "memory");
}
DI void glds16c(const void* gbase, unsigned voff, unsigned lds_dst) {
  unsigned keep;
  asm volatile("s_mov_b32 %0, m0\n\ts_mov_b32 m0, %3\n\ts_nop 0\n\tglobal_load_lds_dwordx4 %1, %2 sc1\n\ts_mov_b32 m0, %0"
               : "=&s"(keep) : "v"(voff), "s"(gbase), "s"(lds_dst) : "memory");
}
#define GL_PRE "s_mov_b32 %0, m0\n\ts_mov_b32 m0, %2\n\ts_nop 0\n\t"
#define GL_POST "s_mov_b32 m0, %0"
DI void gl2(const void* gb, unsigned lds, unsigned v0, unsigned v1) {
  unsigned keep;
  asm volatile(GL_PRE "global_load_lds_dwordx4 %3, %1\n\tglobal_load_lds_dwordx4 %4, %1 offset:1024\n\t" GL_POST
               : "=&s"(keep) : "s"(gb), "s"(lds), "v"(v0), "v"(v1) : "memory");
}
DI void gl3(const void* gb, unsigned lds, unsigned v0, unsigned v1, unsigned v2) {
  unsigned keep;
  asm volatile(GL_PRE "global_load_lds_dwordx4 %3, %1\n\tglobal_load_lds_dwordx4 %4, %1 offset:1024\n\tglobal_load_lds_dwordx4 %5, %1 offset:2048\n\t" GL_POST
               : "=&s"(keep) : "s"(gb), "s"(lds), "v"(v0), "v"(v1), "v"(v2) : "memory");
}
DI void gl4(const void* gb, unsigned lds, unsigned v0, unsigned v1, unsigned v2, unsigned v3) {
  unsigned keep;
  asm volatile(GL_PRE "global_load_lds_dwordx4 %3, %1\n\tglobal_load_lds_dwordx4 %4, %1 offset:1024\n\tglobal_load_lds_dwordx4 %5, %1 offset:2048\n\tglobal_load_lds_dwordx4 %6, %1 offset:3072\n\t" GL_POST
               : "=&s"(keep) : "s"(gb), "s"(lds), "v"(v0), "v"(v1), "v"(v2), "v"(v3) : "memory");
}
DI void gl3c(const void* gb, unsigned lds, unsigned v0) {
  unsigned keep;
  asm volatile(GL_PRE "global_load_lds_dwordx4 %3, %1 sc1\n\tglobal_load_lds_dwordx4 %3, %1 offset:1024 sc1\n\tglobal_load_lds_dwordx4 %3, %1 offset:2048 sc1\n\t" GL_POST
               : "=&s"(keep) : "s"(gb), "s"(lds), "v"(v0) : "memory");
}
DI void gl4c(const void* gb, unsigned lds, unsigned v0) {
  unsigned keep;
  asm volatile(GL_PRE "global_load_lds_dwordx4 %3, %1 sc1\n\tglobal_load_lds_dwordx4 %3, %1 offset:1024 sc1\n\tglobal_load_lds_dwordx4 %3, %1 offset:2048 sc1\n\tglobal_load_lds_dwordx4 %3, %1 offset:3072 sc1\n\t" GL_POST
               : "=&s"(keep) : "s"(gb), "s"(lds), "v"(v0) : "memory");
}
template <int N> DI void wait_vm() { asm volatile("s_waitcnt vmcnt(%0)" :: "n"(N) : "memory"); }

DI bf16x8 lds_b128(unsigned addr) { return *(LAS bf16x8*)(uintptr_t)addr; }
DI bf16x8 lds_2b64(unsigned addr0, unsigned addr1) {
  bf16x4 a = *(LAS bf16x4*)(uintptr_t)addr0, b = *(LAS bf16x4*)(uintptr_t)addr1;
  return __builtin_shufflevector(a, b, 0, 1, 2, 3, 4, 5, 6, 7);
}
DI bf16x8 lds_tr(unsigned addr0, unsigned addr1) {
  bf16x4 a = __builtin_amdgcn_ds_read_tr16_b64_v4i16((LAS bf16x4*)(uintptr_t)addr0);
  bf16x4 b = __builtin_amdgcn_ds_read_tr16_b64_v4i16((LAS bf16x4*)(uintptr_t)addr1);
  return __builtin_shufflevector(a, b, 0, 1, 2, 3, 4, 5, 6, 7);
}
DI float ldsf(unsigned a) { return *(LAS float*)(uintptr_t)a; }
DI void stsf(unsigned a, float v) { *(LAS float*)(uintptr_t)a = v; }
DI f32x4 ldsf4(unsigned a) { return *(LAS f32x4*)(uintptr_t)a; }
DI void stsf4(unsigned a, f32x4 v) { *(LAS f32x4*)(uintptr_t)a = v; }
#define SB0 __builtin_amdgcn_sched_barrier(0)
#define MFMA(a, b, c) __builtin_amdgcn_mfma_f32_16x16x32_bf16((a), (b), (c), 0, 0, 0)

template <int CTRL> DI float dpp(float v) {
  return __builtin_bit_cast(float, __builtin_amdgcn_update_dpp(0, __builtin_bit_cast(int, v), CTRL, 0xf, 0xf, true));
}
DI float sum8(float v) { v += dpp<0xB1>(v); v += dpp<0x4E>(v); v += dpp<0x141>(v); return v; }
DI float sum16(float v) { v = sum8(v); v += dpp<0x140>(v); return v; }

DI float gelu_t(float x) {
  float u = x * (0.7978845608f + 0.0356774081f * x * x);
  float e = __builtin_amdgcn_exp2f(-2.885390082f * u);
  return x * __builtin_amdgcn_rcpf(1.0f + e);
}
DI f32x4 gelu4(f32x4 x) {
  const f32x4 a = x * ((x * x) * -0.1029432397f + -2.302208198f);
  f32x4 e;
#pragma unroll
  for (int j = 0; j < 4; ++j) e[j] = __builtin_amdgcn_exp2f(a[j]);
  e += 1.0f;
#pragma unroll
  for (int j = 0; j < 4; ++j) e[j] = __builtin_amdgcn_rcpf(e[j]);
  return x * e;
}
DI f32x4 phi4(f32x4 t) {
  f32x4 a, m;
#pragma unroll
  for (int j = 0; j < 4; ++j) { a[j] = fminf(t[j], 0.f); m[j] = fmaxf(t[j], 0.f); }
  a *= 1.4426950409f;
#pragma unroll
  for (int j = 0; j < 4; ++j) a[j] = __builtin_amdgcn_exp2f(a[j]);
  return (m + 1e-6f) + a;
}
DI float phi_f(float t) {
  return (fmaxf(t, 0.f) + 1e-6f) + __builtin_amdgcn_exp2f(fminf(t, 0.f) * 1.4426950409f);
}

typedef __amdgpu_buffer_rsrc_t rsrc_t;
DI void st16(rsrc_t r, unsigned off, u32x4 v) { __builtin_amdgcn_raw_buffer_store_b128(v, r, off, 0, 16); }
DI u32x4 ld16(rsrc_t r, unsigned off) { return __builtin_amdgcn_raw_buffer_load_b128(r, off, 0, 16); }
DI void st8(char* ws, unsigned off, u32x2 v) { __hip_atomic_store((GAS u64*)(ws + off), ((u64)v[1] << 32) | v[0], RLX_AGENT); }
DI void st8u(char* ws, unsigned off, u64 v) { __hip_atomic_store((GAS u64*)(ws + off), v, RLX_AGENT); }
DI void st4(char* ws, unsigned off, float v) { __hip_atomic_store((GAS unsigned*)(ws + off), __float_as_uint(v), RLX_AGENT); }
DI u64 ld8(const char* ws, unsigned off) { return __hip_atomic_load((GAS u64*)(ws + off), RLX_AGENT); }
constexpr unsigned SPIN_CAP = 1u << 20;
DI void wait_words(const char* ws, unsigned off, bool act, u64 tag) {
#pragma unroll 1
  for (unsigned sp = 0; sp < SPIN_CAP; ++sp) {
    u64 v = act ? ld8(ws, off) : tag;
    if (__all(v == tag)) break;
    __builtin_amdgcn_s_sleep(1);
  }
  __builtin_amdgcn_fence(__ATOMIC_ACQUIRE, "wavefront");
}

struct Params { const u16 *x, *wqkv, *wo, *win, *wsp, *bsp, *wout; u16* out; char* ws; };

constexpr unsigned KBS = 4224;
constexpr unsigned K1_STAGE = 16384 + 8 * KBS;
constexpr unsigned L_MISC = 153600;
constexpr int LDS_TOTAL = 163840;
constexpr unsigned P2_SLOT = 20480;
constexpr unsigned L_KVT = 135168;
constexpr unsigned P3_SLOT = 36864;

DI void bar() { asm volatile("s_waitcnt lgkmcnt(0)" ::: "memory"); __builtin_amdgcn_s_barrier(); asm volatile("" ::: "memory"); }

struct Side { u32x4 r0, r1; };
DI void side_issue(const Params& P, Side& S, int row, int w) {
  const int l = threadIdx.x & 63;
  const u32x4* src = (const u32x4*)(P.x + (size_t)row * D_);
  S.r0 = src[l]; S.r1 = src[64 + (l & 15)];
}
DI float rdl(float v, int lane) { return __builtin_bit_cast(float, __builtin_amdgcn_readlane(__builtin_bit_cast(int, v), lane)); }
DI void side_finish(const Params& P, rsrc_t rws, const Side& S, int row, int w, unsigned tl, unsigned th) {
  const int l = threadIdx.x & 63;
  float s = 0.f, q = 0.f, s2 = 0.f, q2 = 0.f;
#pragma unroll
  for (int j = 0; j < 4; ++j) {
    float a = lo16(S.r0[j]), b = hi16(S.r0[j]), c = lo16(S.r1[j]), d = hi16(S.r1[j]);
    s += a + b; q += a * a + b * b; s2 += c + d; q2 += c * c + d * d;
  }
  s = sum16(s); q = sum16(q); s2 = sum16(s2); q2 = sum16(q2);
  s = (rdl(s, 0) + rdl(s, 16)) + (rdl(s, 32) + rdl(s, 48)) + s2;
  q = (rdl(q, 0) + rdl(q, 16)) + (rdl(q, 32) + rdl(q, 48)) + q2;
  const float m = s * (1.f / D_);
  const float v = rbf(fmaxf(q * (1.f / D_) - m * m, 0.f));
  const float r = 1.f / rbf(sqrtf(rbf(v + 1e-5f)));
  if (l < 2) st8u(P.ws, WS_GX + row * 16 + l * 8, l == 0 ? (((u64)tl << 32) | __float_as_uint(-rbf(m) * r)) : (((u64)th << 32) | __float_as_uint(r)));
}
DI void wf_job(const Params& P, rsrc_t rws, int w) {
  const int l = threadIdx.x & 63;
  int item = blockIdx.x; if (item > 219) item = 219;
  const int st = item / 5, T = (item - st * 5) * 8 + w, cb = T / 5, nt = T - cb * 5;
  const u16* wsrc = (st < 20 ? P.wo + (size_t)st * 32 * D_ : P.wout + (size_t)(st - 20) * 32 * D_) + (size_t)(l >> 4) * 4 * D_ + T * 16 + (l & 15);
  unsigned wv[8];
#pragma unroll
  for (int e = 0; e < 8; ++e) wv[e] = wsrc[(size_t)(16 * (e >> 2) + (e & 3)) * D_];
  st16(rws, WS_WF + (unsigned)(((cb * 44 + st) * 5 + nt) * 1024) + l * 16,
       (u32x4){wv[0] | (wv[1] << 16), wv[2] | (wv[3] << 16), wv[4] | (wv[5] << 16), wv[6] | (wv[7] << 16)});
}
DI void xstat_gather(const char* ws, int panel, unsigned L_ST, int tid, unsigned tg, u64 gv) {
  if (tid < 256) {
#pragma unroll 1
    for (unsigned sp = 0; sp < SPIN_CAP; ++sp) {
      if (__all((unsigned)(gv >> 32) == tg)) break;
      __builtin_amdgcn_s_sleep(1);
      gv = ld8(ws, WS_GX + panel * 2048 + tid * 8);
    }
    stsf(L_ST + (tid & 1) * 512 + (tid >> 1) * 4, __uint_as_float((unsigned)gv));
  }
}

DI void csum_issue(const char* ws, int blk, int t, u64 (&cv)[10]) {
#pragma unroll
  for (int k = 0; k < 10; ++k) cv[k] = ld8(ws, WS_GC + (blk * 10 + k) * 2048 + t * 8);
}
DI void csum_gather(const char* ws, int blk, int t, unsigned tl, u64 (&cv)[10], unsigned dst) {
#pragma unroll 1
  for (unsigned sp = 0; sp < SPIN_CAP; ++sp) {
    bool ok = true;
#pragma unroll
    for (int k = 0; k < 10; ++k) ok &= (unsigned)(cv[k] >> 32) == tl;
    if (__all(ok)) break;
    __builtin_amdgcn_s_sleep(1);
    csum_issue(ws, blk, t, cv);
  }
  float a = 0.f;
#pragma unroll
  for (int k = 0; k < 10; ++k) a += __uint_as_float((unsigned)cv[k]);
  stsf(dst + t * 4, a);
}
DI void csum_store(char* ws, unsigned off, f32x4 d, int fr, int g, unsigned tl) {
  const float v = fr == 0 ? d[0] : fr == 1 ? d[1] : fr == 2 ? d[2] : d[3];
  if (fr < 4) st8u(ws, off + (4 * g + fr) * 8, ((u64)tl << 32) | __float_as_uint(v));
}

DI void p1q_consumer(const Params& P, rsrc_t rws, int w, int panel, int cbi, unsigned lds0, u64 tag, bf16x8 (&pf)[3]) {
  char* ws = P.ws;
  const int tid = threadIdx.x, l = tid & 63;
  const int wm = w >> 2, wc = (w + 2 * wm) & 3;
  const int g = l >> 4, fr = l & 15, q = (l & 15) >> 2, p = l & 3, g1 = g & 1;
  const unsigned aoff0 = (unsigned)((wm * 64 + fr) * 128 + ((g ^ (l & 7)) << 4));
  const unsigned ttab = (unsigned)(0x4498E765DC32BA10ull >> (16 * wc)) & 0xffffu;
  int tile[4];
  unsigned bo[4];
#pragma unroll
  for (int n = 0; n < 4; ++n) {
    tile[n] = (ttab >> (4 * n)) & 15;
    bo[n] = 16384u + g * KBS + q * 480 + p * 8 + tile[n] * 32;
  }
  const unsigned tl = (unsigned)tag, th = tl * 0x9E3779B1u ^ (unsigned)(tag >> 32);
  Side S; side_issue(P, S, panel * 128 + cbi * 8 + w, w);
  f32x4 acc[4][4];
#pragma unroll
  for (int m = 0; m < 4; ++m)
#pragma unroll
    for (int n = 0; n < 4; ++n) acc[m][n] = (f32x4){0.f, 0.f, 0.f, 0.f};
  const short one = 0x3F80;
  const bf16x8 ones = {one, one, one, one, one, one, one, one};
  const bf16x8 zeros = {0, 0, 0, 0, 0, 0, 0, 0};

  bf16x8 af[2][4], bfr[2][4];
#pragma unroll
  for (int m = 0; m < 4; ++m) { af[1][m] = zeros; bfr[1][m] = zeros; }
  auto mm = [&](int ks) {
#pragma unroll
    for (int n = 0; n < 3; ++n)
#pragma unroll
      for (int m = 0; m < 4; ++m) acc[m][n] = MFMA(bfr[ks][n], af[ks][m], acc[m][n]);
    if (wc < 3) {
#pragma unroll
      for (int m = 0; m < 4; ++m) acc[m][3] = MFMA(bfr[ks][3], af[ks][m], acc[m][3]);
    }
  };
  auto step = [&](unsigned sb, int ks) {
#pragma unroll
    for (int n = 0; n < 4; ++n) bfr[ks][n] = lds_tr(sb + ks * 4 * KBS + bo[n], sb + ks * 4 * KBS + bo[n] + 1920);
#pragma unroll
    for (int m = 0; m < 4; ++m) af[ks][m] = lds_b128(sb + ((aoff0 ^ (ks * 64)) + m * 2048));
    SB0;
    mm(ks ^ 1);
    SB0;
  };
  unsigned sb = lds0;
  auto run = [&](int n) {
#pragma unroll 1
    for (int kt = 0; kt < n; ++kt) {
      bar();
      step(sb, 0);
      step(sb, 1);
      sb += K1_STAGE; if (sb == lds0 + 3 * K1_STAGE) sb = lds0;
    }
  };
  run(1);
  side_finish(P, rws, S, panel * 128 + cbi * 8 + w, w, tl, th);
  if (panel < 10) {
#pragma unroll
    for (int h = 0; h < 2; ++h) {
      const bf16x8 b0 = wm == 0 ? bfr[0][h] : bfr[0][2 + h], b1 = wm == 0 ? bfr[1][h] : bfr[1][2 + h];
      const int tt = wm == 0 ? tile[h] : tile[2 + h];
      f32x4 d = MFMA(b0, ones, ((f32x4){0.f, 0.f, 0.f, 0.f}));
      d = MFMA(b1, ones, d);
      csum_store(ws, WS_GC + (cbi * 10 + panel) * 2048 + tt * 128, d, fr, g, tl);
    }
  }
  run(9);
  mm(1);
  bar();
  const unsigned L_ST = lds0 + L_MISC, L_CS = lds0 + L_MISC + 5120;
  const unsigned L_K = lds0, L_V = L_K + 20480, L_Q = lds0 + 122880;
  float mr[4], rinv[4];
#pragma unroll
  for (int m = 0; m < 4; ++m) { const int rl = wm * 64 + m * 16 + fr; mr[m] = ldsf(L_ST + rl * 4); rinv[m] = ldsf(L_ST + 512 + rl * 4); }
#pragma unroll
  for (int n = 0; n < 4; ++n) {
    if (n < 3 || wc < 3) {
      const int t = tile[n];
      const f32x4 cs = ldsf4(L_CS + (t * 16 + 4 * g) * 4);
      unsigned dst, dstep;
      if (t < 5) { dst = L_Q + (wm * 12 + (t >> 1)) * 1024 + l * 16 + (t & 1) * 8; dstep = 3072; }
      else {
        const int kb = wm * 8 + (fr >> 3), pos = (fr & 7) ^ ((fr >> 3) << 2);
        dst = (t < 10 ? L_K + (t - 5) * 256 : L_V + (t - 10) * 256) + kb * 1280 + pos * 32 + g * 8; dstep = 2560;
      }
      if (t < 10) {
#pragma unroll
        for (int m = 0; m < 4; ++m) {
          f32x4 v = acc[m][n] * rinv[m] + cs * mr[m];
          v = phi4(v);
          *(LAS u32x2*)(uintptr_t)(dst + m * dstep) = (u32x2){cvt_pk(v[0], v[1]), cvt_pk(v[2], v[3])};
        }
      } else {
#pragma unroll
        for (int m = 0; m < 4; ++m) {
          f32x4 v = acc[m][n] * rinv[m] + cs * mr[m];
          *(LAS u32x2*)(uintptr_t)(dst + m * dstep) = (u32x2){cvt_pk(v[0], v[1]), cvt_pk(v[2], v[3])};
        }
      }
    }
  }
  bar();
  {
    const unsigned troff = (unsigned)(g * 5 * 256 + ((4 * g1 + q) * 32) + p * 8);
    f32x4 d[3];
    int jb[3];
#pragma unroll
    for (int r = 0; r < 3; ++r) {
      int job = w + 8 * r; if (job >= 18) job = w;
      jb[r] = job;
      const int i = job < 13 ? (job + 1) / 3 : job - 13;
      const int j = job < 13 ? job - 2 * i : 0;
      d[r] = (f32x4){0.f, 0.f, 0.f, 0.f};
#pragma unroll
      for (int ks = 0; ks < 4; ++ks) {
        const unsigned a0 = L_K + troff + (ks * 20 + i) * 256, b0 = L_V + troff + (ks * 20 + j) * 256;
        const bf16x8 ka = lds_tr(a0, a0 ^ 128);
        bf16x8 vb = lds_tr(b0, b0 ^ 128);
        if (job >= 13) vb = ones;
        d[r] = MFMA(ka, vb, d[r]);
      }
    }
#pragma unroll
    for (int r = 0; r < 3; ++r)
      st16(rws, WS_KVP + (unsigned)(((cbi * 18 + jb[r]) * 16 + panel) * 1024) + l * 16, __builtin_bit_cast(u32x4, d[r]));
    pf[0] = lds_b128(L_Q + w * 3072 + l * 16); pf[1] = lds_b128(L_Q + w * 3072 + 1024 + l * 16);
    const u32x2 q2 = *(LAS u32x2*)(uintptr_t)(L_Q + w * 3072 + 2048 + l * 16);
    pf[2] = __builtin_bit_cast(bf16x8, ((u32x4){q2[0], q2[1], 0u, 0u}));
  }
  wait_vm<0>();
  bar();
  if (tid == 0) st8u(ws, WS_F1 + (panel * 32 + cbi) * 64, tag);
}

DI void p1z_consumer(const Params& P, rsrc_t rws, int w, int panel, int cbj, unsigned lds0, u64 tag) {
  char* ws = P.ws;
  const int tid = threadIdx.x, l = tid & 63;
  const int wm = w & 3, wn = w >> 2;
  const int g = l >> 4, fr = l & 15, q = (l & 15) >> 2, p = l & 3, g1 = g & 1;
  const unsigned aoff0 = (unsigned)((wm * 32 + fr) * 128 + ((g ^ (l & 7)) << 4));
  unsigned bo[3];
  int ca[3];
#pragma unroll
  for (int n = 0; n < 3; ++n) {
    const int c = wn * 3 + n;
    ca[n] = c;
    int pc = c + g1; if (pc == 6) pc = 0;
    bo[n] = 16384u + g * 1536 + q * 192 + pc * 32 + p * 8;
  }
  const short one = 0x3F80;
  const bf16x8 ones = {one, one, one, one, one, one, one, one};
  const bf16x8 zeros = {0, 0, 0, 0, 0, 0, 0, 0};
  f32x4 accA[2][3], accB[2][3];
#pragma unroll
  for (int m = 0; m < 2; ++m)
#pragma unroll
    for (int n = 0; n < 3; ++n) { accA[m][n] = (f32x4){0.f, 0.f, 0.f, 0.f}; accB[m][n] = (f32x4){0.f, 0.f, 0.f, 0.f}; }
  const unsigned tl = (unsigned)tag, th = tl * 0x9E3779B1u ^ (unsigned)(tag >> 32), tg = (tid & 1) ? th : tl;
  bf16x8 af[2][2], bfr[2][3];
  af[1][0] = af[1][1] = zeros; bfr[1][0] = bfr[1][1] = bfr[1][2] = zeros;
  auto rd = [&](unsigned sb, int ks) {
#pragma unroll
    for (int m = 0; m < 2; ++m) af[ks][m] = lds_b128(sb + ((aoff0 ^ (ks * 64)) + m * 2048));
#pragma unroll
    for (int n = 0; n < 3; ++n) { const unsigned o = sb + ks * 4 * 1536 + bo[n]; bfr[ks][n] = lds_tr(o, o + 768); }
    SB0;
  };
  auto mmA = [&](int ks) {
#pragma unroll
    for (int n = 0; n < 3; ++n)
#pragma unroll
      for (int m = 0; m < 2; ++m) accA[m][n] = MFMA(af[ks][m], bfr[ks][n], accA[m][n]);
    SB0;
  };
  auto mmB = [&](int ks) {
#pragma unroll
    for (int n = 0; n < 3; ++n)
#pragma unroll
      for (int m = 0; m < 2; ++m) accB[m][n] = MFMA(bfr[ks][n], af[ks][m], accB[m][n]);
    SB0;
  };
  const unsigned L_ST = lds0 + L_MISC, L_CS = lds0 + L_MISC + 5120;
  const unsigned L_RS = lds0 + L_MISC + 2048;
  const unsigned L_FS = lds0 + L_MISC + 4096;
  unsigned sb = lds0;
  auto nxt = [&]() { sb += K1_STAGE; if (sb == lds0 + 3 * K1_STAGE) sb = lds0; };
  auto runA = [&](int n) {
#pragma unroll 1
    for (int kt = 0; kt < n; ++kt) { bar(); rd(sb, 0); mmA(1); rd(sb, 1); mmA(0); nxt(); }
  };
  auto runB = [&](int n) {
#pragma unroll 1
    for (int kt = 0; kt < n; ++kt) { bar(); rd(sb, 0); mmB(1); rd(sb, 1); mmB(0); nxt(); }
  };
  auto cpub = [&](int blk) {
    if (panel < 10 && wm == 0) {
#pragma unroll
      for (int n = 0; n < 3; ++n) {
        f32x4 d = MFMA(bfr[0][n], ones, ((f32x4){0.f, 0.f, 0.f, 0.f}));
        d = MFMA(bfr[1][n], ones, d);
        csum_store(ws, WS_GC + (blk * 10 + panel) * 2048 + ca[n] * 128, d, fr, g, tl);
      }
    }
  };
  Side S; side_issue(P, S, panel * 128 + 64 + cbj * 8 + w, w);
  runA(1);
  side_finish(P, rws, S, panel * 128 + 64 + cbj * 8 + w, w, tl, th);
  cpub(8 + cbj);
  runA(6);
  const int tc = tid - 256;
  {
    u64 cv[10];
    if (tid < 256) cv[0] = ld8(ws, WS_GX + panel * 2048 + tid * 8);
    else if (tc < 96) csum_issue(ws, 8 + cbj, tc, cv);
    runA(3);
    if (tid < 256) xstat_gather(ws, panel, L_ST, tid, tg, cv[0]);
    else if (tc < 96) csum_gather(ws, 8 + cbj, tc, tl, cv, L_CS);
  }
  bar(); rd(sb, 0); mmA(1); rd(sb, 1);
  cpub(16 + cbj);
  {
    float cn[3];
#pragma unroll
    for (int n = 0; n < 3; ++n) cn[n] = ldsf(L_CS + (ca[n] * 16 + fr) * 4);
#pragma unroll
    for (int m = 0; m < 2; ++m) {
      const int rl = wm * 32 + m * 16 + 4 * g;
      const f32x4 mr = ldsf4(L_ST + rl * 4), rinv = ldsf4(L_ST + 512 + rl * 4);
      f32x4 rs = {0.f, 0.f, 0.f, 0.f}, rq = {0.f, 0.f, 0.f, 0.f};
#pragma unroll
      for (int n = 0; n < 3; ++n) {
        f32x4 v = accA[m][n] * rinv + mr * cn[n];
        v = gelu4(v);
        rs += v; rq += v * v;
        accA[m][n] = v;
      }
#pragma unroll
      for (int j = 0; j < 4; ++j) { rs[j] = sum16(rs[j]); rq[j] = sum16(rq[j]); }
      if (fr == 0) {
        stsf4(L_RS + ((wn * 128 + rl) * 2) * 4, (f32x4){rs[0], rq[0], rs[1], rq[1]});
        stsf4(L_RS + ((wn * 128 + rl) * 2 + 4) * 4, (f32x4){rs[2], rq[2], rs[3], rq[3]});
      }
    }
  }
  mmB(0); nxt();
  bar(); rd(sb, 0);
  float part = 0.f;
  const unsigned gbase = WS_GR + (unsigned)(panel * 8) * 2048 + tid * 8;
  if (tid < 256) {
    part = ldsf(L_RS + tid * 4) + ldsf(L_RS + 1024 + tid * 4);
    st8u(ws, gbase + cbj * 2048, ((u64)tg << 32) | __float_as_uint(part));
  }
  mmB(1); rd(sb, 1); mmB(0); nxt();
  runB(1);
  {
    u64 cv[10];
    if (tid < 256) {
#pragma unroll
      for (int i = 1; i < 8; ++i) cv[i] = ld8(ws, gbase + ((cbj + i) & 7) * 2048);
    }
    runB(1);
    if (tid < 256) {
      float tot;
#pragma unroll 1
      for (unsigned sp = 0; sp < SPIN_CAP; ++sp) {
        bool ok = true; tot = part;
#pragma unroll
        for (int i = 1; i < 8; ++i) { ok &= (unsigned)(cv[i] >> 32) == tg; tot += __uint_as_float((unsigned)cv[i]); }
        if (__all(ok)) break;
#pragma unroll
        for (int i = 1; i < 8; ++i) cv[i] = ld8(ws, gbase + ((cbj + i) & 7) * 2048);
      }
      float other = dpp<0xB1>(tot);
      if ((tid & 1) == 0) {
        float m = tot * (1.f / FH);
        float v = rbf(fmaxf(other * (1.f / FH) - m * m, 0.f));
        float r = 1.f / rbf(sqrtf(rbf(v + 1e-5f)));
        stsf(L_FS + (tid >> 1) * 4, -rbf(m) * r);
        stsf(L_FS + 512 + (tid >> 1) * 4, r);
      }
    }
  }
  bar(); rd(sb, 0);
  {
    f32x4 mr[2], ri[2];
#pragma unroll
    for (int m = 0; m < 2; ++m) { const int rl = wm * 32 + m * 16 + 4 * g; mr[m] = ldsf4(L_FS + rl * 4); ri[m] = ldsf4(L_FS + 512 + rl * 4); }
#pragma unroll
    for (int n = 0; n < 3; ++n) {
      f32x4 a = accA[0][n] * ri[0] + mr[0], b = accA[1][n] * ri[1] + mr[1];
      st16(rws, WS_Z2N + (unsigned)(((cbj * 64 + panel * 4 + wm) * 6 + ca[n]) * 1024) + l * 16,
           (u32x4){cvt_pk(a[0], a[1]), cvt_pk(a[2], a[3]), cvt_pk(b[0], b[1]), cvt_pk(b[2], b[3])});
    }
  }
  mmB(1); rd(sb, 1); mmB(0); nxt();
  runB(2);
  wait_vm<0>();
  bar();
  if (tid == 0) st8u(ws, WS_F1 + (panel * 32 + 8 + cbj) * 64, tag);
  rd(sb, 0); mmB(1); rd(sb, 1); mmB(0); nxt();
  {
    u64 cv[10];
    if (tid >= 256 && tc < 96) csum_issue(ws, 16 + cbj, tc, cv);
    runB(2);
    if (tid >= 256 && tc < 96) csum_gather(ws, 16 + cbj, tc, tl, cv, L_CS + 512);
  }
  mmB(1);
  bar();
  {
    f32x4 cn[3];
#pragma unroll
    for (int n = 0; n < 3; ++n) { const unsigned a = L_CS + 512 + (ca[n] * 16 + 4 * g) * 4; cn[n] = ldsf4(a); }
    float mr[2], rinv[2];
#pragma unroll
    for (int m = 0; m < 2; ++m) { const int rl = wm * 32 + m * 16 + fr; mr[m] = ldsf(L_ST + rl * 4); rinv[m] = ldsf(L_ST + 512 + rl * 4); }
#pragma unroll
    for (int n = 0; n < 3; ++n) {
      u32x4 o;
#pragma unroll
      for (int m = 0; m < 2; ++m) {
        f32x4 v = accB[m][n] * rinv[m] + cn[n] * mr[m];
        v = gelu4(v);
        o[2 * m] = cvt_pk(v[0], v[1]); o[2 * m + 1] = cvt_pk(v[2], v[3]);
      }
      if (wm < 2) *(LAS u32x4*)(uintptr_t)(lds0 + L_KVT + 6144 + (wm * 6 + ca[n]) * 1024 + l * 16) = o;
      else st16(rws, WS_Z1 + (unsigned)(((panel * 8 + cbj) * 4 + wm) * 6 + ca[n]) * 1024 + l * 16, o);
    }
  }
}

__global__ __launch_bounds__(768, 3) void mega_kernel(Params P) {
  extern __shared__ __attribute__((aligned(16))) char smem[];
  const unsigned lds0 = (unsigned)(uintptr_t)(LAS char*)smem;
  char* ws = P.ws;
  const int tid = threadIdx.x, l = tid & 63;
  const int w = __builtin_amdgcn_readfirstlane(tid >> 6);
  const int g = l >> 4, fr = l & 15;
  const int b = blockIdx.x, xcd = b & 7, bi = b >> 3;
  const int panel1 = (xcd >> 1) * 4 + (bi & 3), cb1 = (xcd & 1) * 4 + ((bi >> 2) & 3);
  const bool isq = bi < 16;
  const int panel2 = 2 * panel1 + (isq ? 1 : 0), cb2 = cb1;
  const int panel3 = xcd * 4 + (bi & 3), cb3 = bi >> 2;
  const u64 tag = ld8(ws, WS_EP) + 1;
  if (w == 7 && bi < 20) {
    u64 pc; asm volatile("s_getpc_b64 %0" : "=s"(pc));
    gl2((const char*)(pc & ~127ull) + bi * 2048, lds0 + 3 * K1_STAGE, l * 16, l * 16);
  }

  if (w >= 8) {
    const int lw = w - 8;
    const unsigned vA0 = (unsigned)(((l >> 3) * D_ + (((l & 7) ^ (l >> 3)) << 3)) * 2);
    const unsigned vA1 = vA0 + 8 * D_ * 2 - 1024, vA2 = vA0 + 16 * D_ * 2 - 2048, vA3 = vA0 + 24 * D_ * 2 - 3072;
    const char* gA = (const char*)(P.x + (size_t)(panel1 * 128 + lw * 32) * D_);
    const unsigned v20 = (unsigned)(((l >> 3) * S_ + (((l & 7) ^ (l >> 4)) << 3)) * 2);
    const unsigned v21 = (unsigned)((((l >> 3) + 8) * S_ + (((l & 7) ^ ((l >> 4) | 4)) << 3)) * 2) - 1024;
    const char* gA2 = (const char*)(P.wsp + (size_t)(panel2 * 64 + lw * 16) * S_);
    const int rot2 = 0;
    auto ldA = [&](int kt, unsigned sl) { gl2(gA2 + ((kt + rot2) & 31) * 128, sl + lw * 2048, v20, v21); };
    auto sl2 = [&](int kt) { return lds0 + ((kt + 4) % 6) * P2_SLOT; };
    const unsigned tl = (unsigned)tag, th = tl * 0x9E3779B1u ^ (unsigned)(tag >> 32);
    const int lt = tid - 512;
    auto gather = [&](int blk, int ncol, unsigned dst, bool xs) {
      u64 cv[10]; u64 gv = 0;
      const int tc = lt < ncol ? lt : 0;
      if (xs) gv = ld8(ws, WS_GX + panel1 * 2048 + lt * 8);
      csum_issue(ws, blk, tc, cv);
      if (xs) xstat_gather(ws, panel1, lds0 + L_MISC, lt, (lt & 1) ? th : tl, gv);
      csum_gather(ws, blk, tc, tl, cv, dst);
    };
    const unsigned l16 = l * 16;
    const char* gB2 = ws + WS_Z2N + (size_t)cb2 * 64 * 6144 + lw * 3072;
    auto ldB = [&](int kt, unsigned sl) { gl3c(gB2 + ((kt + rot2) & 31) * 12288, sl + 8192 + lw * 3072, l16); };
    const unsigned zfl = WS_F1 + ((l & 15) * 32 + 8 + cb2) * 64;
    auto try_z = [&]() {
      const u64 v = l < 16 ? ld8(ws, zfl) : tag;
      return (bool)__builtin_amdgcn_readfirstlane((int)__all(v == tag));
    };
    bool early = false;
    const int rot = panel1 < 10 ? panel1 : 0;
    auto krot = [&](int kt) { const int k = kt >= 10 ? kt - 10 : kt; return __builtin_amdgcn_readfirstlane(k == 0 ? rot : (k - 1 < rot ? k - 1 : k)); };
    if (isq) {
      gl4(gA + rot * 128, lds0 + lw * 4096, vA0, vA1, vA2, vA3);
      unsigned vB[4];
#pragma unroll
      for (int i = 0; i < 4; ++i) {
        int c = i * 64 + l; if (c > 239) c = 239;
        const int row = c / 30, cc = c - row * 30, seg = cc / 10, ch = cc - seg * 10;
        vB[i] = (unsigned)((row * NQKV + seg * 640 + cb1 * 80 + ch * 8) * 2) + 4096 - i * 1024;
      }
      const char* gB = (const char*)P.wqkv + (size_t)lw * 16 * NQKV * 2 - 4096;
      auto stageB = [&](int kt, unsigned sb) {
        const char* bp = gB + (size_t)krot(kt) * 64 * NQKV * 2;
        gl4(bp, sb + 16384 + lw * 2 * KBS, vB[0], vB[1], vB[2], vB[3]);
        gl4(bp + 8 * NQKV * 2, sb + 16384 + (lw * 2 + 1) * KBS, vB[0], vB[1], vB[2], vB[3]);
      };
      auto stage = [&](int kt, unsigned sb) { gl4(gA + krot(kt) * 128, sb + lw * 4096, vA0, vA1, vA2, vA3); stageB(kt, sb); };
      stageB(0, lds0); stage(1, lds0 + K1_STAGE);
      wait_vm<12>(); bar();
      stage(2, lds0 + 2 * K1_STAGE);
      unsigned sb = lds0, sc = lds0 + K1_STAGE;
#pragma unroll 1
      for (int kt = 1; kt < 8; ++kt) {
        wait_vm<12>(); bar();
        stage(kt + 2, sb);
        sb = sc; sc += K1_STAGE; if (sc == lds0 + 3 * K1_STAGE) sc = lds0;
      }
      wait_vm<12>(); bar();
      gather(cb1, 240, lds0 + L_MISC + 5120, true);
      bar();
      ldA(0, sl2(0)); ldA(1, sl2(1)); ldA(5, sl2(5));
      bar();
      ldA(4, sl2(4));
#pragma unroll 1
      for (int t = 0; t < 3 && !early; ++t) early = try_z();
      if (early) { ldB(0, sl2(0)); ldB(1, sl2(1)); ldB(4, sl2(4)); ldB(5, sl2(5)); }
      bar();
      if (!early) { early = try_z(); if (early) { ldB(0, sl2(0)); ldB(1, sl2(1)); ldB(4, sl2(4)); ldB(5, sl2(5)); } }
      bar();
    } else {
      gl4(gA + rot * 128, lds0 + lw * 4096, vA0, vA1, vA2, vA3);
      unsigned vB[3];
#pragma unroll
      for (int i = 0; i < 3; ++i) {
        const int c = i * 64 + l, row = c / 12, cc = c - row * 12;
        int pc = (cc >> 1) - (row >> 3); if (pc < 0) pc = 5;
        vB[i] = (unsigned)((row * FIN + cb1 * 96 + pc * 16 + (cc & 1) * 8) * 2) + 4096 - i * 1024;
      }
      const char* gB = (const char*)P.win + (size_t)lw * 16 * FIN * 2 - 4096;
      auto stageB = [&](int kt, unsigned sb) {
        const int k = krot(kt);
        gl3(gB + (kt < 10 ? FH * 2 : 0) + (size_t)k * 64 * FIN * 2, sb + 16384 + lw * 3072, vB[0], vB[1], vB[2]);
      };
      auto stage = [&](int kt, unsigned sb) {
        const int k = krot(kt);
        gl4(gA + k * 128, sb + lw * 4096, vA0, vA1, vA2, vA3); stageB(kt, sb);
      };
      stageB(0, lds0); stage(1, lds0 + K1_STAGE);
      wait_vm<7>(); bar();
      stage(2, lds0 + 2 * K1_STAGE);
      unsigned sb = lds0, sc = lds0 + K1_STAGE;
      auto runL = [&](int k0, int k1) {
#pragma unroll 1
        for (int kt = k0; kt < k1; ++kt) {
          wait_vm<7>(); bar();
          stage(kt + 2, sb);
          sb = sc; sc += K1_STAGE; if (sc == lds0 + 3 * K1_STAGE) sc = lds0;
        }
      };
      runL(1, 11);
      {
        const char* wp = (const char*)(P.wsp + (size_t)(panel1 * 128 + lw * 32) * S_) + (cb1 & 3) * 1024;
        const unsigned vo = (unsigned)((l >> 3) * S_ * 2 + (l & 7) * 128);
        unsigned d0, d1, d2, d3;
        asm volatile("global_load_dword %0, %4, %5\n\tglobal_load_dword %1, %4, %6\n\tglobal_load_dword %2, %4, %7\n\tglobal_load_dword %3, %4, %8"
                     : "=&v"(d0), "=&v"(d1), "=&v"(d2), "=&v"(d3)
                     : "v"(vo), "s"(wp), "s"(wp + 8 * S_ * 2), "s"(wp + 16 * S_ * 2), "s"(wp + 24 * S_ * 2) : "memory");
        runL(11, 13);
        asm volatile("" :: "v"(d0), "v"(d1), "v"(d2), "v"(d3));
      }
      runL(13, 18);
      wait_vm<7>(); bar();
      wait_vm<0>(); bar();
      bar();
      wait_words(ws, zfl, l < 16, tag);
      early = true;
      ldA(0, sl2(0)); ldB(0, sl2(0)); ldA(1, sl2(1)); ldB(1, sl2(1));
      ldA(4, sl2(4)); ldA(5, sl2(5)); ldB(4, sl2(4)); ldB(5, sl2(5));
    }
    if (!early) {
      wait_words(ws, zfl, l < 16, tag);
      ldB(0, sl2(0)); ldB(1, sl2(1)); ldB(4, sl2(4)); ldB(5, sl2(5));
    }
    ldA(2, sl2(2)); ldA(3, sl2(3)); ldB(2, sl2(2)); ldB(3, sl2(3));
    wait_vm<16>(); bar();
    {
      unsigned sl = sl2(0);
      auto ldU = [&](int u) {
        ldA(2 * u, sl); ldB(2 * u, sl); ldA(2 * u + 1, sl + P2_SLOT); ldB(2 * u + 1, sl + P2_SLOT);
        sl += 2 * P2_SLOT; if (sl == lds0 + 6 * P2_SLOT) sl = lds0;
      };
      wait_vm<0>(); bar(); ldU(3);
#pragma unroll 1
      for (int u = 2; u < 14; ++u) {
        wait_vm<10>(); bar();
        ldU(u + 2);
      }
    }
    wait_vm<10>(); bar();
    wait_words(ws, WS_F1 + (((lw * 64 + l) >> 4) * 32 + 16 + (l & 15)) * 64, true, tag);
    wait_vm<0>(); bar();
    if (b == 0 && tid == 512) st8u(ws, WS_EP, tag);
    const char* gA3 = ws + WS_OH + (size_t)panel3 * 44 * 4096 + lw * 4096;
    const char* gB3 = ws + WS_WF + (size_t)cb3 * 44 * 5120 + lw * 5120;
    int r5 = cb3 + 2 * (bi & 3); r5 = r5 >= 10 ? r5 - 10 : r5 >= 5 ? r5 - 5 : r5;
    int r6 = cb3 + 2 * (bi & 3); r6 = r6 >= 12 ? r6 - 12 : r6 >= 6 ? r6 - 6 : r6;
    auto ur = [&](int u) {
      int v;
      if (u < 5) { v = u + r5; if (v >= 5) v -= 5; } else { v = u + r6; if (v >= 11) v -= 6; }
      return __builtin_amdgcn_readfirstlane(v);
    };
    auto ldB3 = [&](int u, unsigned sl) {
      gl4c(gB3 + ur(u) * 20480, sl + 16384 + lw * 5120, l16);
      glds16c(gB3 + ur(u) * 20480 + 4096, l16, sl + 16384 + lw * 5120 + 4096);
    };
    auto ldA3 = [&](int u, unsigned sl) { gl4c(gA3 + ur(u) * 16384, sl + lw * 4096, l16); };
    bar(); bar();
    ldB3(0, lds0); ldB3(1, lds0 + P3_SLOT); ldB3(2, lds0 + 2 * P3_SLOT);
    wait_words(ws, WS_FO + ((panel3 >> 1) * 8 + (l & 7)) * 64, l < 8, tag);
    ldA3(0, lds0); ldA3(1, lds0 + P3_SLOT); ldA3(2, lds0 + 2 * P3_SLOT);
    bar();
    ldA3(3, lds0 + 3 * P3_SLOT); ldB3(3, lds0 + 3 * P3_SLOT);
    wait_vm<17>(); bar();
    wait_vm<13>(); bar(); ldA3(4, lds0); ldB3(4, lds0);
    wait_vm<18>(); bar(); ldB3(5, lds0 + P3_SLOT);
    wait_vm<14>(); bar(); ldB3(6, lds0 + 2 * P3_SLOT);
    wait_vm<10>(); bar(); ldB3(7, lds0 + 3 * P3_SLOT);
    wait_words(ws, WS_F2 + (panel3 * 32 + (l & 31)) * 64, l < 32, tag);
    ldA3(5, lds0 + P3_SLOT); ldA3(6, lds0 + 2 * P3_SLOT); ldA3(7, lds0 + 3 * P3_SLOT);
    wait_vm<8>(); bar(); ldA3(8, lds0); ldB3(8, lds0);
    wait_vm<13>(); bar(); ldA3(9, lds0 + P3_SLOT); ldB3(9, lds0 + P3_SLOT);
    wait_vm<18>(); bar(); ldA3(10, lds0 + 2 * P3_SLOT); ldB3(10, lds0 + 2 * P3_SLOT);
    wait_vm<18>(); bar(); wait_vm<9>(); bar(); wait_vm<0>(); bar();
    bar(); bar();
    return;
  }

  const rsrc_t rws = __builtin_amdgcn_make_buffer_rsrc(ws, 0, (int)WS_END, 0x00020000);
  bf16x8 pf[3];
  if (isq) p1q_consumer(P, rws, w, panel1, cb1, lds0, tag, pf);
  else p1z_consumer(P, rws, w, panel1, cb1, lds0, tag);
  wf_job(P, rws, w);

  u32x4 xv[2];
  unsigned xo0, xo1, la0, la1;
  {
    const int wt = w & 3, kh = w >> 2;
    const int rsw = ((wt * 16 + fr) >> 1) & 7;
    const unsigned ao0 = (unsigned)((wt * 16 + fr) * 128 + (((4 * kh + (g >> 1)) ^ rsw) << 4) + (g & 1) * 8);
    const unsigned ao1 = (unsigned)((wt * 16 + fr) * 128 + (((4 * kh + 2 + (g >> 1)) ^ rsw) << 4) + (g & 1) * 8);
    const unsigned bo = (unsigned)(8192 + kh * 6144 + l * 16);
    const bf16x8 zeros = {0, 0, 0, 0, 0, 0, 0, 0};
    f32x4 acc[6];
#pragma unroll
    for (int n = 0; n < 6; ++n) acc[n] = (f32x4){0.f, 0.f, 0.f, 0.f};
    bf16x8 af[2], bfr[2][6];
    af[1] = zeros;
#pragma unroll
    for (int n = 0; n < 6; ++n) bfr[1][n] = zeros;
    const int zi = (bi & 15) * 8 + xcd;
    const bool reducer = !isq && zi < 72;
    const int rj = zi & 7, rf = zi >> 3;
    const int row = panel2 * 64 + wt * 16 + fr;
    u32x2 zreg[6];
    const float bs = bf2f(P.bsp[row]);
    unsigned sl = lds0 + 4 * P2_SLOT;
    auto step = [&](int cur, int kt) {
      if (cur == 0) bar();
      af[cur] = lds_2b64(sl + ao0, sl + ao1);
#pragma unroll
      for (int n = 0; n < 6; ++n) bfr[cur][n] = lds_b128(sl + bo + n * 1024);
      SB0;
#pragma unroll
      for (int n = 0; n < 6; ++n) acc[n] = MFMA(bfr[cur ^ 1][n], af[cur ^ 1], acc[n]);
      SB0;
      sl += P2_SLOT; if (sl == lds0 + 6 * P2_SLOT) sl = lds0;
    };
    auto run = [&](int k0, int k1) {
#pragma unroll 1
      for (int kt = k0; kt < k1; kt += 2) { step(0, kt); step(1, kt + 1); }
    };
    if (reducer) {
      const int et = (rf + 1) >> 1, st = rf == 0 ? 0 : ((rf + 1) & 1) + (et >= 3 ? 1 : 0);
      wait_words(ws, WS_F1 + ((l & 15) * 32 + rj) * 64, true, tag);
      f32x4 pv[8];
      bool okv[4];
#pragma unroll
      for (int t4 = 0; t4 < 4; ++t4) {
        const int i = 2 * st + (t4 & 1);
        okv[t4] = i < 5 && (t4 >= 2 || (et - i <= 1 && i - et <= 1));
        const int job = okv[t4] ? (t4 >= 2 ? 13 + i : 2 * i + et) : 0;
#pragma unroll
        for (int pp = 0; pp < 2; ++pp)
          pv[t4 * 2 + pp] = __builtin_bit_cast(f32x4, ld16(rws, WS_KVP + (unsigned)(((rj * 18 + job) * 16 + w * 2 + pp) * 1024) + l * 16));
      }
      run(0, 2);
      if (tid == 0) st8u(ws, WS_F1 + (panel1 * 32 + 16 + cb1) * 64, tag);
      const unsigned L_R = lds0 + 122880;
      f32x4 sv[4];
#pragma unroll
      for (int t4 = 0; t4 < 4; ++t4) { sv[t4] = pv[t4 * 2] + pv[t4 * 2 + 1]; if (!okv[t4]) sv[t4] = (f32x4){0.f, 0.f, 0.f, 0.f}; }
      if (w >= 4) {
#pragma unroll
        for (int t4 = 0; t4 < 4; ++t4) stsf4(L_R + ((w - 4) * 4 + t4) * 1024 + l * 16, sv[t4]);
      }
      run(2, 4);
      if (w < 4) {
#pragma unroll
        for (int t4 = 0; t4 < 4; ++t4) stsf4(L_R + (w * 4 + t4) * 1024 + l * 16, sv[t4] + ldsf4(L_R + (w * 4 + t4) * 1024 + l * 16));
      }
      run(4, 6);
      if (w < 2) {
        f32x4 v[2];
#pragma unroll
        for (int h2 = 0; h2 < 2; ++h2) {
          v[h2] = (f32x4){0.f, 0.f, 0.f, 0.f};
#pragma unroll
          for (int ww = 0; ww < 4; ++ww) v[h2] += ldsf4(L_R + (ww * 4 + w * 2 + h2) * 1024 + l * 16);
          const int he = (et * 16 + fr) / 20;
#pragma unroll
          for (int r = 0; r < 4; ++r) { const int d = (2 * st + h2) * 16 + 4 * g + r; if (d / 20 != he) v[h2][r] = 0.f; }
        }
        st16(rws, WS_KVT + (unsigned)(((rj * 2 + w) * 9 + rf) * 1024) + l * 16,
             (u32x4){cvt_pk(v[0][0], v[0][1]), cvt_pk(v[0][2], v[0][3]), cvt_pk(v[1][0], v[1][1]), cvt_pk(v[1][2], v[1][3])});
      }
      run(6, 10);
      wait_vm<0>();
      if (w < 2 && l == 0) st8u(ws, WS_FK + (rj * 32 + w * 16 + rf) * 64, tag);
      run(10, 18);
    } else {
      run(0, 6);
      wait_vm<0>();
      run(6, 8);
      if (tid == 0) st8u(ws, WS_F1 + (panel1 * 32 + (isq ? 24 : 16) + cb1) * 64, tag);
      run(8, 18);
    }
    if (isq) {
      const unsigned foff = (l & 15) < 9 ? WS_FK + (cb2 * 32 + (l & 31)) * 64 : WS_F1 + (panel1 * 32 + 16 + cb2) * 64;
      const u64 fv = ld8(ws, foff);
      run(18, 22);
      if (!__all(fv == tag)) wait_words(ws, foff, l < 32, tag);
#pragma unroll
      for (int i = 0; i < 3; ++i) {
        int ins = w + 8 * i; if (ins >= 18) ins = 17;
        ins = __builtin_amdgcn_readfirstlane(ins);
        glds16c(ws, WS_KVT + cb2 * 18432 + ins * 1024 + l * 16, __builtin_amdgcn_readfirstlane(lds0 + L_KVT + ins * 1024));
      }
      const unsigned zo = WS_Z1 + (unsigned)(((panel1 * 8 + cb2) * 4 + 2 + (wt >> 1)) * 6) * 1024 + l * 16 + (wt & 1) * 8;
#pragma unroll
      for (int n = 0; n < 6; ++n) zreg[n] = __builtin_amdgcn_raw_buffer_load_b64(rws, zo + n * 1024, 0, 16);
      run(22, 24);
      wait_vm<0>();
      run(24, 26);
      u32x2 ov[5];
#pragma unroll
      for (int et = 0; et < 5; ++et) {
        f32x4 num = {0.f, 0.f, 0.f, 0.f}, den = {0.f, 0.f, 0.f, 0.f};
        const int st0 = et >= 3 ? 1 : 0, nst = et == 0 ? 1 : 2, f0 = et == 0 ? 0 : 1 + (et - 1) * 2;
#pragma unroll
        for (int s = 0; s < nst; ++s) {
          bf16x8 ka = lds_b128(lds0 + L_KVT + (f0 + s) * 1024 + l * 16), za = lds_b128(lds0 + L_KVT + (9 + f0 + s) * 1024 + l * 16);
          num = MFMA(ka, pf[st0 + s], num); den = MFMA(za, pf[st0 + s], den);
        }
        float v[4];
#pragma unroll
        for (int j = 0; j < 4; ++j) v[j] = num[j] * __builtin_amdgcn_rcpf(den[j] + 1e-6f);
        ov[et] = (u32x2){cvt_pk(v[0], v[1]), cvt_pk(v[2], v[3])};
      }
      const unsigned ob = WS_OH + (unsigned)(((2 * panel1 + kh) * 44 + ((cb2 * 5) >> 1)) * 4 + wt) * 1024 + l * 16;
      if ((cb2 & 1) == 0) {
        st16(rws, ob, (u32x4){ov[0][0], ov[0][1], ov[1][0], ov[1][1]});
        st16(rws, ob + 4096, (u32x4){ov[2][0], ov[2][1], ov[3][0], ov[3][1]});
        st8(ws, ob + 8192, ov[4]);
      } else {
        st8(ws, ob + 8, ov[0]);
        st16(rws, ob + 4096, (u32x4){ov[1][0], ov[1][1], ov[2][0], ov[2][1]});
        st16(rws, ob + 8192, (u32x4){ov[3][0], ov[3][1], ov[4][0], ov[4][1]});
      }
      run(26, 30);
      wait_vm<0>();
      run(30, 32);
      if (tid == 0) st8u(ws, WS_FO + (panel1 * 8 + cb2) * 64, tag);
    } else {
      run(18, 32);
#pragma unroll
      for (int n = 0; n < 6; ++n) zreg[n] = *(LAS u32x2*)(uintptr_t)(lds0 + L_KVT + 6144 + ((wt >> 1) * 6 + n) * 1024 + l * 16 + (wt & 1) * 8);
    }
#pragma unroll
    for (int n = 0; n < 6; ++n) acc[n] = MFMA(bfr[1][n], af[1], acc[n]);
    {
      const int r0 = tid / 10, h0 = tid - r0 * 10, c1 = (tid & 127) + 512, r1 = c1 / 10, h1 = c1 - r1 * 10;
      xo0 = (unsigned)((panel3 * 64 + r0) * D_ + cb3 * 80 + h0 * 8); xo1 = (unsigned)((panel3 * 64 + r1) * D_ + cb3 * 80 + h1 * 8);
      la0 = lds0 + (unsigned)((r0 * 84 + h0 * 8) * 4); la1 = lds0 + (unsigned)((r1 * 84 + h1 * 8) * 4);
      xv[0] = *(const u32x4*)(P.x + xo0); xv[1] = *(const u32x4*)(P.x + xo1);
    }
    bar();
    const unsigned L_RED = lds0 + 3 * P3_SLOT;
    if (kh == 1) {
#pragma unroll
      for (int n = 0; n < 6; ++n) stsf4(L_RED + (wt * 6 + n) * 1024 + l * 16, acc[n]);
    }
    bar();
    if (kh == 0) {
#pragma unroll
      for (int n = 0; n < 6; ++n) acc[n] += ldsf4(L_RED + (wt * 6 + n) * 1024 + l * 16);
      u32x2 hv[6];
#pragma unroll
      for (int n = 0; n < 6; ++n) {
        f32x4 gg = acc[n] + bs;
        hv[n] = (u32x2){cvt_pk(lo16(zreg[n][0]) * gg[0], hi16(zreg[n][0]) * gg[1]), cvt_pk(lo16(zreg[n][1]) * gg[2], hi16(zreg[n][1]) * gg[3])};
      }
      const unsigned hb = WS_OH + (unsigned)((panel2 * 44 + 20 + cb2 * 3) * 4 + wt) * 1024 + l * 16;
#pragma unroll
      for (int s = 0; s < 3; ++s) st16(rws, hb + s * 4096, (u32x4){hv[2 * s][0], hv[2 * s][1], hv[2 * s + 1][0], hv[2 * s + 1][1]});
      wait_vm<0>();
      if (l == 0) st8u(ws, WS_F2 + ((panel2 * 8 + cb2) * 4 + wt) * 64, tag);
    }
    bar();
  }
  {
    const int wsr = w & 3, kh = w >> 2;
    const unsigned ao = (unsigned)(kh * 8192 + wsr * 1024 + l * 16);
    const unsigned bb = 16384u + kh * 10240 + l * 16;
    const bf16x8 zeros = {0, 0, 0, 0, 0, 0, 0, 0};
    f32x4 acc[5];
#pragma unroll
    for (int n = 0; n < 5; ++n) acc[n] = (f32x4){0.f, 0.f, 0.f, 0.f};
    bf16x8 af[2][2], bfr[2][2][5];
    unsigned sb = lds0;
    auto mm = [&](int c) {
#pragma unroll
      for (int s2 = 0; s2 < 2; ++s2)
#pragma unroll
        for (int n = 0; n < 5; ++n) acc[n] = MFMA(bfr[c][s2][n], af[c][s2], acc[n]);
      SB0;
    };
    auto rdu = [&](int c) {
      bar();
#pragma unroll
      for (int s2 = 0; s2 < 2; ++s2) {
        af[c][s2] = lds_b128(sb + ao + s2 * 4096);
#pragma unroll
        for (int n = 0; n < 5; ++n) bfr[c][s2][n] = lds_b128(sb + bb + s2 * 5120 + n * 1024);
      }
      SB0;
      sb += P3_SLOT; if (sb == lds0 + 4 * P3_SLOT) sb = lds0;
    };
    rdu(0);
#pragma unroll 1
    for (int u = 0; u < 5; ++u) { rdu(1); mm(0); rdu(0); mm(1); }
    mm(0);
    bar();
#pragma unroll
    for (int n = 0; n < 5; ++n) stsf4(lds0 + (unsigned)(((kh * 64 + wsr * 16 + fr) * 84 + n * 16 + 4 * g) * 4), acc[n]);
    bar();
#pragma unroll
    for (int i = 0; i < 2; ++i) {
      if (i == 0 || tid < 128) {
        const unsigned a = i == 0 ? la0 : la1;
        f32x4 a0 = ldsf4(a) + ldsf4(a + 64 * 84 * 4), a1 = ldsf4(a + 16) + ldsf4(a + 64 * 84 * 4 + 16);
        u32x4 o = {cvt_pk(lo16(xv[i][0]) + a0[0], hi16(xv[i][0]) + a0[1]), cvt_pk(lo16(xv[i][1]) + a0[2], hi16(xv[i][1]) + a0[3]),
                   cvt_pk(lo16(xv[i][2]) + a1[0], hi16(xv[i][2]) + a1[1]), cvt_pk(lo16(xv[i][3]) + a1[2], hi16(xv[i][3]) + a1[3])};
        *(u32x4*)(P.out + (i == 0 ? xo0 : xo1)) = o;
      }
    }
  }
}

extern "C" void kernel_launch(void* const* d_in, const int* in_sizes, int n_in, void* d_out, int out_size,
                              void* d_ws, size_t ws_size, hipStream_t stream) {
  Params P;
  P.x = (const u16*)d_in[0];
  P.wqkv = (const u16*)d_in[1];
  P.wo = (const u16*)d_in[2];
  P.win = (const u16*)d_in[4];
  P.wsp = (const u16*)d_in[5];
  P.bsp = (const u16*)d_in[6];
  P.wout = (const u16*)d_in[7];
  P.out = (u16*)d_out;
  P.ws = (char*)d_ws;
  (void)hipFuncSetAttribute((const void*)mega_kernel, hipFuncAttributeMaxDynamicSharedMemorySize, LDS_TOTAL);
  mega_kernel<<<256, 768, LDS_TOTAL, stream>>>(P);
}
```

```cpp
#include <hip/hip_runtime.h>
#include <hip/hip_bf16.h>
#include <stdint.h>

typedef __attribute__((ext_vector_type(8))) short bf16x8;
typedef __attribute__((ext_vector_type(4))) short bf16x4;
typedef __attribute__((ext_vector_type(4))) float f32x4;
typedef __attribute__((ext_vector_type(2))) float f32x2;
typedef __attribute__((ext_vector_type(2))) __bf16 bf2;
typedef __attribute__((ext_vector_type(4))) unsigned u32x4;
typedef __attribute__((ext_vector_type(2))) unsigned u32x2;
typedef unsigned short u16;
typedef unsigned long long u64;
#define LAS __attribute__((address_space(3)))
#define GAS __attribute__((address_space(1)))
#define DI __device__ __forceinline__
#define RLX_AGENT __ATOMIC_RELAXED, __HIP_MEMORY_SCOPE_AGENT

constexpr int S_ = 2048, D_ = 640, NQKV = 1920, FIN = 1536, FH = 768;

constexpr unsigned WS_PQ  = 0;
constexpr unsigned WS_Z1  = WS_PQ + 16 * 8 * 8 * 3 * 1024;
constexpr unsigned WS_Z2N = WS_Z1 + 16 * 8 * 24 * 1024;
constexpr unsigned WS_OH  = WS_Z2N + 8 * 64 * 6 * 1024;
constexpr unsigned WS_WF  = WS_OH + 32 * 44 * 4 * 1024;
constexpr unsigned WS_KVT = WS_WF + 8 * 44 * 5 * 1024;
constexpr unsigned WS_KVP = WS_KVT + 8 * 18 * 1024;
constexpr unsigned WS_GR  = WS_KVP + 8 * 18 * 16 * 1024;
constexpr unsigned WS_GX  = WS_GR + 16 * 8 * 256 * 8;
constexpr unsigned WS_GC  = WS_GX + 2048 * 16;
constexpr unsigned WS_F1  = WS_GC + 24 * 10 * 2048;
constexpr unsigned WS_F2  = WS_F1 + 16 * 32 * 64;
constexpr unsigned WS_FO  = WS_F2 + 1024 * 64;
constexpr unsigned WS_FK  = WS_FO + 256 * 64;
constexpr unsigned WS_EP  = WS_FK + 256 * 64;
constexpr unsigned WS_END = WS_EP + 64;

#define STAMP(i)

DI float bf2f(u16 v) { return __uint_as_float(((unsigned)v) << 16); }
DI unsigned cvt_pk(float lo, float hi) {
  bf2 r; r[0] = (__bf16)lo; r[1] = (__bf16)hi; return __builtin_bit_cast(unsigned, r);
}
DI float rbf(float x) { return (float)((__bf16)x); }
DI float lo16(unsigned u) { return __uint_as_float(u << 16); }
DI float hi16(unsigned u) { return __uint_as_float(u & 0xffff0000u); }

DI void glds16(const void* gbase, unsigned voff, unsigned lds_dst) {
  unsigned keep;
  asm volatile("s_mov_b32 %0, m0\n\ts_mov_b32 m0, %3\n\ts_nop 0\n\tglobal_load_lds_dwordx4 %1, %2\n\ts_mov_b32 m0, %0"
               : "=&s"(keep) : "v"(voff), "s"(gbase), "s"(lds_dst) : "memory");
}
DI void glds16c(const void* gbase, unsigned voff, unsigned lds_dst) {
  unsigned keep;
  asm volatile("s_mov_b32 %0, m0\n\ts_mov_b32 m0, %3\n\ts_nop 0\n\tglobal_load_lds_dwordx4 %1, %2 sc1\n\ts_mov_b32 m0, %0"
               : "=&s"(keep) : "v"(voff), "s"(gbase), "s"(lds_dst) : "memory");
}
#define GL_PRE "s_mov_b32 %0, m0\n\ts_mov_b32 m0, %2\n\ts_nop 0\n\t"
#define GL_POST "s_mov_b32 m0, %0"
DI void gl2(const void* gb, unsigned lds, unsigned v0, unsigned v1) {
  unsigned keep;
  asm volatile(GL_PRE "global_load_lds_dwordx4 %3, %1\n\tglobal_load_lds_dwordx4 %4, %1 offset:1024\n\t" GL_POST
               : "=&s"(keep) : "s"(gb), "s"(lds), "v"(v0), "v"(v1) : "memory");
}
DI void gl3(const void* gb, unsigned lds, unsigned v0, unsigned v1, unsigned v2) {
  unsigned keep;
  asm volatile(GL_PRE "global_load_lds_dwordx4 %3, %1\n\tglobal_load_lds_dwordx4 %4, %1 offset:1024\n\tglobal_load_lds_dwordx4 %5, %1 offset:2048\n\t" GL_POST
               : "=&s"(keep) : "s"(gb), "s"(lds), "v"(v0), "v"(v1), "v"(v2) : "memory");
}
DI void gl4(const void* gb, unsigned lds, unsigned v0, unsigned v1, unsigned v2, unsigned v3) {
  unsigned keep;
  asm volatile(GL_PRE "global_load_lds_dwordx4 %3, %1\n\tglobal_load_lds_dwordx4 %4, %1 offset:1024\n\tglobal_load_lds_dwordx4 %5, %1 offset:2048\n\tglobal_load_lds_dwordx4 %6, %1 offset:3072\n\t" GL_POST
               : "=&s"(keep) : "s"(gb), "s"(lds), "v"(v0), "v"(v1), "v"(v2), "v"(v3) : "memory");
}
DI void gl3c(const void* gb, unsigned lds, unsigned v0) {
  unsigned keep;
  asm volatile(GL_PRE "global_load_lds_dwordx4 %3, %1 sc1\n\tglobal_load_lds_dwordx4 %3, %1 offset:1024 sc1\n\tglobal_load_lds_dwordx4 %3, %1 offset:2048 sc1\n\t" GL_POST
               : "=&s"(keep) : "s"(gb), "s"(lds), "v"(v0) : "memory");
}
DI void gl4c(const void* gb, unsigned lds, unsigned v0) {
  unsigned keep;
  asm volatile(GL_PRE "global_load_lds_dwordx4 %3, %1 sc1\n\tglobal_load_lds_dwordx4 %3, %1 offset:1024 sc1\n\tglobal_load_lds_dwordx4 %3, %1 offset:2048 sc1\n\tglobal_load_lds_dwordx4 %3, %1 offset:3072 sc1\n\t" GL_POST
               : "=&s"(keep) : "s"(gb), "s"(lds), "v"(v0) : "memory");
}
template <int N> DI void wait_vm() { asm volatile("s_waitcnt vmcnt(%0)" :: "n"(N) : "memory"); }

DI bf16x8 lds_b128(unsigned addr) { return *(LAS bf16x8*)(uintptr_t)addr; }
DI bf16x8 lds_2b64(unsigned addr0, unsigned addr1) {
  bf16x4 a = *(LAS bf16x4*)(uintptr_t)addr0, b = *(LAS bf16x4*)(uintptr_t)addr1;
  return __builtin_shufflevector(a, b, 0, 1, 2, 3, 4, 5, 6, 7);
}
DI bf16x8 lds_tr(unsigned addr0, unsigned addr1) {
  bf16x4 a = __builtin_amdgcn_ds_read_tr16_b64_v4i16((LAS bf16x4*)(uintptr_t)addr0);
  bf16x4 b = __builtin_amdgcn_ds_read_tr16_b64_v4i16((LAS bf16x4*)(uintptr_t)addr1);
  return __builtin_shufflevector(a, b, 0, 1, 2, 3, 4, 5, 6, 7);
}
DI float ldsf(unsigned a) { return *(LAS float*)(uintptr_t)a; }
DI void stsf(unsigned a, float v) { *(LAS float*)(uintptr_t)a = v; }
DI f32x4 ldsf4(unsigned a) { return *(LAS f32x4*)(uintptr_t)a; }
DI void stsf4(unsigned a, f32x4 v) { *(LAS f32x4*)(uintptr_t)a = v; }
#define SB0 __builtin_amdgcn_sched_barrier(0)
#define MFMA(a, b, c) __builtin_amdgcn_mfma_f32_16x16x32_bf16((a), (b), (c), 0, 0, 0)

template <int CTRL> DI float dpp(float v) {
  return __builtin_bit_cast(float, __builtin_amdgcn_update_dpp(0, __builtin_bit_cast(int, v), CTRL, 0xf, 0xf, true));
}
DI float sum8(float v) { v += dpp<0xB1>(v); v += dpp<0x4E>(v); v += dpp<0x141>(v); return v; }
DI float sum16(float v) { v = sum8(v); v += dpp<0x140>(v); return v; }

DI float gelu_t(float x) {
  float u = x * (0.7978845608f + 0.0356774081f * x * x);
  float e = __builtin_amdgcn_exp2f(-2.885390082f * u);
  return x * __builtin_amdgcn_rcpf(1.0f + e);
}
DI f32x4 gelu4(f32x4 x) {
  const f32x4 a = x * ((x * x) * -0.1029432397f + -2.302208198f);
  f32x4 e;
#pragma unroll
  for (int j = 0; j < 4; ++j) e[j] = __builtin_amdgcn_exp2f(a[j]);
  e += 1.0f;
#pragma unroll
  for (int j = 0; j < 4; ++j) e[j] = __builtin_amdgcn_rcpf(e[j]);
  return x * e;
}
DI f32x4 phi4(f32x4 t) {
  f32x4 a, m;
#pragma unroll
  for (int j = 0; j < 4; ++j) { a[j] = fminf(t[j], 0.f); m[j] = fmaxf(t[j], 0.f); }
  a *= 1.4426950409f;
#pragma unroll
  for (int j = 0; j < 4; ++j) a[j] = __builtin_amdgcn_exp2f(a[j]);
  return (m + 1e-6f) + a;
}
DI float phi_f(float t) {
  return (fmaxf(t, 0.f) + 1e-6f) + __builtin_amdgcn_exp2f(fminf(t, 0.f) * 1.4426950409f);
}

typedef __amdgpu_buffer_rsrc_t rsrc_t;
DI void st16(rsrc_t r, unsigned off, u32x4 v) { __builtin_amdgcn_raw_buffer_store_b128(v, r, off, 0, 16); }
DI u32x4 ld16(rsrc_t r, unsigned off) { return __builtin_amdgcn_raw_buffer_load_b128(r, off, 0, 16); }
DI void st8(char* ws, unsigned off, u32x2 v) { __hip_atomic_store((GAS u64*)(ws + off), ((u64)v[1] << 32) | v[0], RLX_AGENT); }
DI void st8u(char* ws, unsigned off, u64 v) { __hip_atomic_store((GAS u64*)(ws + off), v, RLX_AGENT); }
DI void st4(char* ws, unsigned off, float v) { __hip_atomic_store((GAS unsigned*)(ws + off), __float_as_uint(v), RLX_AGENT); }
DI u64 ld8(const char* ws, unsigned off) { return __hip_atomic_load((GAS u64*)(ws + off), RLX_AGENT); }
constexpr unsigned SPIN_CAP = 1u << 20;
DI void wait_words(const char* ws, unsigned off, bool act, u64 tag) {
#pragma unroll 1
  for (unsigned sp = 0; sp < SPIN_CAP; ++sp) {
    u64 v = act ? ld8(ws, off) : tag;
    if (__all(v == tag)) break;
    __builtin_amdgcn_s_sleep(1);
  }
  __builtin_amdgcn_fence(__ATOMIC_ACQUIRE, "wavefront");
}

struct Params { const u16 *x, *wqkv, *wo, *win, *wsp, *bsp, *wout; u16* out; char* ws; };

constexpr unsigned KBS = 4224;
constexpr unsigned K1_STAGE = 16384 + 8 * KBS;
constexpr unsigned L_MISC = 153600;
constexpr int LDS_TOTAL = 163840;
constexpr unsigned P2_SLOT = 20480;
constexpr unsigned L_KVT = 135168;
constexpr unsigned P3_SLOT = 36864;

DI void bar() { asm volatile("s_waitcnt lgkmcnt(0)" ::: "memory"); __builtin_amdgcn_s_barrier(); asm volatile("" ::: "memory"); }

struct Side { u32x4 r0, r1; };
DI void side_issue(const Params& P, Side& S, int row, int w) {
  const int l = threadIdx.x & 63;
  const u32x4* src = (const u32x4*)(P.x + (size_t)row * D_);
  S.r0 = src[l]; S.r1 = src[64 + (l & 15)];
}
DI float rdl(float v, int lane) { return __builtin_bit_cast(float, __builtin_amdgcn_readlane(__builtin_bit_cast(int, v), lane)); }
DI void side_finish(const Params& P, rsrc_t rws, const Side& S, int row, int w, unsigned tl, unsigned th) {
  const int l = threadIdx.x & 63;
  float s = 0.f, q = 0.f, s2 = 0.f, q2 = 0.f;
#pragma unroll
  for (int j = 0; j < 4; ++j) {
    float a = lo16(S.r0[j]), b = hi16(S.r0[j]), c = lo16(S.r1[j]), d = hi16(S.r1[j]);
    s += a + b; q += a * a + b * b; s2 += c + d; q2 += c * c + d * d;
  }
  s = sum16(s); q = sum16(q); s2 = sum16(s2); q2 = sum16(q2);
  s = (rdl(s, 0) + rdl(s, 16)) + (rdl(s, 32) + rdl(s, 48)) + s2;
  q = (rdl(q, 0) + rdl(q, 16)) + (rdl(q, 32) + rdl(q, 48)) + q2;
  const float m = s * (1.f / D_);
  const float v = rbf(fmaxf(q * (1.f / D_) - m * m, 0.f));
  const float r = 1.f / rbf(sqrtf(rbf(v + 1e-5f)));
  if (l < 2) st8u(P.ws, WS_GX + row * 16 + l * 8, l == 0 ? (((u64)tl << 32) | __float_as_uint(-rbf(m) * r)) : (((u64)th << 32) | __float_as_uint(r)));
}
DI void wf_job(const Params& P, rsrc_t rws, int w) {
  const int l = threadIdx.x & 63;
  int item = blockIdx.x; if (item > 219) item = 219;
  const int st = item / 5, T = (item - st * 5) * 8 + w, cb = T / 5, nt = T - cb * 5;
  const u16* wsrc = (st < 20 ? P.wo + (size_t)st * 32 * D_ : P.wout + (size_t)(st - 20) * 32 * D_) + (size_t)(l >> 4) * 4 * D_ + T * 16 + (l & 15);
  unsigned wv[8];
#pragma unroll
  for (int e = 0; e < 8; ++e) wv[e] = wsrc[(size_t)(16 * (e >> 2) + (e & 3)) * D_];
  st16(rws, WS_WF + (unsigned)(((cb * 44 + st) * 5 + nt) * 1024) + l * 16,
       (u32x4){wv[0] | (wv[1] << 16), wv[2] | (wv[3] << 16), wv[4] | (wv[5] << 16), wv[6] | (wv[7] << 16)});
}
DI void xstat_gather(const char* ws, int panel, unsigned L_ST, int tid, unsigned tg, u64 gv) {
  if (tid < 256) {
#pragma unroll 1
    for (unsigned sp = 0; sp < SPIN_CAP; ++sp) {
      if (__all((unsigned)(gv >> 32) == tg)) break;
      __builtin_amdgcn_s_sleep(1);
      gv = ld8(ws, WS_GX + panel * 2048 + tid * 8);
    }
    stsf(L_ST + (tid & 1) * 512 + (tid >> 1) * 4, __uint_as_float((unsigned)gv));
  }
}

DI void csum_issue(const char* ws, int blk, int t, u64 (&cv)[10]) {
#pragma unroll
  for (int k = 0; k < 10; ++k) cv[k] = ld8(ws, WS_GC + (blk * 10 + k) * 2048 + t * 8);
}
DI void csum_gather(const char* ws, int blk, int t, unsigned tl, u64 (&cv)[10], unsigned dst) {
#pragma unroll 1
  for (unsigned sp = 0; sp < SPIN_CAP; ++sp) {
    bool ok = true;
#pragma unroll
    for (int k = 0; k < 10; ++k) ok &= (unsigned)(cv[k] >> 32) == tl;
    if (__all(ok)) break;
    __builtin_amdgcn_s_sleep(1);
    csum_issue(ws, blk, t, cv);
  }
  float a = 0.f;
#pragma unroll
  for (int k = 0; k < 10; ++k) a += __uint_as_float((unsigned)cv[k]);
  stsf(dst + t * 4, a);
}
DI void csum_store(char* ws, unsigned off, f32x4 d, int fr, int g, unsigned tl) {
  const float v = fr == 0 ? d[0] : fr == 1 ? d[1] : fr == 2 ? d[2] : d[3];
  if (fr < 4) st8u(ws, off + (4 * g + fr) * 8, ((u64)tl << 32) | __float_as_uint(v));
}

DI void p1q_consumer(const Params& P, rsrc_t rws, int w, int panel, int cbi, unsigned lds0, u64 tag, bf16x8 (&pf)[3]) {
  char* ws = P.ws;
  const int tid = threadIdx.x, l = tid & 63;
  const int wm = w >> 2, wc = (w + 2 * wm) & 3;
  const int g = l >> 4, fr = l & 15, q = (l & 15) >> 2, p = l & 3, g1 = g & 1;
  const unsigned aoff0 = (unsigned)((wm * 64 + fr) * 128 + ((g ^ (l & 7)) << 4));
  const unsigned ttab = (unsigned)(0x4498E765DC32BA10ull >> (16 * wc)) & 0xffffu;
  int tile[4];
  unsigned bo[4];
#pragma unroll
  for (int n = 0; n < 4; ++n) {
    tile[n] = (ttab >> (4 * n)) & 15;
    bo[n] = 16384u + g * KBS + q * 480 + p * 8 + tile[n] * 32;
  }
  const unsigned tl = (unsigned)tag, th = tl * 0x9E3779B1u ^ (unsigned)(tag >> 32);
  Side S; side_issue(P, S, panel * 128 + cbi * 8 + w, w);
  f32x4 acc[4][4];
#pragma unroll
  for (int m = 0; m < 4; ++m)
#pragma unroll
    for (int n = 0; n < 4; ++n) acc[m][n] = (f32x4){0.f, 0.f, 0.f, 0.f};
  const short one = 0x3F80;
  const bf16x8 ones = {one, one, one, one, one, one, one, one};
  const bf16x8 zeros = {0, 0, 0, 0, 0, 0, 0, 0};

  bf16x8 af[2][4], bfr[2][4];
#pragma unroll
  for (int m = 0; m < 4; ++m) { af[1][m] = zeros; bfr[1][m] = zeros; }
  auto mm = [&](int ks) {
#pragma unroll
    for (int n = 0; n < 3; ++n)
#pragma unroll
      for (int m = 0; m < 4; ++m) acc[m][n] = MFMA(bfr[ks][n], af[ks][m], acc[m][n]);
    if (wc < 3) {
#pragma unroll
      for (int m = 0; m < 4; ++m) acc[m][3] = MFMA(bfr[ks][3], af[ks][m], acc[m][3]);
    }
  };
  auto step = [&](unsigned sb, int ks) {
#pragma unroll
    for (int n = 0; n < 4; ++n) bfr[ks][n] = lds_tr(sb + ks * 4 * KBS + bo[n], sb + ks * 4 * KBS + bo[n] + 1920);
#pragma unroll
    for (int m = 0; m < 4; ++m) af[ks][m] = lds_b128(sb + ((aoff0 ^ (ks * 64)) + m * 2048));
    SB0;
    mm(ks ^ 1);
    SB0;
  };
  unsigned sb = lds0;
  auto run = [&](int n) {
#pragma unroll 1
    for (int kt = 0; kt < n; ++kt) {
      bar();
      step(sb, 0);
      step(sb, 1);
      sb += K1_STAGE; if (sb == lds0 + 3 * K1_STAGE) sb = lds0;
    }
  };
  run(1);
  side_finish(P, rws, S, panel * 128 + cbi * 8 + w, w, tl, th);
  run(2);
  if (panel < 10) {
#pragma unroll
    for (int h = 0; h < 2; ++h) {
      const bf16x8 b0 = wm == 0 ? bfr[0][h] : bfr[0][2 + h], b1 = wm == 0 ? bfr[1][h] : bfr[1][2 + h];
      const int tt = wm == 0 ? tile[h] : tile[2 + h];
      f32x4 d = MFMA(b0, ones, ((f32x4){0.f, 0.f, 0.f, 0.f}));
      d = MFMA(b1, ones, d);
      csum_store(ws, WS_GC + (cbi * 10 + panel) * 2048 + tt * 128, d, fr, g, tl);
    }
  }
  run(7);
  mm(1);
  STAMP(1);
  bar();
  STAMP(10);
  const unsigned L_ST = lds0 + L_MISC, L_CS = lds0 + L_MISC + 5120;
  const unsigned L_K = lds0, L_V = L_K + 20480, L_Q = lds0 + 122880;
  float mr[4], rinv[4];
#pragma unroll
  for (int m = 0; m < 4; ++m) { const int rl = wm * 64 + m * 16 + fr; mr[m] = ldsf(L_ST + rl * 4); rinv[m] = ldsf(L_ST + 512 + rl * 4); }
#pragma unroll
  for (int n = 0; n < 4; ++n) {
    if (n < 3 || wc < 3) {
      const int t = tile[n];
      const f32x4 cs = ldsf4(L_CS + (t * 16 + 4 * g) * 4);
      unsigned dst, dstep;
      if (t < 5) { dst = L_Q + (wm * 12 + (t >> 1)) * 1024 + l * 16 + (t & 1) * 8; dstep = 3072; }
      else {
        const int kb = wm * 8 + (fr >> 3), pos = (fr & 7) ^ ((fr >> 3) << 2);
        dst = (t < 10 ? L_K + (t - 5) * 256 : L_V + (t - 10) * 256) + kb * 1280 + pos * 32 + g * 8; dstep = 2560;
      }
      if (t < 10) {
#pragma unroll
        for (int m = 0; m < 4; ++m) {
          f32x4 v = acc[m][n] * rinv[m] + cs * mr[m];
          v = phi4(v);
          *(LAS u32x2*)(uintptr_t)(dst + m * dstep) = (u32x2){cvt_pk(v[0], v[1]), cvt_pk(v[2], v[3])};
        }
      } else {
#pragma unroll
        for (int m = 0; m < 4; ++m) {
          f32x4 v = acc[m][n] * rinv[m] + cs * mr[m];
          *(LAS u32x2*)(uintptr_t)(dst + m * dstep) = (u32x2){cvt_pk(v[0], v[1]), cvt_pk(v[2], v[3])};
        }
      }
    }
  }
  STAMP(8);
  bar();
  {
    const unsigned troff = (unsigned)(g * 5 * 256 + ((4 * g1 + q) * 32) + p * 8);
    f32x4 d[3];
    int jb[3];
#pragma unroll
    for (int r = 0; r < 3; ++r) {
      int job = w + 8 * r; if (job >= 18) job = w;
      jb[r] = job;
      const int i = job < 13 ? (job + 1) / 3 : job - 13;
      const int j = job < 13 ? job - 2 * i : 0;
      d[r] = (f32x4){0.f, 0.f, 0.f, 0.f};
#pragma unroll
      for (int ks = 0; ks < 4; ++ks) {
        const unsigned a0 = L_K + troff + (ks * 20 + i) * 256, b0 = L_V + troff + (ks * 20 + j) * 256;
        const bf16x8 ka = lds_tr(a0, a0 ^ 128);
        bf16x8 vb = lds_tr(b0, b0 ^ 128);
        if (job >= 13) vb = ones;
        d[r] = MFMA(ka, vb, d[r]);
      }
    }
#pragma unroll
    for (int r = 0; r < 3; ++r)
      st16(rws, WS_KVP + (unsigned)(((cbi * 18 + jb[r]) * 16 + panel) * 1024) + l * 16, __builtin_bit_cast(u32x4, d[r]));
    pf[0] = lds_b128(L_Q + w * 3072 + l * 16); pf[1] = lds_b128(L_Q + w * 3072 + 1024 + l * 16);
    const u32x2 q2 = *(LAS u32x2*)(uintptr_t)(L_Q + w * 3072 + 2048 + l * 16);
    pf[2] = __builtin_bit_cast(bf16x8, ((u32x4){q2[0], q2[1], 0u, 0u}));
  }
  STAMP(9);
  wait_vm<0>();
  bar();
  if (tid == 0) st8u(ws, WS_F1 + (panel * 32 + cbi) * 64, tag);
}

DI void p1z_consumer(const Params& P, rsrc_t rws, int w, int panel, int cbj, unsigned lds0, u64 tag) {
  char* ws = P.ws;
  const int tid = threadIdx.x, l = tid & 63;
  const int wm = w & 3, wn = w >> 2;
  const int g = l >> 4, fr = l & 15, q = (l & 15) >> 2, p = l & 3, g1 = g & 1;
  const unsigned aoff0 = (unsigned)((wm * 32 + fr) * 128 + ((g ^ (l & 7)) << 4));
  unsigned bo[3];
  int ca[3];
#pragma unroll
  for (int n = 0; n < 3; ++n) {
    const int c = wn * 3 + n;
    ca[n] = c;
    int pc = c + g1; if (pc == 6) pc = 0;
    bo[n] = 16384u + g * 1536 + q * 192 + pc * 32 + p * 8;
  }
  const short one = 0x3F80;
  const bf16x8 ones = {one, one, one, one, one, one, one, one};
  const bf16x8 zeros = {0, 0, 0, 0, 0, 0, 0, 0};
  f32x4 accA[2][3], accB[2][3];
#pragma unroll
  for (int m = 0; m < 2; ++m)
#pragma unroll
    for (int n = 0; n < 3; ++n) { accA[m][n] = (f32x4){0.f, 0.f, 0.f, 0.f}; accB[m][n] = (f32x4){0.f, 0.f, 0.f, 0.f}; }
  const unsigned tl = (unsigned)tag, th = tl * 0x9E3779B1u ^ (unsigned)(tag >> 32), tg = (tid & 1) ? th : tl;
  bf16x8 af[2][2], bfr[2][3];
  af[1][0] = af[1][1] = zeros; bfr[1][0] = bfr[1][1] = bfr[1][2] = zeros;
  auto rd = [&](unsigned sb, int ks) {
#pragma unroll
    for (int m = 0; m < 2; ++m) af[ks][m] = lds_b128(sb + ((aoff0 ^ (ks * 64)) + m * 2048));
#pragma unroll
    for (int n = 0; n < 3; ++n) { const unsigned o = sb + ks * 4 * 1536 + bo[n]; bfr[ks][n] = lds_tr(o, o + 768); }
    SB0;
  };
  auto mmA = [&](int ks) {
#pragma unroll
    for (int n = 0; n < 3; ++n)
#pragma unroll
      for (int m = 0; m < 2; ++m) accA[m][n] = MFMA(af[ks][m], bfr[ks][n], accA[m][n]);
    SB0;
  };
  auto mmB = [&](int ks) {
#pragma unroll
    for (int n = 0; n < 3; ++n)
#pragma unroll
      for (int m = 0; m < 2; ++m) accB[m][n] = MFMA(bfr[ks][n], af[ks][m], accB[m][n]);
    SB0;
  };
  const unsigned L_ST = lds0 + L_MISC, L_CS = lds0 + L_MISC + 5120;
  const unsigned L_RS = lds0 + L_MISC + 2048;
  const unsigned L_FS = lds0 + L_MISC + 4096;
  unsigned sb = lds0;
  auto nxt = [&]() { sb += K1_STAGE; if (sb == lds0 + 3 * K1_STAGE) sb = lds0; };
  auto runA = [&](int n) {
#pragma unroll 1
    for (int kt = 0; kt < n; ++kt) { bar(); rd(sb, 0); mmA(1); rd(sb, 1); mmA(0); nxt(); }
  };
  auto runB = [&](int n) {
#pragma unroll 1
    for (int kt = 0; kt < n; ++kt) { bar(); rd(sb, 0); mmB(1); rd(sb, 1); mmB(0); nxt(); }
  };
  auto cpub = [&](int blk) {
    if (panel < 10 && wm == 0) {
#pragma unroll
      for (int n = 0; n < 3; ++n) {
        f32x4 d = MFMA(bfr[0][n], ones, ((f32x4){0.f, 0.f, 0.f, 0.f}));
        d = MFMA(bfr[1][n], ones, d);
        csum_store(ws, WS_GC + (blk * 10 + panel) * 2048 + ca[n] * 128, d, fr, g, tl);
      }
    }
  };
  Side S; side_issue(P, S, panel * 128 + 64 + cbj * 8 + w, w);
  runA(1);
  side_finish(P, rws, S, panel * 128 + 64 + cbj * 8 + w, w, tl, th);
  runA(2);
  cpub(8 + cbj);
  runA(4);
  const int tc = tid - 256;
  {
    u64 cv[10];
    if (tid < 256) cv[0] = ld8(ws, WS_GX + panel * 2048 + tid * 8);
    else if (tc < 96) csum_issue(ws, 8 + cbj, tc, cv);
    runA(3);
    if (tid < 256) xstat_gather(ws, panel, L_ST, tid, tg, cv[0]);
    else if (tc < 96) csum_gather(ws, 8 + cbj, tc, tl, cv, L_CS);
  }
  bar(); rd(sb, 0); mmA(1); rd(sb, 1);
  STAMP(8);
  {
    float cn[3];
#pragma unroll
    for (int n = 0; n < 3; ++n) cn[n] = ldsf(L_CS + (ca[n] * 16 + fr) * 4);
#pragma unroll
    for (int m = 0; m < 2; ++m) {
      const int rl = wm * 32 + m * 16 + 4 * g;
      const f32x4 mr = ldsf4(L_ST + rl * 4), rinv = ldsf4(L_ST + 512 + rl * 4);
      f32x4 rs = {0.f, 0.f, 0.f, 0.f}, rq = {0.f, 0.f, 0.f, 0.f};
#pragma unroll
      for (int n = 0; n < 3; ++n) {
        f32x4 v = accA[m][n] * rinv + mr * cn[n];
        v = gelu4(v);
        rs += v; rq += v * v;
        accA[m][n] = v;
      }
#pragma unroll
      for (int j = 0; j < 4; ++j) { rs[j] = sum16(rs[j]); rq[j] = sum16(rq[j]); }
      if (fr == 0) {
        stsf4(L_RS + ((wn * 128 + rl) * 2) * 4, (f32x4){rs[0], rq[0], rs[1], rq[1]});
        stsf4(L_RS + ((wn * 128 + rl) * 2 + 4) * 4, (f32x4){rs[2], rq[2], rs[3], rq[3]});
      }
    }
  }
  mmB(0); nxt();
  bar(); rd(sb, 0);
  float part = 0.f;
  const unsigned gbase = WS_GR + (unsigned)(panel * 8) * 2048 + tid * 8;
  if (tid < 256) {
    part = ldsf(L_RS + tid * 4) + ldsf(L_RS + 1024 + tid * 4);
    st8u(ws, gbase + cbj * 2048, ((u64)tg << 32) | __float_as_uint(part));
  }
  mmB(1); rd(sb, 1); mmB(0); nxt();
  STAMP(9);
  runB(1);
  cpub(16 + cbj);
  {
    u64 cv[10];
    if (tid < 256) {
#pragma unroll
      for (int i = 1; i < 8; ++i) cv[i] = ld8(ws, gbase + ((cbj + i) & 7) * 2048);
    }
    runB(1);
    if (tid < 256) {
      float tot;
#pragma unroll 1
      for (unsigned sp = 0; sp < SPIN_CAP; ++sp) {
        bool ok = true; tot = part;
#pragma unroll
        for (int i = 1; i < 8; ++i) { ok &= (unsigned)(cv[i] >> 32) == tg; tot += __uint_as_float((unsigned)cv[i]); }
        if (__all(ok)) break;
#pragma unroll
        for (int i = 1; i < 8; ++i) cv[i] = ld8(ws, gbase + ((cbj + i) & 7) * 2048);
      }
      float other = dpp<0xB1>(tot);
      if ((tid & 1) == 0) {
        float m = tot * (1.f / FH);
        float v = rbf(fmaxf(other * (1.f / FH) - m * m, 0.f));
        float r = 1.f / rbf(sqrtf(rbf(v + 1e-5f)));
        stsf(L_FS + (tid >> 1) * 4, -rbf(m) * r);
        stsf(L_FS + 512 + (tid >> 1) * 4, r);
      }
    }
  }
  bar(); rd(sb, 0);
  {
    f32x4 mr[2], ri[2];
#pragma unroll
    for (int m = 0; m < 2; ++m) { const int rl = wm * 32 + m * 16 + 4 * g; mr[m] = ldsf4(L_FS + rl * 4); ri[m] = ldsf4(L_FS + 512 + rl * 4); }
#pragma unroll
    for (int n = 0; n < 3; ++n) {
      f32x4 a = accA[0][n] * ri[0] + mr[0], b = accA[1][n] * ri[1] + mr[1];
      st16(rws, WS_Z2N + (unsigned)(((cbj * 64 + panel * 4 + wm) * 6 + ca[n]) * 1024) + l * 16,
           (u32x4){cvt_pk(a[0], a[1]), cvt_pk(a[2], a[3]), cvt_pk(b[0], b[1]), cvt_pk(b[2], b[3])});
    }
  }
  mmB(1); rd(sb, 1); mmB(0); nxt();
  STAMP(10);
  runB(2);
  wait_vm<0>();
  bar();
  if (tid == 0) st8u(ws, WS_F1 + (panel * 32 + 8 + cbj) * 64, tag);
  STAMP(11);
  rd(sb, 0); mmB(1); rd(sb, 1); mmB(0); nxt();
  {
    u64 cv[10];
    if (tid >= 256 && tc < 96) csum_issue(ws, 16 + cbj, tc, cv);
    runB(2);
    if (tid >= 256 && tc < 96) csum_gather(ws, 16 + cbj, tc, tl, cv, L_CS + 512);
  }
  mmB(1);
  STAMP(1);
  bar();
  {
    f32x4 cn[3];
#pragma unroll
    for (int n = 0; n < 3; ++n) { const unsigned a = L_CS + 512 + (ca[n] * 16 + 4 * g) * 4; cn[n] = ldsf4(a); }
    float mr[2], rinv[2];
#pragma unroll
    for (int m = 0; m < 2; ++m) { const int rl = wm * 32 + m * 16 + fr; mr[m] = ldsf(L_ST + rl * 4); rinv[m] = ldsf(L_ST + 512 + rl * 4); }
#pragma unroll
    for (int n = 0; n < 3; ++n) {
      u32x4 o;
#pragma unroll
      for (int m = 0; m < 2; ++m) {
        f32x4 v = accB[m][n] * rinv[m] + cn[n] * mr[m];
        v = gelu4(v);
        o[2 * m] = cvt_pk(v[0], v[1]); o[2 * m + 1] = cvt_pk(v[2], v[3]);
      }
      if (wm < 2) *(LAS u32x4*)(uintptr_t)(lds0 + L_KVT + 6144 + (wm * 6 + ca[n]) * 1024 + l * 16) = o;
      else st16(rws, WS_Z1 + (unsigned)(((panel * 8 + cbj) * 4 + wm) * 6 + ca[n]) * 1024 + l * 16, o);
    }
  }
}

__global__ __launch_bounds__(768, 3) void mega_kernel(Params P) {
  extern __shared__ __attribute__((aligned(16))) char smem[];
  const unsigned lds0 = (unsigned)(uintptr_t)(LAS char*)smem;
  char* ws = P.ws;
  const int tid = threadIdx.x, l = tid & 63;
  const int w = __builtin_amdgcn_readfirstlane(tid >> 6);
  const int g = l >> 4, fr = l & 15;
  const int b = blockIdx.x, xcd = b & 7, bi = b >> 3;
  const int panel1 = (xcd >> 1) * 4 + (bi & 3), cb1 = (xcd & 1) * 4 + ((bi >> 2) & 3);
  const bool isq = bi < 16;
  const int panel2 = 2 * panel1 + (isq ? 1 : 0), cb2 = cb1;
  const int panel3 = xcd * 4 + (bi & 3), cb3 = bi >> 2;
  STAMP(0);
  const u64 tag = ld8(ws, WS_EP) + 1;
  if (w == 7 && bi < 20) {
    u64 pc; asm volatile("s_getpc_b64 %0" : "=s"(pc));
    gl2((const char*)(pc & ~127ull) + bi * 2048, lds0 + 3 * K1_STAGE, l * 16, l * 16);
  }

  if (w >= 8) {
    const int lw = w - 8;
    const unsigned vA0 = (unsigned)(((l >> 3) * D_ + (((l & 7) ^ (l >> 3)) << 3)) * 2);
    const unsigned vA1 = vA0 + 8 * D_ * 2 - 1024, vA2 = vA0 + 16 * D_ * 2 - 2048, vA3 = vA0 + 24 * D_ * 2 - 3072;
    const char* gA = (const char*)(P.x + (size_t)(panel1 * 128 + lw * 32) * D_);
    const unsigned v20 = (unsigned)(((l >> 3) * S_ + (((l & 7) ^ (l >> 4)) << 3)) * 2);
    const unsigned v21 = (unsigned)((((l >> 3) + 8) * S_ + (((l & 7) ^ ((l >> 4) | 4)) << 3)) * 2) - 1024;
    const char* gA2 = (const char*)(P.wsp + (size_t)(panel2 * 64 + lw * 16) * S_);
    const int rot2 = 0;
    auto ldA = [&](int kt, unsigned sl) { gl2(gA2 + ((kt + rot2) & 31) * 128, sl + lw * 2048, v20, v21); };
    auto sl2 = [&](int kt) { return lds0 + ((kt + 4) % 6) * P2_SLOT; };
    const unsigned tl = (unsigned)tag, th = tl * 0x9E3779B1u ^ (unsigned)(tag >> 32);
    const int lt = tid - 512;
    auto gather = [&](int blk, int ncol, unsigned dst, bool xs) {
      u64 cv[10]; u64 gv = 0;
      const int tc = lt < ncol ? lt : 0;
      if (xs) gv = ld8(ws, WS_GX + panel1 * 2048 + lt * 8);
      csum_issue(ws, blk, tc, cv);
      if (xs) xstat_gather(ws, panel1, lds0 + L_MISC, lt, (lt & 1) ? th : tl, gv);
      csum_gather(ws, blk, tc, tl, cv, dst);
    };
    const unsigned l16 = l * 16;
    const char* gB2 = ws + WS_Z2N + (size_t)cb2 * 64 * 6144 + lw * 3072;
    auto ldB = [&](int kt, unsigned sl) { gl3c(gB2 + ((kt + rot2) & 31) * 12288, sl + 8192 + lw * 3072, l16); };
    const unsigned zfl = WS_F1 + ((l & 15) * 32 + 8 + cb2) * 64;
    auto try_z = [&]() {
      const u64 v = l < 16 ? ld8(ws, zfl) : tag;
      return (bool)__builtin_amdgcn_readfirstlane((int)__all(v == tag));
    };
    bool early = false;
    const int rot = panel1 < 10 ? panel1 : 0;
    auto krot = [&](int kt) {
      const int k = kt >= 10 ? kt - 10 : kt;
      const int j = k < 2 ? k : k - 1;
      return __builtin_amdgcn_readfirstlane(k == 2 ? rot : (j < rot ? j : j + 1));
    };
    if (isq) {
      gl4(gA + krot(0) * 128, lds0 + lw * 4096, vA0, vA1, vA2, vA3);
      unsigned vB[4];
#pragma unroll
      for (int i = 0; i < 4; ++i) {
        int c = i * 64 + l; if (c > 239) c = 239;
        const int row = c / 30, cc = c - row * 30, seg = cc / 10, ch = cc - seg * 10;
        vB[i] = (unsigned)((row * NQKV + seg * 640 + cb1 * 80 + ch * 8) * 2) + 4096 - i * 1024;
      }
      const char* gB = (const char*)P.wqkv + (size_t)lw * 16 * NQKV * 2 - 4096;
      auto stageB = [&](int kt, unsigned sb) {
        const char* bp = gB + (size_t)krot(kt) * 64 * NQKV * 2;
        gl4(bp, sb + 16384 + lw * 2 * KBS, vB[0], vB[1], vB[2], vB[3]);
        gl4(bp + 8 * NQKV * 2, sb + 16384 + (lw * 2 + 1) * KBS, vB[0], vB[1], vB[2], vB[3]);
      };
      auto stage = [&](int kt, unsigned sb) { gl4(gA + krot(kt) * 128, sb + lw * 4096, vA0, vA1, vA2, vA3); stageB(kt, sb); };
      stageB(0, lds0); stage(1, lds0 + K1_STAGE);
      wait_vm<12>(); bar();
      stage(2, lds0 + 2 * K1_STAGE);
      unsigned sb = lds0, sc = lds0 + K1_STAGE;
#pragma unroll 1
      for (int kt = 1; kt < 8; ++kt) {
        wait_vm<12>(); bar();
        if (kt == 2) STAMP(10);
        if (kt == 7) STAMP(11);
        stage(kt + 2, sb);
        sb = sc; sc += K1_STAGE; if (sc == lds0 + 3 * K1_STAGE) sc = lds0;
      }
      wait_vm<12>(); bar();
      gather(cb1, 240, lds0 + L_MISC + 5120, true);
      bar();
      ldA(0, sl2(0)); ldA(1, sl2(1)); ldA(5, sl2(5));
      bar();
      ldA(4, sl2(4));
#pragma unroll 1
      for (int t = 0; t < 3 && !early; ++t) early = try_z();
      if (early) { ldB(0, sl2(0)); ldB(1, sl2(1)); ldB(4, sl2(4)); ldB(5, sl2(5)); }
      bar();
      if (!early) { early = try_z(); if (early) { ldB(0, sl2(0)); ldB(1, sl2(1)); ldB(4, sl2(4)); ldB(5, sl2(5)); } }
      bar();
    } else {
      gl4(gA + krot(0) * 128, lds0 + lw * 4096, vA0, vA1, vA2, vA3);
      unsigned vB[3];
#pragma unroll
      for (int i = 0; i < 3; ++i) {
        const int c = i * 64 + l, row = c / 12, cc = c - row * 12;
        int pc = (cc >> 1) - (row >> 3); if (pc < 0) pc = 5;
        vB[i] = (unsigned)((row * FIN + cb1 * 96 + pc * 16 + (cc & 1) * 8) * 2) + 4096 - i * 1024;
      }
      const char* gB = (const char*)P.win + (size_t)lw * 16 * FIN * 2 - 4096;
      auto stageB = [&](int kt, unsigned sb) {
        const int k = krot(kt);
        gl3(gB + (kt < 10 ? FH * 2 : 0) + (size_t)k * 64 * FIN * 2, sb + 16384 + lw * 3072, vB[0], vB[1], vB[2]);
      };
      auto stage = [&](int kt, unsigned sb) {
        const int k = krot(kt);
        gl4(gA + k * 128, sb + lw * 4096, vA0, vA1, vA2, vA3); stageB(kt, sb);
      };
      stageB(0, lds0); stage(1, lds0 + K1_STAGE);
      wait_vm<7>(); bar();
      stage(2, lds0 + 2 * K1_STAGE);
      unsigned sb = lds0, sc = lds0 + K1_STAGE;
      auto runL = [&](int k0, int k1) {
#pragma unroll 1
        for (int kt = k0; kt < k1; ++kt) {
          wait_vm<7>(); bar();
          stage(kt + 2, sb);
          sb = sc; sc += K1_STAGE; if (sc == lds0 + 3 * K1_STAGE) sc = lds0;
        }
      };
      runL(1, 11);
      {
        const char* wp = (const char*)(P.wsp + (size_t)(panel1 * 128 + lw * 32) * S_) + (cb1 & 3) * 1024;
        const unsigned vo = (unsigned)((l >> 3) * S_ * 2 + (l & 7) * 128);
        unsigned d0, d1, d2, d3;
        asm volatile("global_load_dword %0, %4, %5\n\tglobal_load_dword %1, %4, %6\n\tglobal_load_dword %2, %4, %7\n\tglobal_load_dword %3, %4, %8"
                     : "=&v"(d0), "=&v"(d1), "=&v"(d2), "=&v"(d3)
                     : "v"(vo), "s"(wp), "s"(wp + 8 * S_ * 2), "s"(wp + 16 * S_ * 2), "s"(wp + 24 * S_ * 2) : "memory");
        runL(11, 13);
        asm volatile("" :: "v"(d0), "v"(d1), "v"(d2), "v"(d3));
      }
      runL(13, 18);
      wait_vm<7>(); bar();
      wait_vm<0>(); bar();
      bar();
      wait_words(ws, zfl, l < 16, tag);
      early = true;
      ldA(0, sl2(0)); ldB(0, sl2(0)); ldA(1, sl2(1)); ldB(1, sl2(1));
      ldA(4, sl2(4)); ldA(5, sl2(5)); ldB(4, sl2(4)); ldB(5, sl2(5));
    }
    STAMP(2);
    if (!early) {
      wait_words(ws, zfl, l < 16, tag);
      ldB(0, sl2(0)); ldB(1, sl2(1)); ldB(4, sl2(4)); ldB(5, sl2(5));
    }
    STAMP(3);
    ldA(2, sl2(2)); ldA(3, sl2(3)); ldB(2, sl2(2)); ldB(3, sl2(3));
    wait_vm<16>(); bar();
    {
      unsigned sl = sl2(0);
      auto ldU = [&](int u) {
        ldA(2 * u, sl); ldB(2 * u, sl); ldA(2 * u + 1, sl + P2_SLOT); ldB(2 * u + 1, sl + P2_SLOT);
        sl += 2 * P2_SLOT; if (sl == lds0 + 6 * P2_SLOT) sl = lds0;
      };
      wait_vm<0>(); bar(); ldU(3);
#pragma unroll 1
      for (int u = 2; u < 14; ++u) {
        wait_vm<10>(); bar();
        if (u == 4) STAMP(8);
        if (u == 10) STAMP(9);
        ldU(u + 2);
      }
    }
    wait_vm<10>(); bar();
    wait_words(ws, WS_F1 + (((lw * 64 + l) >> 4) * 32 + 16 + (l & 15)) * 64, true, tag);
    wait_vm<0>(); bar();
    if (b == 0 && tid == 512) st8u(ws, WS_EP, tag);
    STAMP(4);
    const char* gA3 = ws + WS_OH + (size_t)panel3 * 44 * 4096 + lw * 4096;
    const char* gB3 = ws + WS_WF + (size_t)cb3 * 44 * 5120 + lw * 5120;
    int r5 = cb3 + 2 * (bi & 3); r5 = r5 >= 10 ? r5 - 10 : r5 >= 5 ? r5 - 5 : r5;
    int r6 = cb3 + 2 * (bi & 3); r6 = r6 >= 12 ? r6 - 12 : r6 >= 6 ? r6 - 6 : r6;
    auto ur = [&](int u) {
      int v;
      if (u < 5) { v = u + r5; if (v >= 5) v -= 5; } else { v = u + r6; if (v >= 11) v -= 6; }
      return __builtin_amdgcn_readfirstlane(v);
    };
    auto ldB3 = [&](int u, unsigned sl) {
      gl4c(gB3 + ur(u) * 20480, sl + 16384 + lw * 5120, l16);
      glds16c(gB3 + ur(u) * 20480 + 4096, l16, sl + 16384 + lw * 5120 + 4096);
    };
    auto ldA3 = [&](int u, unsigned sl) { gl4c(gA3 + ur(u) * 16384, sl + lw * 4096, l16); };
    bar(); bar();
    ldB3(0, lds0); ldB3(1, lds0 + P3_SLOT); ldB3(2, lds0 + 2 * P3_SLOT);
    wait_words(ws, WS_FO + ((panel3 >> 1) * 8 + (l & 7)) * 64, l < 8, tag);
    ldA3(0, lds0); ldA3(1, lds0 + P3_SLOT); ldA3(2, lds0 + 2 * P3_SLOT);
    bar();
    ldA3(3, lds0 + 3 * P3_SLOT); ldB3(3, lds0 + 3 * P3_SLOT);
    wait_vm<17>(); bar();
    wait_vm<13>(); bar(); ldA3(4, lds0); ldB3(4, lds0);
    wait_vm<18>(); bar(); ldB3(5, lds0 + P3_SLOT);
    wait_vm<14>(); bar(); ldB3(6, lds0 + 2 * P3_SLOT);
    wait_vm<10>(); bar(); ldB3(7, lds0 + 3 * P3_SLOT);
    wait_words(ws, WS_F2 + (panel3 * 32 + (l & 31)) * 64, l < 32, tag);
    STAMP(5);
    ldA3(5, lds0 + P3_SLOT); ldA3(6, lds0 + 2 * P3_SLOT); ldA3(7, lds0 + 3 * P3_SLOT);
    wait_vm<8>(); bar(); ldA3(8, lds0); ldB3(8, lds0);
    wait_vm<13>(); bar(); ldA3(9, lds0 + P3_SLOT); ldB3(9, lds0 + P3_SLOT);
    wait_vm<18>(); bar(); ldA3(10, lds0 + 2 * P3_SLOT); ldB3(10, lds0 + 2 * P3_SLOT);
    wait_vm<18>(); bar(); wait_vm<9>(); bar(); wait_vm<0>(); bar();
    bar(); bar();
    STAMP(6);
    return;
  }

  const rsrc_t rws = __builtin_amdgcn_make_buffer_rsrc(ws, 0, (int)WS_END, 0x00020000);
  bf16x8 pf[3];
  if (isq) p1q_consumer(P, rws, w, panel1, cb1, lds0, tag, pf);
  else p1z_consumer(P, rws, w, panel1, cb1, lds0, tag);
  STAMP(2);
  wf_job(P, rws, w);

  u32x4 xv[2];
  unsigned xo0, xo1, la0, la1;
  {
    const int wt = w & 3, kh = w >> 2;
    const int rsw = ((wt * 16 + fr) >> 1) & 7;
    const unsigned ao0 = (unsigned)((wt * 16 + fr) * 128 + (((4 * kh + (g >> 1)) ^ rsw) << 4) + (g & 1) * 8);
    const unsigned ao1 = (unsigned)((wt * 16 + fr) * 128 + (((4 * kh + 2 + (g >> 1)) ^ rsw) << 4) + (g & 1) * 8);
    const unsigned bo = (unsigned)(8192 + kh * 6144 + l * 16);
    const bf16x8 zeros = {0, 0, 0, 0, 0, 0, 0, 0};
    f32x4 acc[6];
#pragma unroll
    for (int n = 0; n < 6; ++n) acc[n] = (f32x4){0.f, 0.f, 0.f, 0.f};
    bf16x8 af[2], bfr[2][6];
    af[1] = zeros;
#pragma unroll
    for (int n = 0; n < 6; ++n) bfr[1][n] = zeros;
    const int zi = (bi & 15) * 8 + xcd;
    const bool reducer = !isq && zi < 72;
    const int rj = zi & 7, rf = zi >> 3;
    const int row = panel2 * 64 + wt * 16 + fr;
    u32x2 zreg[6];
    const float bs = bf2f(P.bsp[row]);
    unsigned sl = lds0 + 4 * P2_SLOT;
    auto step = [&](int cur, int kt) {
      if (cur == 0) bar();
      af[cur] = lds_2b64(sl + ao0, sl + ao1);
#pragma unroll
      for (int n = 0; n < 6; ++n) bfr[cur][n] = lds_b128(sl + bo + n * 1024);
      SB0;
#pragma unroll
      for (int n = 0; n < 6; ++n) acc[n] = MFMA(bfr[cur ^ 1][n], af[cur ^ 1], acc[n]);
      SB0;
      sl += P2_SLOT; if (sl == lds0 + 6 * P2_SLOT) sl = lds0;
    };
    auto run = [&](int k0, int k1) {
#pragma unroll 1
      for (int kt = k0; kt < k1; kt += 2) { step(0, kt); step(1, kt + 1); }
    };
    if (reducer) {
      const int et = (rf + 1) >> 1, st = rf == 0 ? 0 : ((rf + 1) & 1) + (et >= 3 ? 1 : 0);
      wait_words(ws, WS_F1 + ((l & 15) * 32 + rj) * 64, true, tag);
      f32x4 pv[8];
      bool okv[4];
#pragma unroll
      for (int t4 = 0; t4 < 4; ++t4) {
        const int i = 2 * st + (t4 & 1);
        okv[t4] = i < 5 && (t4 >= 2 || (et - i <= 1 && i - et <= 1));
        const int job = okv[t4] ? (t4 >= 2 ? 13 + i : 2 * i + et) : 0;
#pragma unroll
        for (int pp = 0; pp < 2; ++pp)
          pv[t4 * 2 + pp] = __builtin_bit_cast(f32x4, ld16(rws, WS_KVP + (unsigned)(((rj * 18 + job) * 16 + w * 2 + pp) * 1024) + l * 16));
      }
      run(0, 2);
      if (tid == 0) st8u(ws, WS_F1 + (panel1 * 32 + 16 + cb1) * 64, tag);
      const unsigned L_R = lds0 + 122880;
      f32x4 sv[4];
#pragma unroll
      for (int t4 = 0; t4 < 4; ++t4) { sv[t4] = pv[t4 * 2] + pv[t4 * 2 + 1]; if (!okv[t4]) sv[t4] = (f32x4){0.f, 0.f, 0.f, 0.f}; }
      if (w >= 4) {
#pragma unroll
        for (int t4 = 0; t4 < 4; ++t4) stsf4(L_R + ((w - 4) * 4 + t4) * 1024 + l * 16, sv[t4]);
      }
      run(2, 4);
      if (w < 4) {
#pragma unroll
        for (int t4 = 0; t4 < 4; ++t4) stsf4(L_R + (w * 4 + t4) * 1024 + l * 16, sv[t4] + ldsf4(L_R + (w * 4 + t4) * 1024 + l * 16));
      }
      run(4, 6);
      if (w < 2) {
        f32x4 v[2];
#pragma unroll
        for (int h2 = 0; h2 < 2; ++h2) {
          v[h2] = (f32x4){0.f, 0.f, 0.f, 0.f};
#pragma unroll
          for (int ww = 0; ww < 4; ++ww) v[h2] += ldsf4(L_R + (ww * 4 + w * 2 + h2) * 1024 + l * 16);
          const int he = (et * 16 + fr) / 20;
#pragma unroll
          for (int r = 0; r < 4; ++r) { const int d = (2 * st + h2) * 16 + 4 * g + r; if (d / 20 != he) v[h2][r] = 0.f; }
        }
        st16(rws, WS_KVT + (unsigned)(((rj * 2 + w) * 9 + rf) * 1024) + l * 16,
             (u32x4){cvt_pk(v[0][0], v[0][1]), cvt_pk(v[0][2], v[0][3]), cvt_pk(v[1][0], v[1][1]), cvt_pk(v[1][2], v[1][3])});
      }
      run(6, 10);
      wait_vm<0>();
      if (w < 2 && l == 0) st8u(ws, WS_FK + (rj * 32 + w * 16 + rf) * 64, tag);
      run(10, 18);
    } else {
      run(0, 6);
      wait_vm<0>();
      run(6, 8);
      if (tid == 0) st8u(ws, WS_F1 + (panel1 * 32 + (isq ? 24 : 16) + cb1) * 64, tag);
      run(8, 18);
    }
    if (isq) {
      const unsigned foff = (l & 15) < 9 ? WS_FK + (cb2 * 32 + (l & 31)) * 64 : WS_F1 + (panel1 * 32 + 16 + cb2) * 64;
      const u64 fv = ld8(ws, foff);
      run(18, 22);
      if (!__all(fv == tag)) wait_words(ws, foff, l < 32, tag);
#pragma unroll
      for (int i = 0; i < 3; ++i) {
        int ins = w + 8 * i; if (ins >= 18) ins = 17;
        ins = __builtin_amdgcn_readfirstlane(ins);
        glds16c(ws, WS_KVT + cb2 * 18432 + ins * 1024 + l * 16, __builtin_amdgcn_readfirstlane(lds0 + L_KVT + ins * 1024));
      }
      const unsigned zo = WS_Z1 + (unsigned)(((panel1 * 8 + cb2) * 4 + 2 + (wt >> 1)) * 6) * 1024 + l * 16 + (wt & 1) * 8;
#pragma unroll
      for (int n = 0; n < 6; ++n) zreg[n] = __builtin_amdgcn_raw_buffer_load_b64(rws, zo + n * 1024, 0, 16);
      run(22, 24);
      wait_vm<0>();
      run(24, 26);
      u32x2 ov[5];
#pragma unroll
      for (int et = 0; et < 5; ++et) {
        f32x4 num = {0.f, 0.f, 0.f, 0.f}, den = {0.f, 0.f, 0.f, 0.f};
        const int st0 = et >= 3 ? 1 : 0, nst = et == 0 ? 1 : 2, f0 = et == 0 ? 0 : 1 + (et - 1) * 2;
#pragma unroll
        for (int s = 0; s < nst; ++s) {
          bf16x8 ka = lds_b128(lds0 + L_KVT + (f0 + s) * 1024 + l * 16), za = lds_b128(lds0 + L_KVT + (9 + f0 + s) * 1024 + l * 16);
          num = MFMA(ka, pf[st0 + s], num); den = MFMA(za, pf[st0 + s], den);
        }
        float v[4];
#pragma unroll
        for (int j = 0; j < 4; ++j) v[j] = num[j] * __builtin_amdgcn_rcpf(den[j] + 1e-6f);
        ov[et] = (u32x2){cvt_pk(v[0], v[1]), cvt_pk(v[2], v[3])};
      }
      const unsigned ob = WS_OH + (unsigned)(((2 * panel1 + kh) * 44 + ((cb2 * 5) >> 1)) * 4 + wt) * 1024 + l * 16;
      if ((cb2 & 1) == 0) {
        st16(rws, ob, (u32x4){ov[0][0], ov[0][1], ov[1][0], ov[1][1]});
        st16(rws, ob + 4096, (u32x4){ov[2][0], ov[2][1], ov[3][0], ov[3][1]});
        st8(ws, ob + 8192, ov[4]);
      } else {
        st8(ws, ob + 8, ov[0]);
        st16(rws, ob + 4096, (u32x4){ov[1][0], ov[1][1], ov[2][0], ov[2][1]});
        st16(rws, ob + 8192, (u32x4){ov[3][0], ov[3][1], ov[4][0], ov[4][1]});
      }
      run(26, 30);
      wait_vm<0>();
      run(30, 32);
      if (tid == 0) st8u(ws, WS_FO + (panel1 * 8 + cb2) * 64, tag);
    } else {
      run(18, 32);
#pragma unroll
      for (int n = 0; n < 6; ++n) zreg[n] = *(LAS u32x2*)(uintptr_t)(lds0 + L_KVT + 6144 + ((wt >> 1) * 6 + n) * 1024 + l * 16 + (wt & 1) * 8);
    }
#pragma unroll
    for (int n = 0; n < 6; ++n) acc[n] = MFMA(bfr[1][n], af[1], acc[n]);
    STAMP(4);
    {
      const int r0 = tid / 10, h0 = tid - r0 * 10, c1 = (tid & 127) + 512, r1 = c1 / 10, h1 = c1 - r1 * 10;
      xo0 = (unsigned)((panel3 * 64 + r0) * D_ + cb3 * 80 + h0 * 8); xo1 = (unsigned)((panel3 * 64 + r1) * D_ + cb3 * 80 + h1 * 8);
      la0 = lds0 + (unsigned)((r0 * 84 + h0 * 8) * 4); la1 = lds0 + (unsigned)((r1 * 84 + h1 * 8) * 4);
      xv[0] = *(const u32x4*)(P.x + xo0); xv[1] = *(const u32x4*)(P.x + xo1);
    }
    bar();
    const unsigned L_RED = lds0 + 3 * P3_SLOT;
    if (kh == 1) {
#pragma unroll
      for (int n = 0; n < 6; ++n) stsf4(L_RED + (wt * 6 + n) * 1024 + l * 16, acc[n]);
    }
    bar();
    if (kh == 0) {
#pragma unroll
      for (int n = 0; n < 6; ++n) acc[n] += ldsf4(L_RED + (wt * 6 + n) * 1024 + l * 16);
      u32x2 hv[6];
#pragma unroll
      for (int n = 0; n < 6; ++n) {
        f32x4 gg = acc[n] + bs;
        hv[n] = (u32x2){cvt_pk(lo16(zreg[n][0]) * gg[0], hi16(zreg[n][0]) * gg[1]), cvt_pk(lo16(zreg[n][1]) * gg[2], hi16(zreg[n][1]) * gg[3])};
      }
      const unsigned hb = WS_OH + (unsigned)((panel2 * 44 + 20 + cb2 * 3) * 4 + wt) * 1024 + l * 16;
#pragma unroll
      for (int s = 0; s < 3; ++s) st16(rws, hb + s * 4096, (u32x4){hv[2 * s][0], hv[2 * s][1], hv[2 * s + 1][0], hv[2 * s + 1][1]});
      wait_vm<0>();
      if (l == 0) st8u(ws, WS_F2 + ((panel2 * 8 + cb2) * 4 + wt) * 64, tag);
    }
    bar();
    STAMP(5);
  }
  {
    const int wsr = w & 3, kh = w >> 2;
    const unsigned ao = (unsigned)(kh * 8192 + wsr * 1024 + l * 16);
    const unsigned bb = 16384u + kh * 10240 + l * 16;
    const bf16x8 zeros = {0, 0, 0, 0, 0, 0, 0, 0};
    f32x4 acc[5];
#pragma unroll
    for (int n = 0; n < 5; ++n) acc[n] = (f32x4){0.f, 0.f, 0.f, 0.f};
    bf16x8 af[2][2], bfr[2][2][5];
    unsigned sb = lds0;
    auto mm = [&](int c) {
#pragma unroll
      for (int s2 = 0; s2 < 2; ++s2)
#pragma unroll
        for (int n = 0; n < 5; ++n) acc[n] = MFMA(bfr[c][s2][n], af[c][s2], acc[n]);
      SB0;
    };
    auto rdu = [&](int c) {
      bar();
#pragma unroll
      for (int s2 = 0; s2 < 2; ++s2) {
        af[c][s2] = lds_b128(sb + ao + s2 * 4096);
#pragma unroll
        for (int n = 0; n < 5; ++n) bfr[c][s2][n] = lds_b128(sb + bb + s2 * 5120 + n * 1024);
      }
      SB0;
      sb += P3_SLOT; if (sb == lds0 + 4 * P3_SLOT) sb = lds0;
    };
    rdu(0);
#pragma unroll 1
    for (int u = 0; u < 5; ++u) { rdu(1); mm(0); rdu(0); mm(1); }
    mm(0);
    STAMP(6);
    bar();
#pragma unroll
    for (int n = 0; n < 5; ++n) stsf4(lds0 + (unsigned)(((kh * 64 + wsr * 16 + fr) * 84 + n * 16 + 4 * g) * 4), acc[n]);
    bar();
#pragma unroll
    for (int i = 0; i < 2; ++i) {
      if (i == 0 || tid < 128) {
        const unsigned a = i == 0 ? la0 : la1;
        f32x4 a0 = ldsf4(a) + ldsf4(a + 64 * 84 * 4), a1 = ldsf4(a + 16) + ldsf4(a + 64 * 84 * 4 + 16);
        u32x4 o = {cvt_pk(lo16(xv[i][0]) + a0[0], hi16(xv[i][0]) + a0[1]), cvt_pk(lo16(xv[i][1]) + a0[2], hi16(xv[i][1]) + a0[3]),
                   cvt_pk(lo16(xv[i][2]) + a1[0], hi16(xv[i][2]) + a1[1]), cvt_pk(lo16(xv[i][3]) + a1[2], hi16(xv[i][3]) + a1[3])};
        *(u32x4*)(P.out + (i == 0 ? xo0 : xo1)) = o;
      }
    }
    STAMP(7);
  }
}

extern "C" void kernel_launch(void* const* d_in, const int* in_sizes, int n_in, void* d_out, int out_size,
                              void* d_ws, size_t ws_size, hipStream_t stream) {
  Params P;
  P.x = (const u16*)d_in[0];
  P.wqkv = (const u16*)d_in[1];
  P.wo = (const u16*)d_in[2];
  P.win = (const u16*)d_in[4];
  P.wsp = (const u16*)d_in[5];
  P.bsp = (const u16*)d_in[6];
  P.wout = (const u16*)d_in[7];
  P.out = (u16*)d_out;
  P.ws = (char*)d_ws;
  (void)hipFuncSetAttribute((const void*)mega_kernel, hipFuncAttributeMaxDynamicSharedMemorySize, LDS_TOTAL);
  mega_kernel<<<256, 768, LDS_TOTAL, stream>>>(P);
}
```

```cpp
#include <hip/hip_runtime.h>
#include <hip/hip_bf16.h>
#include <stdint.h>

typedef __attribute__((ext_vector_type(8))) short bf16x8;
typedef __attribute__((ext_vector_type(4))) short bf16x4;
typedef __attribute__((ext_vector_type(4))) float f32x4;
typedef __attribute__((ext_vector_type(2))) float f32x2;
typedef __attribute__((ext_vector_type(2))) __bf16 bf2;
typedef __attribute__((ext_vector_type(4))) unsigned u32x4;
typedef __attribute__((ext_vector_type(2))) unsigned u32x2;
typedef unsigned short u16;
typedef unsigned long long u64;
#define LAS __attribute__((address_space(3)))
#define GAS __attribute__((address_space(1)))
#define DI __device__ __forceinline__
#define RLX_AGENT __ATOMIC_RELAXED, __HIP_MEMORY_SCOPE_AGENT

constexpr int S_ = 2048, D_ = 640, NQKV = 1920, FIN = 1536, FH = 768;

constexpr unsigned WS_PQ  = 0;
constexpr unsigned WS_Z1  = WS_PQ + 16 * 8 * 8 * 3 * 1024;
constexpr unsigned WS_Z2N = WS_Z1 + 16 * 8 * 24 * 1024;
constexpr unsigned WS_OH  = WS_Z2N + 8 * 64 * 6 * 1024;
constexpr unsigned WS_WF  = WS_OH + 32 * 44 * 4 * 1024;
constexpr unsigned WS_KVT = WS_WF + 8 * 44 * 5 * 1024;
constexpr unsigned WS_KVP = WS_KVT + 8 * 18 * 1024;
constexpr unsigned WS_GR  = WS_KVP + 8 * 18 * 16 * 1024;
constexpr unsigned WS_GX  = WS_GR + 16 * 8 * 256 * 8;
constexpr unsigned WS_GC  = WS_GX + 2048 * 16;
constexpr unsigned WS_F1  = WS_GC + 24 * 10 * 2048;
constexpr unsigned WS_F2  = WS_F1 + 16 * 32 * 64;
constexpr unsigned WS_FO  = WS_F2 + 1024 * 64;
constexpr unsigned WS_FK  = WS_FO + 256 * 64;
constexpr unsigned WS_EP  = WS_FK + 256 * 64;
constexpr unsigned WS_END = WS_EP + 64;

#define STAMP(i)

DI float bf2f(u16 v) { return __uint_as_float(((unsigned)v) << 16); }
DI unsigned cvt_pk(float lo, float hi) {
  bf2 r; r[0] = (__bf16)lo; r[1] = (__bf16)hi; return __builtin_bit_cast(unsigned, r);
}
DI float rbf(float x) { return (float)((__bf16)x); }
DI float lo16(unsigned u) { return __uint_as_float(u << 16); }
DI float hi16(unsigned u) { return __uint_as_float(u & 0xffff0000u); }

DI void glds16(const void* gbase, unsigned voff, unsigned lds_dst) {
  unsigned keep;
  asm volatile("s_mov_b32 %0, m0\n\ts_mov_b32 m0, %3\n\ts_nop 0\n\tglobal_load_lds_dwordx4 %1, %2\n\ts_mov_b32 m0, %0"
               : "=&s"(keep) : "v"(voff), "s"(gbase), "s"(lds_dst) : "memory");
}
DI void glds16c(const void* gbase, unsigned voff, unsigned lds_dst) {
  unsigned keep;
  asm volatile("s_mov_b32 %0, m0\n\ts_mov_b32 m0, %3\n\ts_nop 0\n\tglobal_load_lds_dwordx4 %1, %2 sc1\n\ts_mov_b32 m0, %0"
               : "=&s"(keep) : "v"(voff), "s"(gbase), "s"(lds_dst) : "memory");
}
#define GL_PRE "s_mov_b32 %0, m0\n\ts_mov_b32 m0, %2\n\ts_nop 0\n\t"
#define GL_POST "s_mov_b32 m0, %0"
DI void gl2(const void* gb, unsigned lds, unsigned v0, unsigned v1) {
  unsigned keep;
  asm volatile(GL_PRE "global_load_lds_dwordx4 %3, %1\n\tglobal_load_lds_dwordx4 %4, %1 offset:1024\n\t" GL_POST
               : "=&s"(keep) : "s"(gb), "s"(lds), "v"(v0), "v"(v1) : "memory");
}
DI void gl3(const void* gb, unsigned lds, unsigned v0, unsigned v1, unsigned v2) {
  unsigned keep;
  asm volatile(GL_PRE "global_load_lds_dwordx4 %3, %1\n\tglobal_load_lds_dwordx4 %4, %1 offset:1024\n\tglobal_load_lds_dwordx4 %5, %1 offset:2048\n\t" GL_POST
               : "=&s"(keep) : "s"(gb), "s"(lds), "v"(v0), "v"(v1), "v"(v2) : "memory");
}
DI void gl4(const void* gb, unsigned lds, unsigned v0, unsigned v1, unsigned v2, unsigned v3) {
  unsigned keep;
  asm volatile(GL_PRE "global_load_lds_dwordx4 %3, %1\n\tglobal_load_lds_dwordx4 %4, %1 offset:1024\n\tglobal_load_lds_dwordx4 %5, %1 offset:2048\n\tglobal_load_lds_dwordx4 %6, %1 offset:3072\n\t" GL_POST
               : "=&s"(keep) : "s"(gb), "s"(lds), "v"(v0), "v"(v1), "v"(v2), "v"(v3) : "memory");
}
DI void gl3c(const void* gb, unsigned lds, unsigned v0) {
  unsigned keep;
  asm volatile(GL_PRE "global_load_lds_dwordx4 %3, %1 sc1\n\tglobal_load_lds_dwordx4 %3, %1 offset:1024 sc1\n\tglobal_load_lds_dwordx4 %3, %1 offset:2048 sc1\n\t" GL_POST
               : "=&s"(keep) : "s"(gb), "s"(lds), "v"(v0) : "memory");
}
DI void gl4c(const void* gb, unsigned lds, unsigned v0) {
  unsigned keep;
  asm volatile(GL_PRE "global_load_lds_dwordx4 %3, %1 sc1\n\tglobal_load_lds_dwordx4 %3, %1 offset:1024 sc1\n\tglobal_load_lds_dwordx4 %3, %1 offset:2048 sc1\n\tglobal_load_lds_dwordx4 %3, %1 offset:3072 sc1\n\t" GL_POST
               : "=&s"(keep) : "s"(gb), "s"(lds), "v"(v0) : "memory");
}
template <int N> DI void wait_vm() { asm volatile("s_waitcnt vmcnt(%0)" :: "n"(N) : "memory"); }

DI bf16x8 lds_b128(unsigned addr) { return *(LAS bf16x8*)(uintptr_t)addr; }
DI bf16x8 lds_2b64(unsigned addr0, unsigned addr1) {
  bf16x4 a = *(LAS bf16x4*)(uintptr_t)addr0, b = *(LAS bf16x4*)(uintptr_t)addr1;
  return __builtin_shufflevector(a, b, 0, 1, 2, 3, 4, 5, 6, 7);
}
DI bf16x8 lds_tr(unsigned addr0, unsigned addr1) {
  bf16x4 a = __builtin_amdgcn_ds_read_tr16_b64_v4i16((LAS bf16x4*)(uintptr_t)addr0);
  bf16x4 b = __builtin_amdgcn_ds_read_tr16_b64_v4i16((LAS bf16x4*)(uintptr_t)addr1);
  return __builtin_shufflevector(a, b, 0, 1, 2, 3, 4, 5, 6, 7);
}
DI float ldsf(unsigned a) { return *(LAS float*)(uintptr_t)a; }
DI void stsf(unsigned a, float v) { *(LAS float*)(uintptr_t)a = v; }
DI f32x4 ldsf4(unsigned a) { return *(LAS f32x4*)(uintptr_t)a; }
DI void stsf4(unsigned a, f32x4 v) { *(LAS f32x4*)(uintptr_t)a = v; }
#define SB0 __builtin_amdgcn_sched_barrier(0)
#define MFMA(a, b, c) __builtin_amdgcn_mfma_f32_16x16x32_bf16((a), (b), (c), 0, 0, 0)

template <int CTRL> DI float dpp(float v) {
  return __builtin_bit_cast(float, __builtin_amdgcn_update_dpp(0, __builtin_bit_cast(int, v), CTRL, 0xf, 0xf, true));
}
DI float sum8(float v) { v += dpp<0xB1>(v); v += dpp<0x4E>(v); v += dpp<0x141>(v); return v; }
DI float sum16(float v) { v = sum8(v); v += dpp<0x140>(v); return v; }

DI float gelu_t(float x) {
  float u = x * (0.7978845608f + 0.0356774081f * x * x);
  float e = __builtin_amdgcn_exp2f(-2.885390082f * u);
  return x * __builtin_amdgcn_rcpf(1.0f + e);
}
DI f32x4 gelu4(f32x4 x) {
  const f32x4 a = x * ((x * x) * -0.1029432397f + -2.302208198f);
  f32x4 e;
#pragma unroll
  for (int j = 0; j < 4; ++j) e[j] = __builtin_amdgcn_exp2f(a[j]);
  e += 1.0f;
#pragma unroll
  for (int j = 0; j < 4; ++j) e[j] = __builtin_amdgcn_rcpf(e[j]);
  return x * e;
}
DI f32x4 phi4(f32x4 t) {
  f32x4 a, m;
#pragma unroll
  for (int j = 0; j < 4; ++j) { a[j] = fminf(t[j], 0.f); m[j] = fmaxf(t[j], 0.f); }
  a *= 1.4426950409f;
#pragma unroll
  for (int j = 0; j < 4; ++j) a[j] = __builtin_amdgcn_exp2f(a[j]);
  return (m + 1e-6f) + a;
}
DI float phi_f(float t) {
  return (fmaxf(t, 0.f) + 1e-6f) + __builtin_amdgcn_exp2f(fminf(t, 0.f) * 1.4426950409f);
}

typedef __amdgpu_buffer_rsrc_t rsrc_t;
DI void st16(rsrc_t r, unsigned off, u32x4 v) { __builtin_amdgcn_raw_buffer_store_b128(v, r, off, 0, 16); }
DI u32x4 ld16(rsrc_t r, unsigned off) { return __builtin_amdgcn_raw_buffer_load_b128(r, off, 0, 16); }
DI void st8(char* ws, unsigned off, u32x2 v) { __hip_atomic_store((GAS u64*)(ws + off), ((u64)v[1] << 32) | v[0], RLX_AGENT); }
DI void st8u(char* ws, unsigned off, u64 v) { __hip_atomic_store((GAS u64*)(ws + off), v, RLX_AGENT); }
DI void st4(char* ws, unsigned off, float v) { __hip_atomic_store((GAS unsigned*)(ws + off), __float_as_uint(v), RLX_AGENT); }
DI u64 ld8(const char* ws, unsigned off) { return __hip_atomic_load((GAS u64*)(ws + off), RLX_AGENT); }
constexpr unsigned SPIN_CAP = 1u << 20;
DI void wait_words(const char* ws, unsigned off, bool act, u64 tag) {
#pragma unroll 1
  for (unsigned sp = 0; sp < SPIN_CAP; ++sp) {
    u64 v = act ? ld8(ws, off) : tag;
    if (__all(v == tag)) break;
    __builtin_amdgcn_s_sleep(1);
  }
  __builtin_amdgcn_fence(__ATOMIC_ACQUIRE, "wavefront");
}

struct Params { const u16 *x, *wqkv, *wo, *win, *wsp, *bsp, *wout; u16* out; char* ws; };

constexpr unsigned KBS = 4224;
constexpr unsigned K1_STAGE = 16384 + 8 * KBS;
constexpr unsigned L_MISC = 153600;
constexpr int LDS_TOTAL = 163840;
constexpr unsigned P2_SLOT = 20480;
constexpr unsigned L_KVT = 135168;
constexpr unsigned P3_SLOT = 36864;

DI void bar() { asm volatile("s_waitcnt lgkmcnt(0)" ::: "memory"); __builtin_amdgcn_s_barrier(); asm volatile("" ::: "memory"); }

struct Side { u32x4 r0, r1; };
DI void side_issue(const Params& P, Side& S, int row, int w) {
  const int l = threadIdx.x & 63;
  const u32x4* src = (const u32x4*)(P.x + (size_t)row * D_);
  S.r0 = src[l]; S.r1 = src[64 + (l & 15)];
}
DI float rdl(float v, int lane) { return __builtin_bit_cast(float, __builtin_amdgcn_readlane(__builtin_bit_cast(int, v), lane)); }
DI void side_finish(const Params& P, rsrc_t rws, const Side& S, int row, int w, unsigned tl, unsigned th) {
  const int l = threadIdx.x & 63;
  float s = 0.f, q = 0.f, s2 = 0.f, q2 = 0.f;
#pragma unroll
  for (int j = 0; j < 4; ++j) {
    float a = lo16(S.r0[j]), b = hi16(S.r0[j]), c = lo16(S.r1[j]), d = hi16(S.r1[j]);
    s += a + b; q += a * a + b * b; s2 += c + d; q2 += c * c + d * d;
  }
  s = sum16(s); q = sum16(q); s2 = sum16(s2); q2 = sum16(q2);
  s = (rdl(s, 0) + rdl(s, 16)) + (rdl(s, 32) + rdl(s, 48)) + s2;
  q = (rdl(q, 0) + rdl(q, 16)) + (rdl(q, 32) + rdl(q, 48)) + q2;
  const float m = s * (1.f / D_);
  const float v = rbf(fmaxf(q * (1.f / D_) - m * m, 0.f));
  const float r = 1.f / rbf(sqrtf(rbf(v + 1e-5f)));
  if (l < 2) st8u(P.ws, WS_GX + row * 16 + l * 8, l == 0 ? (((u64)tl << 32) | __float_as_uint(-rbf(m) * r)) : (((u64)th << 32) | __float_as_uint(r)));
}
DI void wf_job(const Params& P, rsrc_t rws, int w) {
  const int l = threadIdx.x & 63;
  int item = blockIdx.x; if (item > 219) item = 219;
  const int st = item / 5, T = (item - st * 5) * 8 + w, cb = T / 5, nt = T - cb * 5;
  const u16* wsrc = (st < 20 ? P.wo + (size_t)st * 32 * D_ : P.wout + (size_t)(st - 20) * 32 * D_) + (size_t)(l >> 4) * 4 * D_ + T * 16 + (l & 15);
  unsigned wv[8];
#pragma unroll
  for (int e = 0; e < 8; ++e) wv[e] = wsrc[(size_t)(16 * (e >> 2) + (e & 3)) * D_];
  st16(rws, WS_WF + (unsigned)(((cb * 44 + st) * 5 + nt) * 1024) + l * 16,
       (u32x4){wv[0] | (wv[1] << 16), wv[2] | (wv[3] << 16), wv[4] | (wv[5] << 16), wv[6] | (wv[7] << 16)});
}
DI void xstat_gather(const char* ws, int panel, unsigned L_ST, int tid, unsigned tg, u64 gv) {
  if (tid < 256) {
#pragma unroll 1
    for (unsigned sp = 0; sp < SPIN_CAP; ++sp) {
      if (__all((unsigned)(gv >> 32) == tg)) break;
      __builtin_amdgcn_s_sleep(1);
      gv = ld8(ws, WS_GX + panel * 2048 + tid * 8);
    }
    stsf(L_ST + (tid & 1) * 512 + (tid >> 1) * 4, __uint_as_float((unsigned)gv));
  }
}

DI void csum_issue(const char* ws, int blk, int t, u64 (&cv)[10]) {
#pragma unroll
  for (int k = 0; k < 10; ++k) cv[k] = ld8(ws, WS_GC + (blk * 10 + k) * 2048 + t * 8);
}
DI void csum_gather(const char* ws, int blk, int t, unsigned tl, u64 (&cv)[10], unsigned dst) {
#pragma unroll 1
  for (unsigned sp = 0; sp < SPIN_CAP; ++sp) {
    bool ok = true;
#pragma unroll
    for (int k = 0; k < 10; ++k) ok &= (unsigned)(cv[k] >> 32) == tl;
    if (__all(ok)) break;
    __builtin_amdgcn_s_sleep(1);
    csum_issue(ws, blk, t, cv);
  }
  float a = 0.f;
#pragma unroll
  for (int k = 0; k < 10; ++k) a += __uint_as_float((unsigned)cv[k]);
  stsf(dst + t * 4, a);
}
DI void csum_store(char* ws, unsigned off, f32x4 d, int fr, int g, unsigned tl) {
  const float v = fr == 0 ? d[0] : fr == 1 ? d[1] : fr == 2 ? d[2] : d[3];
  if (fr < 4) st8u(ws, off + (4 * g + fr) * 8, ((u64)tl << 32) | __float_as_uint(v));
}

DI void p1q_consumer(const Params& P, rsrc_t rws, int w, int panel, int cbi, unsigned lds0, u64 tag, bf16x8 (&pf)[3]) {
  char* ws = P.ws;
  const int tid = threadIdx.x, l = tid & 63;
  const int wm = w >> 2, wc = (w + 2 * wm) & 3;
  const int g = l >> 4, fr = l & 15, q = (l & 15) >> 2, p = l & 3, g1 = g & 1;
  const unsigned aoff0 = (unsigned)((wm * 64 + fr) * 128 + ((g ^ (l & 7)) << 4));
  const unsigned ttab = (unsigned)(0x4498E765DC32BA10ull >> (16 * wc)) & 0xffffu;
  int tile[4];
  unsigned bo[4];
#pragma unroll
  for (int n = 0; n < 4; ++n) {
    tile[n] = (ttab >> (4 * n)) & 15;
    bo[n] = 16384u + g * KBS + q * 480 + p * 8 + tile[n] * 32;
  }
  const unsigned tl = (unsigned)tag, th = tl * 0x9E3779B1u ^ (unsigned)(tag >> 32);
  Side S; side_issue(P, S, panel * 128 + cbi * 8 + w, w);
  f32x4 acc[4][4];
#pragma unroll
  for (int m = 0; m < 4; ++m)
#pragma unroll
    for (int n = 0; n < 4; ++n) acc[m][n] = (f32x4){0.f, 0.f, 0.f, 0.f};
  const short one = 0x3F80;
  const bf16x8 ones = {one, one, one, one, one, one, one, one};
  const bf16x8 zeros = {0, 0, 0, 0, 0, 0, 0, 0};

  bf16x8 af[2][4], bfr[2][4];
#pragma unroll
  for (int m = 0; m < 4; ++m) { af[1][m] = zeros; bfr[1][m] = zeros; }
  auto mm = [&](int ks) {
#pragma unroll
    for (int n = 0; n < 3; ++n)
#pragma unroll
      for (int m = 0; m < 4; ++m) acc[m][n] = MFMA(bfr[ks][n], af[ks][m], acc[m][n]);
    if (wc < 3) {
#pragma unroll
      for (int m = 0; m < 4; ++m) acc[m][3] = MFMA(bfr[ks][3], af[ks][m], acc[m][3]);
    }
  };
  auto step = [&](unsigned sb, int ks) {
#pragma unroll
    for (int n = 0; n < 4; ++n) bfr[ks][n] = lds_tr(sb + ks * 4 * KBS + bo[n], sb + ks * 4 * KBS + bo[n] + 1920);
#pragma unroll
    for (int m = 0; m < 4; ++m) af[ks][m] = lds_b128(sb + ((aoff0 ^ (ks * 64)) + m * 2048));
    SB0;
    mm(ks ^ 1);
    SB0;
  };
  unsigned sb = lds0;
  auto run = [&](int n) {
#pragma unroll 1
    for (int kt = 0; kt < n; ++kt) {
      bar();
      step(sb, 0);
      step(sb, 1);
      sb += K1_STAGE; if (sb == lds0 + 3 * K1_STAGE) sb = lds0;
    }
  };
  run(1);
  side_finish(P, rws, S, panel * 128 + cbi * 8 + w, w, tl, th);
  if (panel < 10) {
#pragma unroll
    for (int h = 0; h < 2; ++h) {
      const bf16x8 b0 = wm == 0 ? bfr[0][h] : bfr[0][2 + h], b1 = wm == 0 ? bfr[1][h] : bfr[1][2 + h];
      const int tt = wm == 0 ? tile[h] : tile[2 + h];
      f32x4 d = MFMA(b0, ones, ((f32x4){0.f, 0.f, 0.f, 0.f}));
      d = MFMA(b1, ones, d);
      csum_store(ws, WS_GC + (cbi * 10 + panel) * 2048 + tt * 128, d, fr, g, tl);
    }
  }
  run(9);
  mm(1);
  STAMP(1);
  bar();
  STAMP(10);
  const unsigned L_ST = lds0 + L_MISC, L_CS = lds0 + L_MISC + 5120;
  const unsigned L_K = lds0, L_V = L_K + 20480, L_Q = lds0 + 122880;
  float mr[4], rinv[4];
#pragma unroll
  for (int m = 0; m < 4; ++m) { const int rl = wm * 64 + m * 16 + fr; mr[m] = ldsf(L_ST + rl * 4); rinv[m] = ldsf(L_ST + 512 + rl * 4); }
#pragma unroll
  for (int n = 0; n < 4; ++n) {
    if (n < 3 || wc < 3) {
      const int t = tile[n];
      const f32x4 cs = ldsf4(L_CS + (t * 16 + 4 * g) * 4);
      unsigned dst, dstep;
      if (t < 5) { dst = L_Q + (wm * 12 + (t >> 1)) * 1024 + l * 16 + (t & 1) * 8; dstep = 3072; }
      else {
        const int kb = wm * 8 + (fr >> 3), pos = (fr & 7) ^ ((fr >> 3) << 2);
        dst = (t < 10 ? L_K + (t - 5) * 256 : L_V + (t - 10) * 256) + kb * 1280 + pos * 32 + g * 8; dstep = 2560;
      }
      if (t < 10) {
#pragma unroll
        for (int m = 0; m < 4; ++m) {
          f32x4 v = acc[m][n] * rinv[m] + cs * mr[m];
          v = phi4(v);
          *(LAS u32x2*)(uintptr_t)(dst + m * dstep) = (u32x2){cvt_pk(v[0], v[1]), cvt_pk(v[2], v[3])};
        }
      } else {
#pragma unroll
        for (int m = 0; m < 4; ++m) {
          f32x4 v = acc[m][n] * rinv[m] + cs * mr[m];
          *(LAS u32x2*)(uintptr_t)(dst + m * dstep) = (u32x2){cvt_pk(v[0], v[1]), cvt_pk(v[2], v[3])};
        }
      }
    }
  }
  STAMP(8);
  bar();
  {
    const unsigned troff = (unsigned)(g * 5 * 256 + ((4 * g1 + q) * 32) + p * 8);
    f32x4 d[3];
    int jb[3];
#pragma unroll
    for (int r = 0; r < 3; ++r) {
      int job = w + 8 * r; if (job >= 18) job = w;
      jb[r] = job;
      const int i = job < 13 ? (job + 1) / 3 : job - 13;
      const int j = job < 13 ? job - 2 * i : 0;
      d[r] = (f32x4){0.f, 0.f, 0.f, 0.f};
#pragma unroll
      for (int ks = 0; ks < 4; ++ks) {
        const unsigned a0 = L_K + troff + (ks * 20 + i) * 256, b0 = L_V + troff + (ks * 20 + j) * 256;
        const bf16x8 ka = lds_tr(a0, a0 ^ 128);
        bf16x8 vb = lds_tr(b0, b0 ^ 128);
        if (job >= 13) vb = ones;
        d[r] = MFMA(ka, vb, d[r]);
      }
    }
#pragma unroll
    for (int r = 0; r < 3; ++r)
      st16(rws, WS_KVP + (unsigned)(((cbi * 18 + jb[r]) * 16 + panel) * 1024) + l * 16, __builtin_bit_cast(u32x4, d[r]));
    pf[0] = lds_b128(L_Q + w * 3072 + l * 16); pf[1] = lds_b128(L_Q + w * 3072 + 1024 + l * 16);
    const u32x2 q2 = *(LAS u32x2*)(uintptr_t)(L_Q + w * 3072 + 2048 + l * 16);
    pf[2] = __builtin_bit_cast(bf16x8, ((u32x4){q2[0], q2[1], 0u, 0u}));
  }
  STAMP(9);
  wait_vm<0>();
  bar();
  if (tid == 0) st8u(ws, WS_F1 + (panel * 32 + cbi) * 64, tag);
}

DI void p1z_consumer(const Params& P, rsrc_t rws, int w, int panel, int cbj, unsigned lds0, u64 tag) {
  char* ws = P.ws;
  const int tid = threadIdx.x, l = tid & 63;
  const int wm = w & 3, wn = w >> 2;
  const int g = l >> 4, fr = l & 15, q = (l & 15) >> 2, p = l & 3, g1 = g & 1;
  const unsigned aoff0 = (unsigned)((wm * 32 + fr) * 128 + ((g ^ (l & 7)) << 4));
  unsigned bo[3];
  int ca[3];
#pragma unroll
  for (int n = 0; n < 3; ++n) {
    const int c = wn * 3 + n;
    ca[n] = c;
    int pc = c + g1; if (pc == 6) pc = 0;
    bo[n] = 16384u + g * 1536 + q * 192 + pc * 32 + p * 8;
  }
  const short one = 0x3F80;
  const bf16x8 ones = {one, one, one, one, one, one, one, one};
  const bf16x8 zeros = {0, 0, 0, 0, 0, 0, 0, 0};
  f32x4 accA[2][3], accB[2][3];
#pragma unroll
  for (int m = 0; m < 2; ++m)
#pragma unroll
    for (int n = 0; n < 3; ++n) { accA[m][n] = (f32x4){0.f, 0.f, 0.f, 0.f}; accB[m][n] = (f32x4){0.f, 0.f, 0.f, 0.f}; }
  const unsigned tl = (unsigned)tag, th = tl * 0x9E3779B1u ^ (unsigned)(tag >> 32), tg = (tid & 1) ? th : tl;
  bf16x8 af[2][2], bfr[2][3];
  af[1][0] = af[1][1] = zeros; bfr[1][0] = bfr[1][1] = bfr[1][2] = zeros;
  auto rd = [&](unsigned sb, int ks) {
#pragma unroll
    for (int m = 0; m < 2; ++m) af[ks][m] = lds_b128(sb + ((aoff0 ^ (ks * 64)) + m * 2048));
#pragma unroll
    for (int n = 0; n < 3; ++n) { const unsigned o = sb + ks * 4 * 1536 + bo[n]; bfr[ks][n] = lds_tr(o, o + 768); }
    SB0;
  };
  auto mmA = [&](int ks) {
#pragma unroll
    for (int n = 0; n < 3; ++n)
#pragma unroll
      for (int m = 0; m < 2; ++m) accA[m][n] = MFMA(af[ks][m], bfr[ks][n], accA[m][n]);
    SB0;
  };
  auto mmB = [&](int ks) {
#pragma unroll
    for (int n = 0; n < 3; ++n)
#pragma unroll
      for (int m = 0; m < 2; ++m) accB[m][n] = MFMA(bfr[ks][n], af[ks][m], accB[m][n]);
    SB0;
  };
  const unsigned L_ST = lds0 + L_MISC, L_CS = lds0 + L_MISC + 5120;
  const unsigned L_RS = lds0 + L_MISC + 2048;
  const unsigned L_FS = lds0 + L_MISC + 4096;
  unsigned sb = lds0;
  auto nxt = [&]() { sb += K1_STAGE; if (sb == lds0 + 3 * K1_STAGE) sb = lds0; };
  auto runA = [&](int n) {
#pragma unroll 1
    for (int kt = 0; kt < n; ++kt) { bar(); rd(sb, 0); mmA(1); rd(sb, 1); mmA(0); nxt(); }
  };
  auto runB = [&](int n) {
#pragma unroll 1
    for (int kt = 0; kt < n; ++kt) { bar(); rd(sb, 0); mmB(1); rd(sb, 1); mmB(0); nxt(); }
  };
  auto cpub = [&](int blk) {
    if (panel < 10 && wm == 0) {
#pragma unroll
      for (int n = 0; n < 3; ++n) {
        f32x4 d = MFMA(bfr[0][n], ones, ((f32x4){0.f, 0.f, 0.f, 0.f}));
        d = MFMA(bfr[1][n], ones, d);
        csum_store(ws, WS_GC + (blk * 10 + panel) * 2048 + ca[n] * 128, d, fr, g, tl);
      }
    }
  };
  Side S; side_issue(P, S, panel * 128 + 64 + cbj * 8 + w, w);
  runA(1);
  side_finish(P, rws, S, panel * 128 + 64 + cbj * 8 + w, w, tl, th);
  cpub(8 + cbj);
  runA(6);
  const int tc = tid - 256;
  {
    u64 cv[10];
    if (tid < 256) cv[0] = ld8(ws, WS_GX + panel * 2048 + tid * 8);
    else if (tc < 96) csum_issue(ws, 8 + cbj, tc, cv);
    runA(3);
    if (tid < 256) xstat_gather(ws, panel, L_ST, tid, tg, cv[0]);
    else if (tc < 96) csum_gather(ws, 8 + cbj, tc, tl, cv, L_CS);
  }
  bar(); rd(sb, 0); mmA(1); rd(sb, 1);
  cpub(16 + cbj);
  STAMP(8);
  {
    float cn[3];
#pragma unroll
    for (int n = 0; n < 3; ++n) cn[n] = ldsf(L_CS + (ca[n] * 16 + fr) * 4);
#pragma unroll
    for (int m = 0; m < 2; ++m) {
      const int rl = wm * 32 + m * 16 + 4 * g;
      const f32x4 mr = ldsf4(L_ST + rl * 4), rinv = ldsf4(L_ST + 512 + rl * 4);
      f32x4 rs = {0.f, 0.f, 0.f, 0.f}, rq = {0.f, 0.f, 0.f, 0.f};
#pragma unroll
      for (int n = 0; n < 3; ++n) {
        f32x4 v = accA[m][n] * rinv + mr * cn[n];
        v = gelu4(v);
        rs += v; rq += v * v;
        accA[m][n] = v;
      }
#pragma unroll
      for (int j = 0; j < 4; ++j) { rs[j] = sum16(rs[j]); rq[j] = sum16(rq[j]); }
      if (fr == 0) {
        stsf4(L_RS + ((wn * 128 + rl) * 2) * 4, (f32x4){rs[0], rq[0], rs[1], rq[1]});
        stsf4(L_RS + ((wn * 128 + rl) * 2 + 4) * 4, (f32x4){rs[2], rq[2], rs[3], rq[3]});
      }
    }
  }
  mmB(0); nxt();
  bar(); rd(sb, 0);
  float part = 0.f;
  const unsigned gbase = WS_GR + (unsigned)(panel * 8) * 2048 + tid * 8;
  if (tid < 256) {
    part = ldsf(L_RS + tid * 4) + ldsf(L_RS + 1024 + tid * 4);
    st8u(ws, gbase + cbj * 2048, ((u64)tg << 32) | __float_as_uint(part));
  }
  mmB(1); rd(sb, 1); mmB(0); nxt();
  STAMP(9);
  runB(1);
  {
    u64 cv[10];
    if (tid < 256) {
#pragma unroll
      for (int i = 1; i < 8; ++i) cv[i] = ld8(ws, gbase + ((cbj + i) & 7) * 2048);
    }
    runB(1);
    if (tid < 256) {
      float tot;
#pragma unroll 1
      for (unsigned sp = 0; sp < SPIN_CAP; ++sp) {
        bool ok = true; tot = part;
#pragma unroll
        for (int i = 1; i < 8; ++i) { ok &= (unsigned)(cv[i] >> 32) == tg; tot += __uint_as_float((unsigned)cv[i]); }
        if (__all(ok)) break;
#pragma unroll
        for (int i = 1; i < 8; ++i) cv[i] = ld8(ws, gbase + ((cbj + i) & 7) * 2048);
      }
      float other = dpp<0xB1>(tot);
      if ((tid & 1) == 0) {
        float m = tot * (1.f / FH);
        float v = rbf(fmaxf(other * (1.f / FH) - m * m, 0.f));
        float r = 1.f / rbf(sqrtf(rbf(v + 1e-5f)));
        stsf(L_FS + (tid >> 1) * 4, -rbf(m) * r);
        stsf(L_FS + 512 + (tid >> 1) * 4, r);
      }
    }
  }
  bar(); rd(sb, 0);
  {
    f32x4 mr[2], ri[2];
#pragma unroll
    for (int m = 0; m < 2; ++m) { const int rl = wm * 32 + m * 16 + 4 * g; mr[m] = ldsf4(L_FS + rl * 4); ri[m] = ldsf4(L_FS + 512 + rl * 4); }
#pragma unroll
    for (int n = 0; n < 3; ++n) {
      f32x4 a = accA[0][n] * ri[0] + mr[0], b = accA[1][n] * ri[1] + mr[1];
      st16(rws, WS_Z2N + (unsigned)(((cbj * 64 + panel * 4 + wm) * 6 + ca[n]) * 1024) + l * 16,
           (u32x4){cvt_pk(a[0], a[1]), cvt_pk(a[2], a[3]), cvt_pk(b[0], b[1]), cvt_pk(b[2], b[3])});
    }
  }
  mmB(1); rd(sb, 1); mmB(0); nxt();
  STAMP(10);
  runB(2);
  wait_vm<0>();
  bar();
  if (tid == 0) st8u(ws, WS_F1 + (panel * 32 + 8 + cbj) * 64, tag);
  STAMP(11);
  rd(sb, 0); mmB(1); rd(sb, 1); mmB(0); nxt();
  {
    u64 cv[10];
    if (tid >= 256 && tc < 96) csum_issue(ws, 16 + cbj, tc, cv);
    runB(2);
    if (tid >= 256 && tc < 96) csum_gather(ws, 16 + cbj, tc, tl, cv, L_CS + 512);
  }
  mmB(1);
  STAMP(1);
  bar();
  {
    f32x4 cn[3];
#pragma unroll
    for (int n = 0; n < 3; ++n) { const unsigned a = L_CS + 512 + (ca[n] * 16 + 4 * g) * 4; cn[n] = ldsf4(a); }
    float mr[2], rinv[2];
#pragma unroll
    for (int m = 0; m < 2; ++m) { const int rl = wm * 32 + m * 16 + fr; mr[m] = ldsf(L_ST + rl * 4); rinv[m] = ldsf(L_ST + 512 + rl * 4); }
#pragma unroll
    for (int n = 0; n < 3; ++n) {
      u32x4 o;
#pragma unroll
      for (int m = 0; m < 2; ++m) {
        f32x4 v = accB[m][n] * rinv[m] + cn[n] * mr[m];
        v = gelu4(v);
        o[2 * m] = cvt_pk(v[0], v[1]); o[2 * m + 1] = cvt_pk(v[2], v[3]);
      }
      if (wm < 2) *(LAS u32x4*)(uintptr_t)(lds0 + L_KVT + 6144 + (wm * 6 + ca[n]) * 1024 + l * 16) = o;
      else st16(rws, WS_Z1 + (unsigned)(((panel * 8 + cbj) * 4 + wm) * 6 + ca[n]) * 1024 + l * 16, o);
    }
  }
}

__global__ __launch_bounds__(768, 3) void mega_kernel(Params P) {
  extern __shared__ __attribute__((aligned(16))) char smem[];
  const unsigned lds0 = (unsigned)(uintptr_t)(LAS char*)smem;
  char* ws = P.ws;
  const int tid = threadIdx.x, l = tid & 63;
  const int w = __builtin_amdgcn_readfirstlane(tid >> 6);
  const int g = l >> 4, fr = l & 15;
  const int b = blockIdx.x, xcd = b & 7, bi = b >> 3;
  const int panel1 = (xcd >> 1) * 4 + (bi & 3), cb1 = (xcd & 1) * 4 + ((bi >> 2) & 3);
  const bool isq = bi < 16;
  const int panel2 = 2 * panel1 + (isq ? 1 : 0), cb2 = cb1;
  const int panel3 = xcd * 4 + (bi & 3), cb3 = bi >> 2;
  STAMP(0);
  const int lw = w - 8;
  const unsigned vA0 = (unsigned)(((l >> 3) * D_ + (((l & 7) ^ (l >> 3)) << 3)) * 2);
  const unsigned vA1 = vA0 + 8 * D_ * 2 - 1024, vA2 = vA0 + 16 * D_ * 2 - 2048, vA3 = vA0 + 24 * D_ * 2 - 3072;
  const char* gA = (const char*)(P.x + (size_t)(panel1 * 128 + lw * 32) * D_);
  const int rot = panel1 < 10 ? panel1 : 0;
  unsigned vB[4] = {0u, 0u, 0u, 0u};
  const char* gB = isq ? (const char*)P.wqkv + (size_t)lw * 16 * NQKV * 2 - 4096 : (const char*)P.win + (size_t)lw * 16 * FIN * 2 - 4096;
  if (w >= 8) {
    const int k1 = rot == 0 ? 1 : 0;
    gl4(gA + rot * 128, lds0 + lw * 4096, vA0, vA1, vA2, vA3);
    if (isq) {
#pragma unroll
      for (int i = 0; i < 4; ++i) {
        int c = i * 64 + l; if (c > 239) c = 239;
        const int row = c / 30, cc = c - row * 30, seg = cc / 10, ch = cc - seg * 10;
        vB[i] = (unsigned)((row * NQKV + seg * 640 + cb1 * 80 + ch * 8) * 2) + 4096 - i * 1024;
      }
      const char* bp = gB + (size_t)rot * 64 * NQKV * 2;
      gl4(bp, lds0 + 16384 + lw * 2 * KBS, vB[0], vB[1], vB[2], vB[3]);
      gl4(bp + 8 * NQKV * 2, lds0 + 16384 + (lw * 2 + 1) * KBS, vB[0], vB[1], vB[2], vB[3]);
      gl4(gA + k1 * 128, lds0 + K1_STAGE + lw * 4096, vA0, vA1, vA2, vA3);
      const char* bq = gB + (size_t)k1 * 64 * NQKV * 2;
      gl4(bq, lds0 + K1_STAGE + 16384 + lw * 2 * KBS, vB[0], vB[1], vB[2], vB[3]);
      gl4(bq + 8 * NQKV * 2, lds0 + K1_STAGE + 16384 + (lw * 2 + 1) * KBS, vB[0], vB[1], vB[2], vB[3]);
    } else {
#pragma unroll
      for (int i = 0; i < 3; ++i) {
        const int c = i * 64 + l, row = c / 12, cc = c - row * 12;
        int pc = (cc >> 1) - (row >> 3); if (pc < 0) pc = 5;
        vB[i] = (unsigned)((row * FIN + cb1 * 96 + pc * 16 + (cc & 1) * 8) * 2) + 4096 - i * 1024;
      }
      gl3(gB + FH * 2 + (size_t)rot * 64 * FIN * 2, lds0 + 16384 + lw * 3072, vB[0], vB[1], vB[2]);
      gl4(gA + k1 * 128, lds0 + K1_STAGE + lw * 4096, vA0, vA1, vA2, vA3);
      gl3(gB + FH * 2 + (size_t)k1 * 64 * FIN * 2, lds0 + K1_STAGE + 16384 + lw * 3072, vB[0], vB[1], vB[2]);
    }
  }
  const u64 tag = ld8(ws, WS_EP) + 1;
  if (w == 7 && bi < 20) {
    u64 pc; asm volatile("s_getpc_b64 %0" : "=s"(pc));
    gl2((const char*)(pc & ~127ull) + bi * 2048, lds0 + 3 * K1_STAGE, l * 16, l * 16);
  }

  if (w >= 8) {
    const unsigned v20 = (unsigned)(((l >> 3) * S_ + (((l & 7) ^ (l >> 4)) << 3)) * 2);
    const unsigned v21 = (unsigned)((((l >> 3) + 8) * S_ + (((l & 7) ^ ((l >> 4) | 4)) << 3)) * 2) - 1024;
    const char* gA2 = (const char*)(P.wsp + (size_t)(panel2 * 64 + lw * 16) * S_);
    const int rot2 = 0;
    auto ldA = [&](int kt, unsigned sl) { gl2(gA2 + ((kt + rot2) & 31) * 128, sl + lw * 2048, v20, v21); };
    auto sl2 = [&](int kt) { return lds0 + ((kt + 4) % 6) * P2_SLOT; };
    const unsigned tl = (unsigned)tag, th = tl * 0x9E3779B1u ^ (unsigned)(tag >> 32);
    const int lt = tid - 512;
    auto gather = [&](int blk, int ncol, unsigned dst, bool xs) {
      u64 cv[10]; u64 gv = 0;
      const int tc = lt < ncol ? lt : 0;
      if (xs) gv = ld8(ws, WS_GX + panel1 * 2048 + lt * 8);
      csum_issue(ws, blk, tc, cv);
      if (xs) xstat_gather(ws, panel1, lds0 + L_MISC, lt, (lt & 1) ? th : tl, gv);
      csum_gather(ws, blk, tc, tl, cv, dst);
    };
    const unsigned l16 = l * 16;
    const char* gB2 = ws + WS_Z2N + (size_t)cb2 * 64 * 6144 + lw * 3072;
    auto ldB = [&](int kt, unsigned sl) { gl3c(gB2 + ((kt + rot2) & 31) * 12288, sl + 8192 + lw * 3072, l16); };
    const unsigned zfl = WS_F1 + ((l & 15) * 32 + 8 + cb2) * 64;
    auto try_z = [&]() {
      const u64 v = l < 16 ? ld8(ws, zfl) : tag;
      return (bool)__builtin_amdgcn_readfirstlane((int)__all(v == tag));
    };
    bool early = false;
    auto krot = [&](int kt) { const int k = kt >= 10 ? kt - 10 : kt; return __builtin_amdgcn_readfirstlane(k == 0 ? rot : (k - 1 < rot ? k - 1 : k)); };
    if (isq) {
      auto stageB = [&](int kt, unsigned sb) {
        const char* bp = gB + (size_t)krot(kt) * 64 * NQKV * 2;
        gl4(bp, sb + 16384 + lw * 2 * KBS, vB[0], vB[1], vB[2], vB[3]);
        gl4(bp + 8 * NQKV * 2, sb + 16384 + (lw * 2 + 1) * KBS, vB[0], vB[1], vB[2], vB[3]);
      };
      auto stage = [&](int kt, unsigned sb) { gl4(gA + krot(kt) * 128, sb + lw * 4096, vA0, vA1, vA2, vA3); stageB(kt, sb); };
      wait_vm<13>(); bar();
      stage(2, lds0 + 2 * K1_STAGE);
      unsigned sb = lds0, sc = lds0 + K1_STAGE;
#pragma unroll 1
      for (int kt = 1; kt < 8; ++kt) {
        wait_vm<12>(); bar();
        if (kt == 2) STAMP(10);
        if (kt == 7) STAMP(11);
        stage(kt + 2, sb);
        sb = sc; sc += K1_STAGE; if (sc == lds0 + 3 * K1_STAGE) sc = lds0;
      }
      wait_vm<12>(); bar();
      gather(cb1, 240, lds0 + L_MISC + 5120, true);
      bar();
      ldA(0, sl2(0)); ldA(1, sl2(1)); ldA(5, sl2(5));
      bar();
      ldA(4, sl2(4));
#pragma unroll 1
      for (int t = 0; t < 3 && !early; ++t) early = try_z();
      if (early) { ldB(0, sl2(0)); ldB(1, sl2(1)); ldB(4, sl2(4)); ldB(5, sl2(5)); }
      bar();
      if (!early) { early = try_z(); if (early) { ldB(0, sl2(0)); ldB(1, sl2(1)); ldB(4, sl2(4)); ldB(5, sl2(5)); } }
      bar();
    } else {
      auto stageB = [&](int kt, unsigned sb) {
        const int k = krot(kt);
        gl3(gB + (kt < 10 ? FH * 2 : 0) + (size_t)k * 64 * FIN * 2, sb + 16384 + lw * 3072, vB[0], vB[1], vB[2]);
      };
      auto stage = [&](int kt, unsigned sb) {
        const int k = krot(kt);
        gl4(gA + k * 128, sb + lw * 4096, vA0, vA1, vA2, vA3); stageB(kt, sb);
      };
      wait_vm<8>(); bar();
      stage(2, lds0 + 2 * K1_STAGE);
      unsigned sb = lds0, sc = lds0 + K1_STAGE;
      auto runL = [&](int k0, int k1) {
#pragma unroll 1
        for (int kt = k0; kt < k1; ++kt) {
          wait_vm<7>(); bar();
          stage(kt + 2, sb);
          sb = sc; sc += K1_STAGE; if (sc == lds0 + 3 * K1_STAGE) sc = lds0;
        }
      };
      runL(1, 11);
      {
        const char* wp = (const char*)(P.wsp + (size_t)(panel1 * 128 + lw * 32) * S_) + (cb1 & 3) * 1024;
        const unsigned vo = (unsigned)((l >> 3) * S_ * 2 + (l & 7) * 128);
        unsigned d0, d1, d2, d3;
        asm volatile("global_load_dword %0, %4, %5\n\tglobal_load_dword %1, %4, %6\n\tglobal_load_dword %2, %4, %7\n\tglobal_load_dword %3, %4, %8"
                     : "=&v"(d0), "=&v"(d1), "=&v"(d2), "=&v"(d3)
                     : "v"(vo), "s"(wp), "s"(wp + 8 * S_ * 2), "s"(wp + 16 * S_ * 2), "s"(wp + 24 * S_ * 2) : "memory");
        runL(11, 13);
        asm volatile("" :: "v"(d0), "v"(d1), "v"(d2), "v"(d3));
      }
      runL(13, 18);
      wait_vm<7>(); bar();
      wait_vm<0>(); bar();
      bar();
      wait_words(ws, zfl, l < 16, tag);
      early = true;
      ldA(0, sl2(0)); ldB(0, sl2(0)); ldA(1, sl2(1)); ldB(1, sl2(1));
      ldA(4, sl2(4)); ldA(5, sl2(5)); ldB(4, sl2(4)); ldB(5, sl2(5));
    }
    STAMP(2);
    if (!early) {
      wait_words(ws, zfl, l < 16, tag);
      ldB(0, sl2(0)); ldB(1, sl2(1)); ldB(4, sl2(4)); ldB(5, sl2(5));
    }
    STAMP(3);
    ldA(2, sl2(2)); ldA(3, sl2(3)); ldB(2, sl2(2)); ldB(3, sl2(3));
    wait_vm<16>(); bar();
    {
      unsigned sl = sl2(0);
      auto ldU = [&](int u) {
        ldA(2 * u, sl); ldB(2 * u, sl); ldA(2 * u + 1, sl + P2_SLOT); ldB(2 * u + 1, sl + P2_SLOT);
        sl += 2 * P2_SLOT; if (sl == lds0 + 6 * P2_SLOT) sl = lds0;
      };
      wait_vm<0>(); bar(); ldU(3);
#pragma unroll 1
      for (int u = 2; u < 14; ++u) {
        wait_vm<10>(); bar();
        if (u == 4) STAMP(8);
        if (u == 10) STAMP(9);
        ldU(u + 2);
      }
    }
    wait_vm<10>(); bar();
    wait_words(ws, WS_F1 + (((lw * 64 + l) >> 4) * 32 + 16 + (l & 15)) * 64, true, tag);
    wait_vm<0>(); bar();
    if (b == 0 && tid == 512) st8u(ws, WS_EP, tag);
    STAMP(4);
    const char* gA3 = ws + WS_OH + (size_t)panel3 * 44 * 4096 + lw * 4096;
    const char* gB3 = ws + WS_WF + (size_t)cb3 * 44 * 5120 + lw * 5120;
    int r5 = cb3 + 2 * (bi & 3); r5 = r5 >= 10 ? r5 - 10 : r5 >= 5 ? r5 - 5 : r5;
    int r6 = cb3 + 2 * (bi & 3); r6 = r6 >= 12 ? r6 - 12 : r6 >= 6 ? r6 - 6 : r6;
    auto ur = [&](int u) {
      int v;
      if (u < 5) { v = u + r5; if (v >= 5) v -= 5; } else { v = u + r6; if (v >= 11) v -= 6; }
      return __builtin_amdgcn_readfirstlane(v);
    };
    auto ldB3 = [&](int u, unsigned sl) {
      gl4c(gB3 + ur(u) * 20480, sl + 16384 + lw * 5120, l16);
      glds16c(gB3 + ur(u) * 20480 + 4096, l16, sl + 16384 + lw * 5120 + 4096);
    };
    auto ldA3 = [&](int u, unsigned sl) { gl4c(gA3 + ur(u) * 16384, sl + lw * 4096, l16); };
    bar(); bar();
    ldB3(0, lds0); ldB3(1, lds0 + P3_SLOT); ldB3(2, lds0 + 2 * P3_SLOT);
    wait_words(ws, WS_FO + ((panel3 >> 1) * 8 + (l & 7)) * 64, l < 8, tag);
    ldA3(0, lds0); ldA3(1, lds0 + P3_SLOT); ldA3(2, lds0 + 2 * P3_SLOT);
    bar();
    ldA3(3, lds0 + 3 * P3_SLOT); ldB3(3, lds0 + 3 * P3_SLOT);
    wait_vm<17>(); bar();
    wait_vm<13>(); bar(); ldA3(4, lds0); ldB3(4, lds0);
    wait_vm<18>(); bar(); ldB3(5, lds0 + P3_SLOT);
    wait_vm<14>(); bar(); ldB3(6, lds0 + 2 * P3_SLOT);
    wait_vm<10>(); bar(); ldB3(7, lds0 + 3 * P3_SLOT);
    wait_words(ws, WS_F2 + (panel3 * 32 + (l & 31)) * 64, l < 32, tag);
    STAMP(5);
    ldA3(5, lds0 + P3_SLOT); ldA3(6, lds0 + 2 * P3_SLOT); ldA3(7, lds0 + 3 * P3_SLOT);
    wait_vm<8>(); bar(); ldA3(8, lds0); ldB3(8, lds0);
    wait_vm<13>(); bar(); ldA3(9, lds0 + P3_SLOT); ldB3(9, lds0 + P3_SLOT);
    wait_vm<18>(); bar(); ldA3(10, lds0 + 2 * P3_SLOT); ldB3(10, lds0 + 2 * P3_SLOT);
    wait_vm<18>(); bar(); wait_vm<9>(); bar(); wait_vm<0>(); bar();
    bar(); bar();
    STAMP(6);
    return;
  }

  const rsrc_t rws = __builtin_amdgcn_make_buffer_rsrc(ws, 0, (int)WS_END, 0x00020000);
  bf16x8 pf[3];
  if (isq) p1q_consumer(P, rws, w, panel1, cb1, lds0, tag, pf);
  else p1z_consumer(P, rws, w, panel1, cb1, lds0, tag);
  STAMP(2);
  wf_job(P, rws, w);

  u32x4 xv[2];
  unsigned xo0, xo1, la0, la1;
  {
    const int wt = w & 3, kh = w >> 2;
    const int rsw = ((wt * 16 + fr) >> 1) & 7;
    const unsigned ao0 = (unsigned)((wt * 16 + fr) * 128 + (((4 * kh + (g >> 1)) ^ rsw) << 4) + (g & 1) * 8);
    const unsigned ao1 = (unsigned)((wt * 16 + fr) * 128 + (((4 * kh + 2 + (g >> 1)) ^ rsw) << 4) + (g & 1) * 8);
    const unsigned bo = (unsigned)(8192 + kh * 6144 + l * 16);
    const bf16x8 zeros = {0, 0, 0, 0, 0, 0, 0, 0};
    f32x4 acc[6];
#pragma unroll
    for (int n = 0; n < 6; ++n) acc[n] = (f32x4){0.f, 0.f, 0.f, 0.f};
    bf16x8 af[2], bfr[2][6];
    af[1] = zeros;
#pragma unroll
    for (int n = 0; n < 6; ++n) bfr[1][n] = zeros;
    const int zi = (bi & 15) * 8 + xcd;
    const bool reducer = !isq && zi < 72;
    const int rj = zi & 7, rf = zi >> 3;
    const int row = panel2 * 64 + wt * 16 + fr;
    u32x2 zreg[6];
    const float bs = bf2f(P.bsp[row]);
    unsigned sl = lds0 + 4 * P2_SLOT;
    auto step = [&](int cur, int kt) {
      if (cur == 0) bar();
      af[cur] = lds_2b64(sl + ao0, sl + ao1);
#pragma unroll
      for (int n = 0; n < 6; ++n) bfr[cur][n] = lds_b128(sl + bo + n * 1024);
      SB0;
#pragma unroll
      for (int n = 0; n < 6; ++n) acc[n] = MFMA(bfr[cur ^ 1][n], af[cur ^ 1], acc[n]);
      SB0;
      sl += P2_SLOT; if (sl == lds0 + 6 * P2_SLOT) sl = lds0;
    };
    auto run = [&](int k0, int k1) {
#pragma unroll 1
      for (int kt = k0; kt < k1; kt += 2) { step(0, kt); step(1, kt + 1); }
    };
    if (reducer) {
      const int et = (rf + 1) >> 1, st = rf == 0 ? 0 : ((rf + 1) & 1) + (et >= 3 ? 1 : 0);
      wait_words(ws, WS_F1 + ((l & 15) * 32 + rj) * 64, true, tag);
      f32x4 pv[8];
      bool okv[4];
#pragma unroll
      for (int t4 = 0; t4 < 4; ++t4) {
        const int i = 2 * st + (t4 & 1);
        okv[t4] = i < 5 && (t4 >= 2 || (et - i <= 1 && i - et <= 1));
        const int job = okv[t4] ? (t4 >= 2 ? 13 + i : 2 * i + et) : 0;
#pragma unroll
        for (int pp = 0; pp < 2; ++pp)
          pv[t4 * 2 + pp] = __builtin_bit_cast(f32x4, ld16(rws, WS_KVP + (unsigned)(((rj * 18 + job) * 16 + w * 2 + pp) * 1024) + l * 16));
      }
      run(0, 2);
      if (tid == 0) st8u(ws, WS_F1 + (panel1 * 32 + 16 + cb1) * 64, tag);
      const unsigned L_R = lds0 + 122880;
      f32x4 sv[4];
#pragma unroll
      for (int t4 = 0; t4 < 4; ++t4) { sv[t4] = pv[t4 * 2] + pv[t4 * 2 + 1]; if (!okv[t4]) sv[t4] = (f32x4){0.f, 0.f, 0.f, 0.f}; }
      if (w >= 4) {
#pragma unroll
        for (int t4 = 0; t4 < 4; ++t4) stsf4(L_R + ((w - 4) * 4 + t4) * 1024 + l * 16, sv[t4]);
      }
      run(2, 4);
      if (w < 4) {
#pragma unroll
        for (int t4 = 0; t4 < 4; ++t4) stsf4(L_R + (w * 4 + t4) * 1024 + l * 16, sv[t4] + ldsf4(L_R + (w * 4 + t4) * 1024 + l * 16));
      }
      run(4, 6);
      if (w < 2) {
        f32x4 v[2];
#pragma unroll
        for (int h2 = 0; h2 < 2; ++h2) {
          v[h2] = (f32x4){0.f, 0.f, 0.f, 0.f};
#pragma unroll
          for (int ww = 0; ww < 4; ++ww) v[h2] += ldsf4(L_R + (ww * 4 + w * 2 + h2) * 1024 + l * 16);
          const int he = (et * 16 + fr) / 20;
#pragma unroll
          for (int r = 0; r < 4; ++r) { const int d = (2 * st + h2) * 16 + 4 * g + r; if (d / 20 != he) v[h2][r] = 0.f; }
        }
        st16(rws, WS_KVT + (unsigned)(((rj * 2 + w) * 9 + rf) * 1024) + l * 16,
             (u32x4){cvt_pk(v[0][0], v[0][1]), cvt_pk(v[0][2], v[0][3]), cvt_pk(v[1][0], v[1][1]), cvt_pk(v[1][2], v[1][3])});
      }
      run(6, 10);
      wait_vm<0>();
      if (w < 2 && l == 0) st8u(ws, WS_FK + (rj * 32 + w * 16 + rf) * 64, tag);
      run(10, 18);
    } else {
      run(0, 6);
      wait_vm<0>();
      run(6, 8);
      if (tid == 0) st8u(ws, WS_F1 + (panel1 * 32 + (isq ? 24 : 16) + cb1) * 64, tag);
      run(8, 18);
    }
    if (isq) {
      const unsigned foff = (l & 15) < 9 ? WS_FK + (cb2 * 32 + (l & 31)) * 64 : WS_F1 + (panel1 * 32 + 16 + cb2) * 64;
      const u64 fv = ld8(ws, foff);
      run(18, 22);
      if (!__all(fv == tag)) wait_words(ws, foff, l < 32, tag);
#pragma unroll
      for (int i = 0; i < 3; ++i) {
        int ins = w + 8 * i; if (ins >= 18) ins = 17;
        ins = __builtin_amdgcn_readfirstlane(ins);
        glds16c(ws, WS_KVT + cb2 * 18432 + ins * 1024 + l * 16, __builtin_amdgcn_readfirstlane(lds0 + L_KVT + ins * 1024));
      }
      const unsigned zo = WS_Z1 + (unsigned)(((panel1 * 8 + cb2) * 4 + 2 + (wt >> 1)) * 6) * 1024 + l * 16 + (wt & 1) * 8;
#pragma unroll
      for (int n = 0; n < 6; ++n) zreg[n] = __builtin_amdgcn_raw_buffer_load_b64(rws, zo + n * 1024, 0, 16);
      run(22, 24);
      wait_vm<0>();
      run(24, 26);
      u32x2 ov[5];
#pragma unroll
      for (int et = 0; et < 5; ++et) {
        f32x4 num = {0.f, 0.f, 0.f, 0.f}, den = {0.f, 0.f, 0.f, 0.f};
        const int st0 = et >= 3 ? 1 : 0, nst = et == 0 ? 1 : 2, f0 = et == 0 ? 0 : 1 + (et - 1) * 2;
#pragma unroll
        for (int s = 0; s < nst; ++s) {
          bf16x8 ka = lds_b128(lds0 + L_KVT + (f0 + s) * 1024 + l * 16), za = lds_b128(lds0 + L_KVT + (9 + f0 + s) * 1024 + l * 16);
          num = MFMA(ka, pf[st0 + s], num); den = MFMA(za, pf[st0 + s], den);
        }
        float v[4];
#pragma unroll
        for (int j = 0; j < 4; ++j) v[j] = num[j] * __builtin_amdgcn_rcpf(den[j] + 1e-6f);
        ov[et] = (u32x2){cvt_pk(v[0], v[1]), cvt_pk(v[2], v[3])};
      }
      const unsigned ob = WS_OH + (unsigned)(((2 * panel1 + kh) * 44 + ((cb2 * 5) >> 1)) * 4 + wt) * 1024 + l * 16;
      if ((cb2 & 1) == 0) {
        st16(rws, ob, (u32x4){ov[0][0], ov[0][1], ov[1][0], ov[1][1]});
        st16(rws, ob + 4096, (u32x4){ov[2][0], ov[2][1], ov[3][0], ov[3][1]});
        st8(ws, ob + 8192, ov[4]);
      } else {
        st8(ws, ob + 8, ov[0]);
        st16(rws, ob + 4096, (u32x4){ov[1][0], ov[1][1], ov[2][0], ov[2][1]});
        st16(rws, ob + 8192, (u32x4){ov[3][0], ov[3][1], ov[4][0], ov[4][1]});
      }
      run(26, 30);
      wait_vm<0>();
      run(30, 32);
      if (tid == 0) st8u(ws, WS_FO + (panel1 * 8 + cb2) * 64, tag);
    } else {
      run(18, 32);
#pragma unroll
      for (int n = 0; n < 6; ++n) zreg[n] = *(LAS u32x2*)(uintptr_t)(lds0 + L_KVT + 6144 + ((wt >> 1) * 6 + n) * 1024 + l * 16 + (wt & 1) * 8);
    }
#pragma unroll
    for (int n = 0; n < 6; ++n) acc[n] = MFMA(bfr[1][n], af[1], acc[n]);
    STAMP(4);
    {
      const int r0 = tid / 10, h0 = tid - r0 * 10, c1 = (tid & 127) + 512, r1 = c1 / 10, h1 = c1 - r1 * 10;
      xo0 = (unsigned)((panel3 * 64 + r0) * D_ + cb3 * 80 + h0 * 8); xo1 = (unsigned)((panel3 * 64 + r1) * D_ + cb3 * 80 + h1 * 8);
      la0 = lds0 + (unsigned)((r0 * 84 + h0 * 8) * 4); la1 = lds0 + (unsigned)((r1 * 84 + h1 * 8) * 4);
      xv[0] = *(const u32x4*)(P.x + xo0); xv[1] = *(const u32x4*)(P.x + xo1);
    }
    bar();
    const unsigned L_RED = lds0 + 3 * P3_SLOT;
    if (kh == 1) {
#pragma unroll
      for (int n = 0; n < 6; ++n) stsf4(L_RED + (wt * 6 + n) * 1024 + l * 16, acc[n]);
    }
    bar();
    if (kh == 0) {
#pragma unroll
      for (int n = 0; n < 6; ++n) acc[n] += ldsf4(L_RED + (wt * 6 + n) * 1024 + l * 16);
      u32x2 hv[6];
#pragma unroll
      for (int n = 0; n < 6; ++n) {
        f32x4 gg = acc[n] + bs;
        hv[n] = (u32x2){cvt_pk(lo16(zreg[n][0]) * gg[0], hi16(zreg[n][0]) * gg[1]), cvt_pk(lo16(zreg[n][1]) * gg[2], hi16(zreg[n][1]) * gg[3])};
      }
      const unsigned hb = WS_OH + (unsigned)((panel2 * 44 + 20 + cb2 * 3) * 4 + wt) * 1024 + l * 16;
#pragma unroll
      for (int s = 0; s < 3; ++s) st16(rws, hb + s * 4096, (u32x4){hv[2 * s][0], hv[2 * s][1], hv[2 * s + 1][0], hv[2 * s + 1][1]});
      wait_vm<0>();
      if (l == 0) st8u(ws, WS_F2 + ((panel2 * 8 + cb2) * 4 + wt) * 64, tag);
    }
    bar();
    STAMP(5);
  }
  {
    const int wsr = w & 3, kh = w >> 2;
    const unsigned ao = (unsigned)(kh * 8192 + wsr * 1024 + l * 16);
    const unsigned bb = 16384u + kh * 10240 + l * 16;
    const bf16x8 zeros = {0, 0, 0, 0, 0, 0, 0, 0};
    f32x4 acc[5];
#pragma unroll
    for (int n = 0; n < 5; ++n) acc[n] = (f32x4){0.f, 0.f, 0.f, 0.f};
    bf16x8 af[2][2], bfr[2][2][5];
    unsigned sb = lds0;
    auto mm = [&](int c) {
#pragma unroll
      for (int s2 = 0; s2 < 2; ++s2)
#pragma unroll
        for (int n = 0; n < 5; ++n) acc[n] = MFMA(bfr[c][s2][n], af[c][s2], acc[n]);
      SB0;
    };
    auto rdu = [&](int c) {
      bar();
#pragma unroll
      for (int s2 = 0; s2 < 2; ++s2) {
        af[c][s2] = lds_b128(sb + ao + s2 * 4096);
#pragma unroll
        for (int n = 0; n < 5; ++n) bfr[c][s2][n] = lds_b128(sb + bb + s2 * 5120 + n * 1024);
      }
      SB0;
      sb += P3_SLOT; if (sb == lds0 + 4 * P3_SLOT) sb = lds0;
    };
    rdu(0);
#pragma unroll 1
    for (int u = 0; u < 5; ++u) { rdu(1); mm(0); rdu(0); mm(1); }
    mm(0);
    STAMP(6);
    bar();
#pragma unroll
    for (int n = 0; n < 5; ++n) stsf4(lds0 + (unsigned)(((kh * 64 + wsr * 16 + fr) * 84 + n * 16 + 4 * g) * 4), acc[n]);
    bar();
#pragma unroll
    for (int i = 0; i < 2; ++i) {
      if (i == 0 || tid < 128) {
        const unsigned a = i == 0 ? la0 : la1;
        f32x4 a0 = ldsf4(a) + ldsf4(a + 64 * 84 * 4), a1 = ldsf4(a + 16) + ldsf4(a + 64 * 84 * 4 + 16);
        u32x4 o = {cvt_pk(lo16(xv[i][0]) + a0[0], hi16(xv[i][0]) + a0[1]), cvt_pk(lo16(xv[i][1]) + a0[2], hi16(xv[i][1]) + a0[3]),
                   cvt_pk(lo16(xv[i][2]) + a1[0], hi16(xv[i][2]) + a1[1]), cvt_pk(lo16(xv[i][3]) + a1[2], hi16(xv[i][3]) + a1[3])};
        *(u32x4*)(P.out + (i == 0 ? xo0 : xo1)) = o;
      }
    }
    STAMP(7);
  }
}

extern "C" void kernel_launch(void* const* d_in, const int* in_sizes, int n_in, void* d_out, int out_size,
                              void* d_ws, size_t ws_size, hipStream_t stream) {
  Params P;
  P.x = (const u16*)d_in[0];
  P.wqkv = (const u16*)d_in[1];
  P.wo = (const u16*)d_in[2];
  P.win = (const u16*)d_in[4];
  P.wsp = (const u16*)d_in[5];
  P.bsp = (const u16*)d_in[6];
  P.wout = (const u16*)d_in[7];
  P.out = (u16*)d_out;
  P.ws = (char*)d_ws;
  (void)hipFuncSetAttribute((const void*)mega_kernel, hipFuncAttributeMaxDynamicSharedMemorySize, LDS_TOTAL);
  mega_kernel<<<256, 768, LDS_TOTAL, stream>>>(P);
}
```

```cpp
#include <hip/hip_runtime.h>
#include <hip/hip_bf16.h>
#include <stdint.h>

typedef __attribute__((ext_vector_type(8))) short bf16x8;
typedef __attribute__((ext_vector_type(4))) short bf16x4;
typedef __attribute__((ext_vector_type(4))) float f32x4;
typedef __attribute__((ext_vector_type(2))) float f32x2;
typedef __attribute__((ext_vector_type(2))) __bf16 bf2;
typedef __attribute__((ext_vector_type(4))) unsigned u32x4;
typedef __attribute__((ext_vector_type(2))) unsigned u32x2;
typedef unsigned short u16;
typedef unsigned long long u64;
#define LAS __attribute__((address_space(3)))
#define GAS __attribute__((address_space(1)))
#define DI __device__ __forceinline__
#define RLX_AGENT __ATOMIC_RELAXED, __HIP_MEMORY_SCOPE_AGENT

constexpr int S_ = 2048, D_ = 640, NQKV = 1920, FIN = 1536, FH = 768;

constexpr unsigned WS_PQ  = 0;
constexpr unsigned WS_Z1  = WS_PQ + 16 * 8 * 8 * 3 * 1024;
constexpr unsigned WS_Z2N = WS_Z1 + 16 * 8 * 24 * 1024;
constexpr unsigned WS_OH  = WS_Z2N + 8 * 64 * 6 * 1024;
constexpr unsigned WS_WF  = WS_OH + 32 * 44 * 4 * 1024;
constexpr unsigned WS_KVT = WS_WF + 8 * 44 * 5 * 1024;
constexpr unsigned WS_KVP = WS_KVT + 8 * 18 * 1024;
constexpr unsigned WS_GR  = WS_KVP + 8 * 18 * 16 * 1024;
constexpr unsigned WS_GX  = WS_GR + 16 * 8 * 256 * 8;
constexpr unsigned WS_GC  = WS_GX + 2048 * 16;
constexpr unsigned WS_F1  = WS_GC + 24 * 10 * 2048;
constexpr unsigned WS_F2  = WS_F1 + 16 * 32 * 64;
constexpr unsigned WS_FO  = WS_F2 + 1024 * 64;
constexpr unsigned WS_FK  = WS_FO + 256 * 64;
constexpr unsigned WS_EP  = WS_FK + 256 * 64;
constexpr unsigned WS_END = WS_EP + 64;

#define STAMP(i)

DI float bf2f(u16 v) { return __uint_as_float(((unsigned)v) << 16); }
DI unsigned cvt_pk(float lo, float hi) {
  bf2 r; r[0] = (__bf16)lo; r[1] = (__bf16)hi; return __builtin_bit_cast(unsigned, r);
}
DI float rbf(float x) { return (float)((__bf16)x); }
DI float lo16(unsigned u) { return __uint_as_float(u << 16); }
DI float hi16(unsigned u) { return __uint_as_float(u & 0xffff0000u); }

DI void glds16(const void* gbase, unsigned voff, unsigned lds_dst) {
  unsigned keep;
  asm volatile("s_mov_b32 %0, m0\n\ts_mov_b32 m0, %3\n\ts_nop 0\n\tglobal_load_lds_dwordx4 %1, %2\n\ts_mov_b32 m0, %0"
               : "=&s"(keep) : "v"(voff), "s"(gbase), "s"(lds_dst) : "memory");
}
DI void glds16c(const void* gbase, unsigned voff, unsigned lds_dst) {
  unsigned keep;
  asm volatile("s_mov_b32 %0, m0\n\ts_mov_b32 m0, %3\n\ts_nop 0\n\tglobal_load_lds_dwordx4 %1, %2 sc1\n\ts_mov_b32 m0, %0"
               : "=&s"(keep) : "v"(voff), "s"(gbase), "s"(lds_dst) : "memory");
}
#define GL_PRE "s_mov_b32 %0, m0\n\ts_mov_b32 m0, %2\n\ts_nop 0\n\t"
#define GL_POST "s_mov_b32 m0, %0"
DI void gl2(const void* gb, unsigned lds, unsigned v0, unsigned v1) {
  unsigned keep;
  asm volatile(GL_PRE "global_load_lds_dwordx4 %3, %1\n\tglobal_load_lds_dwordx4 %4, %1 offset:1024\n\t" GL_POST
               : "=&s"(keep) : "s"(gb), "s"(lds), "v"(v0), "v"(v1) : "memory");
}
DI void gl3(const void* gb, unsigned lds, unsigned v0, unsigned v1, unsigned v2) {
  unsigned keep;
  asm volatile(GL_PRE "global_load_lds_dwordx4 %3, %1\n\tglobal_load_lds_dwordx4 %4, %1 offset:1024\n\tglobal_load_lds_dwordx4 %5, %1 offset:2048\n\t" GL_POST
               : "=&s"(keep) : "s"(gb), "s"(lds), "v"(v0), "v"(v1), "v"(v2) : "memory");
}
DI void gl4(const void* gb, unsigned lds, unsigned v0, unsigned v1, unsigned v2, unsigned v3) {
  unsigned keep;
  asm volatile(GL_PRE "global_load_lds_dwordx4 %3, %1\n\tglobal_load_lds_dwordx4 %4, %1 offset:1024\n\tglobal_load_lds_dwordx4 %5, %1 offset:2048\n\tglobal_load_lds_dwordx4 %6, %1 offset:3072\n\t" GL_POST
               : "=&s"(keep) : "s"(gb), "s"(lds), "v"(v0), "v"(v1), "v"(v2), "v"(v3) : "memory");
}
DI void gl3c(const void* gb, unsigned lds, unsigned v0) {
  unsigned keep;
  asm volatile(GL_PRE "global_load_lds_dwordx4 %3, %1 sc1\n\tglobal_load_lds_dwordx4 %3, %1 offset:1024 sc1\n\tglobal_load_lds_dwordx4 %3, %1 offset:2048 sc1\n\t" GL_POST
               : "=&s"(keep) : "s"(gb), "s"(lds), "v"(v0) : "memory");
}
DI void gl4c(const void* gb, unsigned lds, unsigned v0) {
  unsigned keep;
  asm volatile(GL_PRE "global_load_lds_dwordx4 %3, %1 sc1\n\tglobal_load_lds_dwordx4 %3, %1 offset:1024 sc1\n\tglobal_load_lds_dwordx4 %3, %1 offset:2048 sc1\n\tglobal_load_lds_dwordx4 %3, %1 offset:3072 sc1\n\t" GL_POST
               : "=&s"(keep) : "s"(gb), "s"(lds), "v"(v0) : "memory");
}
template <int N> DI void wait_vm() { asm volatile("s_waitcnt vmcnt(%0)" :: "n"(N) : "memory"); }

DI bf16x8 lds_b128(unsigned addr) { return *(LAS bf16x8*)(uintptr_t)addr; }
DI bf16x8 lds_2b64(unsigned addr0, unsigned addr1) {
  bf16x4 a = *(LAS bf16x4*)(uintptr_t)addr0, b = *(LAS bf16x4*)(uintptr_t)addr1;
  return __builtin_shufflevector(a, b, 0, 1, 2, 3, 4, 5, 6, 7);
}
DI bf16x8 lds_tr(unsigned addr0, unsigned addr1) {
  bf16x4 a = __builtin_amdgcn_ds_read_tr16_b64_v4i16((LAS bf16x4*)(uintptr_t)addr0);
  bf16x4 b = __builtin_amdgcn_ds_read_tr16_b64_v4i16((LAS bf16x4*)(uintptr_t)addr1);
  return __builtin_shufflevector(a, b, 0, 1, 2, 3, 4, 5, 6, 7);
}
DI float ldsf(unsigned a) { return *(LAS float*)(uintptr_t)a; }
DI void stsf(unsigned a, float v) { *(LAS float*)(uintptr_t)a = v; }
DI f32x4 ldsf4(unsigned a) { return *(LAS f32x4*)(uintptr_t)a; }
DI void stsf4(unsigned a, f32x4 v) { *(LAS f32x4*)(uintptr_t)a = v; }
#define SB0 __builtin_amdgcn_sched_barrier(0)
#define MFMA(a, b, c) __builtin_amdgcn_mfma_f32_16x16x32_bf16((a), (b), (c), 0, 0, 0)

template <int CTRL> DI float dpp(float v) {
  return __builtin_bit_cast(float, __builtin_amdgcn_update_dpp(0, __builtin_bit_cast(int, v), CTRL, 0xf, 0xf, true));
}
DI float sum8(float v) { v += dpp<0xB1>(v); v += dpp<0x4E>(v); v += dpp<0x141>(v); return v; }
DI float sum16(float v) { v = sum8(v); v += dpp<0x140>(v); return v; }

DI float gelu_t(float x) {
  float u = x * (0.7978845608f + 0.0356774081f * x * x);
  float e = __builtin_amdgcn_exp2f(-2.885390082f * u);
  return x * __builtin_amdgcn_rcpf(1.0f + e);
}
DI f32x4 gelu4(f32x4 x) {
  const f32x4 a = x * ((x * x) * -0.1029432397f + -2.302208198f);
  f32x4 e;
#pragma unroll
  for (int j = 0; j < 4; ++j) e[j] = __builtin_amdgcn_exp2f(a[j]);
  e += 1.0f;
#pragma unroll
  for (int j = 0; j < 4; ++j) e[j] = __builtin_amdgcn_rcpf(e[j]);
  return x * e;
}
DI f32x4 phi4(f32x4 t) {
  f32x4 a, m;
#pragma unroll
  for (int j = 0; j < 4; ++j) { a[j] = fminf(t[j], 0.f); m[j] = fmaxf(t[j], 0.f); }
  a *= 1.4426950409f;
#pragma unroll
  for (int j = 0; j < 4; ++j) a[j] = __builtin_amdgcn_exp2f(a[j]);
  return (m + 1e-6f) + a;
}
DI float phi_f(float t) {
  return (fmaxf(t, 0.f) + 1e-6f) + __builtin_amdgcn_exp2f(fminf(t, 0.f) * 1.4426950409f);
}

typedef __amdgpu_buffer_rsrc_t rsrc_t;
DI void st16(rsrc_t r, unsigned off, u32x4 v) { __builtin_amdgcn_raw_buffer_store_b128(v, r, off, 0, 16); }
DI u32x4 ld16(rsrc_t r, unsigned off) { return __builtin_amdgcn_raw_buffer_load_b128(r, off, 0, 16); }
DI void st8(char* ws, unsigned off, u32x2 v) { __hip_atomic_store((GAS u64*)(ws + off), ((u64)v[1] << 32) | v[0], RLX_AGENT); }
DI void st8u(char* ws, unsigned off, u64 v) { __hip_atomic_store((GAS u64*)(ws + off), v, RLX_AGENT); }
DI void st4(char* ws, unsigned off, float v) { __hip_atomic_store((GAS unsigned*)(ws + off), __float_as_uint(v), RLX_AGENT); }
DI u64 ld8(const char* ws, unsigned off) { return __hip_atomic_load((GAS u64*)(ws + off), RLX_AGENT); }
constexpr unsigned SPIN_CAP = 1u << 20;
DI void wait_words(const char* ws, unsigned off, bool act, u64 tag) {
#pragma unroll 1
  for (unsigned sp = 0; sp < SPIN_CAP; ++sp) {
    u64 v = act ? ld8(ws, off) : tag;
    if (__all(v == tag)) break;
    __builtin_amdgcn_s_sleep(1);
  }
  __builtin_amdgcn_fence(__ATOMIC_ACQUIRE, "wavefront");
}

struct Params { const u16 *x, *wqkv, *wo, *win, *wsp, *bsp, *wout; u16* out; char* ws; };

constexpr unsigned KBS = 4224;
constexpr unsigned K1_STAGE = 16384 + 8 * KBS;
constexpr unsigned KZ_STAGE = 28672;
constexpr unsigned L_MISC = 153600;
constexpr int LDS_TOTAL = 163840;
constexpr unsigned P2_SLOT = 20480;
constexpr unsigned L_KVT = 135168;
constexpr unsigned P3_SLOT = 36864;

DI void bar() { asm volatile("s_waitcnt lgkmcnt(0)" ::: "memory"); __builtin_amdgcn_s_barrier(); asm volatile("" ::: "memory"); }

struct Side { u32x4 r0, r1; };
DI void side_issue(const Params& P, Side& S, int row, int w) {
  const int l = threadIdx.x & 63;
  const u32x4* src = (const u32x4*)(P.x + (size_t)row * D_);
  S.r0 = src[l]; S.r1 = src[64 + (l & 15)];
}
DI float rdl(float v, int lane) { return __builtin_bit_cast(float, __builtin_amdgcn_readlane(__builtin_bit_cast(int, v), lane)); }
DI void side_finish(const Params& P, rsrc_t rws, const Side& S, int row, int w, unsigned tl, unsigned th) {
  const int l = threadIdx.x & 63;
  float s = 0.f, q = 0.f, s2 = 0.f, q2 = 0.f;
#pragma unroll
  for (int j = 0; j < 4; ++j) {
    float a = lo16(S.r0[j]), b = hi16(S.r0[j]), c = lo16(S.r1[j]), d = hi16(S.r1[j]);
    s += a + b; q += a * a + b * b; s2 += c + d; q2 += c * c + d * d;
  }
  s = sum16(s); q = sum16(q); s2 = sum16(s2); q2 = sum16(q2);
  s = (rdl(s, 0) + rdl(s, 16)) + (rdl(s, 32) + rdl(s, 48)) + s2;
  q = (rdl(q, 0) + rdl(q, 16)) + (rdl(q, 32) + rdl(q, 48)) + q2;
  const float m = s * (1.f / D_);
  const float v = rbf(fmaxf(q * (1.f / D_) - m * m, 0.f));
  const float r = 1.f / rbf(sqrtf(rbf(v + 1e-5f)));
  if (l < 2) st8u(P.ws, WS_GX + row * 16 + l * 8, l == 0 ? (((u64)tl << 32) | __float_as_uint(-rbf(m) * r)) : (((u64)th << 32) | __float_as_uint(r)));
}
DI void wf_job(const Params& P, rsrc_t rws, int w) {
  const int l = threadIdx.x & 63;
  int item = blockIdx.x; if (item > 219) item = 219;
  const int st = item / 5, T = (item - st * 5) * 8 + w, cb = T / 5, nt = T - cb * 5;
  const u16* wsrc = (st < 20 ? P.wo + (size_t)st * 32 * D_ : P.wout + (size_t)(st - 20) * 32 * D_) + (size_t)(l >> 4) * 4 * D_ + T * 16 + (l & 15);
  unsigned wv[8];
#pragma unroll
  for (int e = 0; e < 8; ++e) wv[e] = wsrc[(size_t)(16 * (e >> 2) + (e & 3)) * D_];
  st16(rws, WS_WF + (unsigned)(((cb * 44 + st) * 5 + nt) * 1024) + l * 16,
       (u32x4){wv[0] | (wv[1] << 16), wv[2] | (wv[3] << 16), wv[4] | (wv[5] << 16), wv[6] | (wv[7] << 16)});
}
DI void xstat_gather(const char* ws, int panel, unsigned L_ST, int tid, unsigned tg, u64 gv) {
  if (tid < 256) {
#pragma unroll 1
    for (unsigned sp = 0; sp < SPIN_CAP; ++sp) {
      if (__all((unsigned)(gv >> 32) == tg)) break;
      __builtin_amdgcn_s_sleep(1);
      gv = ld8(ws, WS_GX + panel * 2048 + tid * 8);
    }
    stsf(L_ST + (tid & 1) * 512 + (tid >> 1) * 4, __uint_as_float((unsigned)gv));
  }
}

DI void csum_issue(const char* ws, int blk, int t, u64 (&cv)[10]) {
#pragma unroll
  for (int k = 0; k < 10; ++k) cv[k] = ld8(ws, WS_GC + (blk * 10 + k) * 2048 + t * 8);
}
DI void csum_gather(const char* ws, int blk, int t, unsigned tl, u64 (&cv)[10], unsigned dst) {
#pragma unroll 1
  for (unsigned sp = 0; sp < SPIN_CAP; ++sp) {
    bool ok = true;
#pragma unroll
    for (int k = 0; k < 10; ++k) ok &= (unsigned)(cv[k] >> 32) == tl;
    if (__all(ok)) break;
    __builtin_amdgcn_s_sleep(1);
    csum_issue(ws, blk, t, cv);
  }
  float a = 0.f;
#pragma unroll
  for (int k = 0; k < 10; ++k) a += __uint_as_float((unsigned)cv[k]);
  stsf(dst + t * 4, a);
}
DI void csum_store(char* ws, unsigned off, f32x4 d, int fr, int g, unsigned tl) {
  const float v = fr == 0 ? d[0] : fr == 1 ? d[1] : fr == 2 ? d[2] : d[3];
  if (fr < 4) st8u(ws, off + (4 * g + fr) * 8, ((u64)tl << 32) | __float_as_uint(v));
}

DI void p1q_consumer(const Params& P, rsrc_t rws, int w, int panel, int cbi, unsigned lds0, u64 tag, bf16x8 (&pf)[3]) {
  char* ws = P.ws;
  const int tid = threadIdx.x, l = tid & 63;
  const int wm = w >> 2, wc = (w + 2 * wm) & 3;
  const int g = l >> 4, fr = l & 15, q = (l & 15) >> 2, p = l & 3, g1 = g & 1;
  const unsigned aoff0 = (unsigned)((wm * 64 + fr) * 128 + ((g ^ (l & 7)) << 4));
  const unsigned ttab = (unsigned)(0x4498E765DC32BA10ull >> (16 * wc)) & 0xffffu;
  int tile[4];
  unsigned bo[4];
#pragma unroll
  for (int n = 0; n < 4; ++n) {
    tile[n] = (ttab >> (4 * n)) & 15;
    bo[n] = 16384u + g * KBS + q * 480 + p * 8 + tile[n] * 32;
  }
  const unsigned tl = (unsigned)tag, th = tl * 0x9E3779B1u ^ (unsigned)(tag >> 32);
  {
    Side S; side_issue(P, S, panel * 128 + cbi * 8 + w, w);
    side_finish(P, rws, S, panel * 128 + cbi * 8 + w, w, tl, th);
  }
  f32x4 acc[4][4];
#pragma unroll
  for (int m = 0; m < 4; ++m)
#pragma unroll
    for (int n = 0; n < 4; ++n) acc[m][n] = (f32x4){0.f, 0.f, 0.f, 0.f};
  const short one = 0x3F80;
  const bf16x8 ones = {one, one, one, one, one, one, one, one};
  const bf16x8 zeros = {0, 0, 0, 0, 0, 0, 0, 0};

  bf16x8 af[2][4], bfr[2][4];
#pragma unroll
  for (int m = 0; m < 4; ++m) { af[1][m] = zeros; bfr[1][m] = zeros; }
  auto mm = [&](int ks) {
#pragma unroll
    for (int n = 0; n < 3; ++n)
#pragma unroll
      for (int m = 0; m < 4; ++m) acc[m][n] = MFMA(bfr[ks][n], af[ks][m], acc[m][n]);
    if (wc < 3) {
#pragma unroll
      for (int m = 0; m < 4; ++m) acc[m][3] = MFMA(bfr[ks][3], af[ks][m], acc[m][3]);
    }
  };
  auto step = [&](unsigned sb, int ks) {
#pragma unroll
    for (int n = 0; n < 4; ++n) bfr[ks][n] = lds_tr(sb + ks * 4 * KBS + bo[n], sb + ks * 4 * KBS + bo[n] + 1920);
#pragma unroll
    for (int m = 0; m < 4; ++m) af[ks][m] = lds_b128(sb + ((aoff0 ^ (ks * 64)) + m * 2048));
    SB0;
    mm(ks ^ 1);
    SB0;
  };
  unsigned sb = lds0;
  auto run = [&](int n) {
#pragma unroll 1
    for (int kt = 0; kt < n; ++kt) {
      bar();
      step(sb, 0);
      step(sb, 1);
      sb += K1_STAGE; if (sb == lds0 + 3 * K1_STAGE) sb = lds0;
    }
  };
  run(1);
  if (panel < 10) {
#pragma unroll
    for (int h = 0; h < 2; ++h) {
      const bf16x8 b0 = wm == 0 ? bfr[0][h] : bfr[0][2 + h], b1 = wm == 0 ? bfr[1][h] : bfr[1][2 + h];
      const int tt = wm == 0 ? tile[h] : tile[2 + h];
      f32x4 d = MFMA(b0, ones, ((f32x4){0.f, 0.f, 0.f, 0.f}));
      d = MFMA(b1, ones, d);
      csum_store(ws, WS_GC + (cbi * 10 + panel) * 2048 + tt * 128, d, fr, g, tl);
    }
  }
  run(9);
  mm(1);
  STAMP(1);
  bar();
  STAMP(10);
  const unsigned L_ST = lds0 + L_MISC, L_CS = lds0 + L_MISC + 5120;
  const unsigned L_K = lds0, L_V = L_K + 20480, L_Q = lds0 + 122880;
  float mr[4], rinv[4];
#pragma unroll
  for (int m = 0; m < 4; ++m) { const int rl = wm * 64 + m * 16 + fr; mr[m] = ldsf(L_ST + rl * 4); rinv[m] = ldsf(L_ST + 512 + rl * 4); }
#pragma unroll
  for (int n = 0; n < 4; ++n) {
    if (n < 3 || wc < 3) {
      const int t = tile[n];
      const f32x4 cs = ldsf4(L_CS + (t * 16 + 4 * g) * 4);
      unsigned dst, dstep;
      if (t < 5) { dst = L_Q + (wm * 12 + (t >> 1)) * 1024 + l * 16 + (t & 1) * 8; dstep = 3072; }
      else {
        const int kb = wm * 8 + (fr >> 3), pos = (fr & 7) ^ ((fr >> 3) << 2);
        dst = (t < 10 ? L_K + (t - 5) * 256 : L_V + (t - 10) * 256) + kb * 1280 + pos * 32 + g * 8; dstep = 2560;
      }
      if (t < 10) {
#pragma unroll
        for (int m = 0; m < 4; ++m) {
          f32x4 v = acc[m][n] * rinv[m] + cs * mr[m];
          v = phi4(v);
          *(LAS u32x2*)(uintptr_t)(dst + m * dstep) = (u32x2){cvt_pk(v[0], v[1]), cvt_pk(v[2], v[3])};
        }
      } else {
#pragma unroll
        for (int m = 0; m < 4; ++m) {
          f32x4 v = acc[m][n] * rinv[m] + cs * mr[m];
          *(LAS u32x2*)(uintptr_t)(dst + m * dstep) = (u32x2){cvt_pk(v[0], v[1]), cvt_pk(v[2], v[3])};
        }
      }
    }
  }
  STAMP(8);
  bar();
  {
    const unsigned troff = (unsigned)(g * 5 * 256 + ((4 * g1 + q) * 32) + p * 8);
    f32x4 d[3];
    int jb[3];
#pragma unroll
    for (int r = 0; r < 3; ++r) {
      int job = w + 8 * r; if (job >= 18) job = w;
      jb[r] = job;
      const int i = job < 13 ? (job + 1) / 3 : job - 13;
      const int j = job < 13 ? job - 2 * i : 0;
      d[r] = (f32x4){0.f, 0.f, 0.f, 0.f};
#pragma unroll
      for (int ks = 0; ks < 4; ++ks) {
        const unsigned a0 = L_K + troff + (ks * 20 + i) * 256, b0 = L_V + troff + (ks * 20 + j) * 256;
        const bf16x8 ka = lds_tr(a0, a0 ^ 128);
        bf16x8 vb = lds_tr(b0, b0 ^ 128);
        if (job >= 13) vb = ones;
        d[r] = MFMA(ka, vb, d[r]);
      }
    }
#pragma unroll
    for (int r = 0; r < 3; ++r)
      st16(rws, WS_KVP + (unsigned)(((cbi * 18 + jb[r]) * 16 + panel) * 1024) + l * 16, __builtin_bit_cast(u32x4, d[r]));
    pf[0] = lds_b128(L_Q + w * 3072 + l * 16); pf[1] = lds_b128(L_Q + w * 3072 + 1024 + l * 16);
    const u32x2 q2 = *(LAS u32x2*)(uintptr_t)(L_Q + w * 3072 + 2048 + l * 16);
    pf[2] = __builtin_bit_cast(bf16x8, ((u32x4){q2[0], q2[1], 0u, 0u}));
  }
  STAMP(9);
  wait_vm<0>();
  bar();
  if (tid == 0) st8u(ws, WS_F1 + (panel * 32 + cbi) * 64, tag);
}

DI void p1z_consumer(const Params& P, rsrc_t rws, int w, int panel, int cbj, unsigned lds0, u64 tag) {
  char* ws = P.ws;
  const int tid = threadIdx.x, l = tid & 63;
  const int wm = w & 3, wn = w >> 2;
  const int g = l >> 4, fr = l & 15, q = (l & 15) >> 2, p = l & 3, g1 = g & 1;
  const unsigned aoff0 = (unsigned)((wm * 32 + fr) * 128 + ((g ^ (l & 7)) << 4));
  unsigned bo[3];
  int ca[3];
#pragma unroll
  for (int n = 0; n < 3; ++n) {
    const int c = wn * 3 + n;
    ca[n] = c;
    int pc = c + g1; if (pc == 6) pc = 0;
    bo[n] = 16384u + g * 1536 + q * 192 + pc * 32 + p * 8;
  }
  const short one = 0x3F80;
  const bf16x8 ones = {one, one, one, one, one, one, one, one};
  const bf16x8 zeros = {0, 0, 0, 0, 0, 0, 0, 0};
  f32x4 accA[2][3], accB[2][3];
#pragma unroll
  for (int m = 0; m < 2; ++m)
#pragma unroll
    for (int n = 0; n < 3; ++n) { accA[m][n] = (f32x4){0.f, 0.f, 0.f, 0.f}; accB[m][n] = (f32x4){0.f, 0.f, 0.f, 0.f}; }
  const unsigned tl = (unsigned)tag, th = tl * 0x9E3779B1u ^ (unsigned)(tag >> 32), tg = (tid & 1) ? th : tl;
  bf16x8 af[2][2][2], bfr[2][2][3];
  unsigned sb = lds0;
  auto rd = [&](int c) {
    bar();
#pragma unroll
    for (int ks = 0; ks < 2; ++ks) {
#pragma unroll
      for (int m = 0; m < 2; ++m) af[c][ks][m] = lds_b128(sb + ((aoff0 ^ (ks * 64)) + m * 2048));
#pragma unroll
      for (int n = 0; n < 3; ++n) { const unsigned o = sb + ks * 4 * 1536 + bo[n]; bfr[c][ks][n] = lds_tr(o, o + 768); }
    }
    SB0;
    sb += KZ_STAGE; if (sb == lds0 + 5 * KZ_STAGE) sb = lds0;
  };
  auto mmA = [&](int c) {
#pragma unroll
    for (int ks = 0; ks < 2; ++ks)
#pragma unroll
      for (int n = 0; n < 3; ++n)
#pragma unroll
        for (int m = 0; m < 2; ++m) accA[m][n] = MFMA(af[c][ks][m], bfr[c][ks][n], accA[m][n]);
    SB0;
  };
  auto mmB = [&](int c) {
#pragma unroll
    for (int ks = 0; ks < 2; ++ks)
#pragma unroll
      for (int n = 0; n < 3; ++n)
#pragma unroll
        for (int m = 0; m < 2; ++m) accB[m][n] = MFMA(bfr[c][ks][n], af[c][ks][m], accB[m][n]);
    SB0;
  };
  const unsigned L_ST = lds0 + L_MISC, L_CS = lds0 + L_MISC + 5120;
  const unsigned L_RS = lds0 + L_MISC + 2048;
  const unsigned L_FS = lds0 + L_MISC + 4096;
  auto cpub = [&](int blk) {
    if (panel < 10 && wm == 0) {
#pragma unroll
      for (int n = 0; n < 3; ++n) {
        f32x4 d = MFMA(bfr[0][0][n], ones, ((f32x4){0.f, 0.f, 0.f, 0.f}));
        d = MFMA(bfr[0][1][n], ones, d);
        csum_store(ws, WS_GC + (blk * 10 + panel) * 2048 + ca[n] * 128, d, fr, g, tl);
      }
    }
  };
  {
    Side S; side_issue(P, S, panel * 128 + 64 + cbj * 8 + w, w);
    side_finish(P, rws, S, panel * 128 + 64 + cbj * 8 + w, w, tl, th);
  }
  rd(0);
  cpub(8 + cbj);
#pragma unroll 1
  for (int i = 0; i < 3; ++i) { rd(1); mmA(0); rd(0); mmA(1); }
  const int tc = tid - 256;
  {
    u64 cv[10];
    if (tid < 256) cv[0] = ld8(ws, WS_GX + panel * 2048 + tid * 8);
    else if (tc < 96) csum_issue(ws, 8 + cbj, tc, cv);
    rd(1); mmA(0); rd(0); mmA(1); rd(1); mmA(0);
    if (tid < 256) xstat_gather(ws, panel, L_ST, tid, tg, cv[0]);
    else if (tc < 96) csum_gather(ws, 8 + cbj, tc, tl, cv, L_CS);
  }
  rd(0); mmA(1);
  cpub(16 + cbj);
  STAMP(8);
  {
    float cn[3];
#pragma unroll
    for (int n = 0; n < 3; ++n) cn[n] = ldsf(L_CS + (ca[n] * 16 + fr) * 4);
#pragma unroll
    for (int m = 0; m < 2; ++m) {
      const int rl = wm * 32 + m * 16 + 4 * g;
      const f32x4 mr = ldsf4(L_ST + rl * 4), rinv = ldsf4(L_ST + 512 + rl * 4);
      f32x4 rs = {0.f, 0.f, 0.f, 0.f}, rq = {0.f, 0.f, 0.f, 0.f};
#pragma unroll
      for (int n = 0; n < 3; ++n) {
        f32x4 v = accA[m][n] * rinv + mr * cn[n];
        v = gelu4(v);
        rs += v; rq += v * v;
        accA[m][n] = v;
      }
#pragma unroll
      for (int j = 0; j < 4; ++j) { rs[j] = sum16(rs[j]); rq[j] = sum16(rq[j]); }
      if (fr == 0) {
        stsf4(L_RS + ((wn * 128 + rl) * 2) * 4, (f32x4){rs[0], rq[0], rs[1], rq[1]});
        stsf4(L_RS + ((wn * 128 + rl) * 2 + 4) * 4, (f32x4){rs[2], rq[2], rs[3], rq[3]});
      }
    }
  }
  rd(1);
  float part = 0.f;
  const unsigned gbase = WS_GR + (unsigned)(panel * 8) * 2048 + tid * 8;
  if (tid < 256) {
    part = ldsf(L_RS + tid * 4) + ldsf(L_RS + 1024 + tid * 4);
    st8u(ws, gbase + cbj * 2048, ((u64)tg << 32) | __float_as_uint(part));
  }
  mmB(0);
  STAMP(9);
  rd(0); mmB(1);
  {
    u64 cv[10];
    if (tid < 256) {
#pragma unroll
      for (int i = 1; i < 8; ++i) cv[i] = ld8(ws, gbase + ((cbj + i) & 7) * 2048);
    }
    rd(1); mmB(0);
    if (tid < 256) {
      float tot;
#pragma unroll 1
      for (unsigned sp = 0; sp < SPIN_CAP; ++sp) {
        bool ok = true; tot = part;
#pragma unroll
        for (int i = 1; i < 8; ++i) { ok &= (unsigned)(cv[i] >> 32) == tg; tot += __uint_as_float((unsigned)cv[i]); }
        if (__all(ok)) break;
#pragma unroll
        for (int i = 1; i < 8; ++i) cv[i] = ld8(ws, gbase + ((cbj + i) & 7) * 2048);
      }
      float other = dpp<0xB1>(tot);
      if ((tid & 1) == 0) {
        float m = tot * (1.f / FH);
        float v = rbf(fmaxf(other * (1.f / FH) - m * m, 0.f));
        float r = 1.f / rbf(sqrtf(rbf(v + 1e-5f)));
        stsf(L_FS + (tid >> 1) * 4, -rbf(m) * r);
        stsf(L_FS + 512 + (tid >> 1) * 4, r);
      }
    }
  }
  rd(0);
  {
    f32x4 mr[2], ri[2];
#pragma unroll
    for (int m = 0; m < 2; ++m) { const int rl = wm * 32 + m * 16 + 4 * g; mr[m] = ldsf4(L_FS + rl * 4); ri[m] = ldsf4(L_FS + 512 + rl * 4); }
#pragma unroll
    for (int n = 0; n < 3; ++n) {
      f32x4 a = accA[0][n] * ri[0] + mr[0], b = accA[1][n] * ri[1] + mr[1];
      st16(rws, WS_Z2N + (unsigned)(((cbj * 64 + panel * 4 + wm) * 6 + ca[n]) * 1024) + l * 16,
           (u32x4){cvt_pk(a[0], a[1]), cvt_pk(a[2], a[3]), cvt_pk(b[0], b[1]), cvt_pk(b[2], b[3])});
    }
  }
  mmB(1);
  STAMP(10);
  rd(1); mmB(0); rd(0); mmB(1);
  wait_vm<0>();
  rd(1);
  if (tid == 0) st8u(ws, WS_F1 + (panel * 32 + 8 + cbj) * 64, tag);
  STAMP(11);
  mmB(0);
  {
    u64 cv[10];
    if (tid >= 256 && tc < 96) csum_issue(ws, 16 + cbj, tc, cv);
    rd(0); mmB(1); rd(1); mmB(0);
    if (tid >= 256 && tc < 96) csum_gather(ws, 16 + cbj, tc, tl, cv, L_CS + 512);
  }
  mmB(1);
  STAMP(1);
  bar();
  {
    f32x4 cn[3];
#pragma unroll
    for (int n = 0; n < 3; ++n) { const unsigned a = L_CS + 512 + (ca[n] * 16 + 4 * g) * 4; cn[n] = ldsf4(a); }
    float mr[2], rinv[2];
#pragma unroll
    for (int m = 0; m < 2; ++m) { const int rl = wm * 32 + m * 16 + fr; mr[m] = ldsf(L_ST + rl * 4); rinv[m] = ldsf(L_ST + 512 + rl * 4); }
#pragma unroll
    for (int n = 0; n < 3; ++n) {
      u32x4 o;
#pragma unroll
      for (int m = 0; m < 2; ++m) {
        f32x4 v = accB[m][n] * rinv[m] + cn[n] * mr[m];
        v = gelu4(v);
        o[2 * m] = cvt_pk(v[0], v[1]); o[2 * m + 1] = cvt_pk(v[2], v[3]);
      }
      if (wm < 2) *(LAS u32x4*)(uintptr_t)(lds0 + L_KVT + 6144 + (wm * 6 + ca[n]) * 1024 + l * 16) = o;
      else st16(rws, WS_Z1 + (unsigned)(((panel * 8 + cbj) * 4 + wm) * 6 + ca[n]) * 1024 + l * 16, o);
    }
  }
}

__global__ __launch_bounds__(768, 3) void mega_kernel(Params P) {
  extern __shared__ __attribute__((aligned(16))) char smem[];
  const unsigned lds0 = (unsigned)(uintptr_t)(LAS char*)smem;
  char* ws = P.ws;
  const int tid = threadIdx.x, l = tid & 63;
  const int w = __builtin_amdgcn_readfirstlane(tid >> 6);
  const int g = l >> 4, fr = l & 15;
  const int b = blockIdx.x, xcd = b & 7, bi = b >> 3;
  const int panel1 = (xcd >> 1) * 4 + (bi & 3), cb1 = (xcd & 1) * 4 + ((bi >> 2) & 3);
  const bool isq = bi < 16;
  const int panel2 = 2 * panel1 + (isq ? 1 : 0), cb2 = cb1;
  const int panel3 = xcd * 4 + (bi & 3), cb3 = bi >> 2;
  STAMP(0);
  const u64 tag = ld8(ws, WS_EP) + 1;
  if (w == 7 && bi < 20) {
    u64 pc; asm volatile("s_getpc_b64 %0" : "=s"(pc));
    gl2((const char*)(pc & ~127ull) + bi * 2048, lds0 + 3 * K1_STAGE, l * 16, l * 16);
  }

  if (w >= 8) {
    const int lw = w - 8;
    const unsigned vA0 = (unsigned)(((l >> 3) * D_ + (((l & 7) ^ (l >> 3)) << 3)) * 2);
    const unsigned vA1 = vA0 + 8 * D_ * 2 - 1024, vA2 = vA0 + 16 * D_ * 2 - 2048, vA3 = vA0 + 24 * D_ * 2 - 3072;
    const char* gA = (const char*)(P.x + (size_t)(panel1 * 128 + lw * 32) * D_);
    const unsigned v20 = (unsigned)(((l >> 3) * S_ + (((l & 7) ^ (l >> 4)) << 3)) * 2);
    const unsigned v21 = (unsigned)((((l >> 3) + 8) * S_ + (((l & 7) ^ ((l >> 4) | 4)) << 3)) * 2) - 1024;
    const char* gA2 = (const char*)(P.wsp + (size_t)(panel2 * 64 + lw * 16) * S_);
    const int rot2 = 0;
    auto ldA = [&](int kt, unsigned sl) { gl2(gA2 + ((kt + rot2) & 31) * 128, sl + lw * 2048, v20, v21); };
    auto sl2 = [&](int kt) { return lds0 + ((kt + 4) % 6) * P2_SLOT; };
    const unsigned tl = (unsigned)tag, th = tl * 0x9E3779B1u ^ (unsigned)(tag >> 32);
    const int lt = tid - 512;
    auto gather = [&](int blk, int ncol, unsigned dst, bool xs) {
      u64 cv[10]; u64 gv = 0;
      const int tc = lt < ncol ? lt : 0;
      if (xs) gv = ld8(ws, WS_GX + panel1 * 2048 + lt * 8);
      csum_issue(ws, blk, tc, cv);
      if (xs) xstat_gather(ws, panel1, lds0 + L_MISC, lt, (lt & 1) ? th : tl, gv);
      csum_gather(ws, blk, tc, tl, cv, dst);
    };
    const unsigned l16 = l * 16;
    const char* gB2 = ws + WS_Z2N + (size_t)cb2 * 64 * 6144 + lw * 3072;
    auto ldB = [&](int kt, unsigned sl) { gl3c(gB2 + ((kt + rot2) & 31) * 12288, sl + 8192 + lw * 3072, l16); };
    const unsigned zfl = WS_F1 + ((l & 15) * 32 + 8 + cb2) * 64;
    auto try_z = [&]() {
      const u64 v = l < 16 ? ld8(ws, zfl) : tag;
      return (bool)__builtin_amdgcn_readfirstlane((int)__all(v == tag));
    };
    bool early = false;
    const int rot = panel1 < 10 ? panel1 : 0;
    auto krot = [&](int kt) { const int k = kt >= 10 ? kt - 10 : kt; return __builtin_amdgcn_readfirstlane(k == 0 ? rot : (k - 1 < rot ? k - 1 : k)); };
    if (isq) {
      gl4(gA + rot * 128, lds0 + lw * 4096, vA0, vA1, vA2, vA3);
      unsigned vB[4];
#pragma unroll
      for (int i = 0; i < 4; ++i) {
        int c = i * 64 + l; if (c > 239) c = 239;
        const int row = c / 30, cc = c - row * 30, seg = cc / 10, ch = cc - seg * 10;
        vB[i] = (unsigned)((row * NQKV + seg * 640 + cb1 * 80 + ch * 8) * 2) + 4096 - i * 1024;
      }
      const char* gB = (const char*)P.wqkv + (size_t)lw * 16 * NQKV * 2 - 4096;
      auto stageB = [&](int kt, unsigned sb) {
        const char* bp = gB + (size_t)krot(kt) * 64 * NQKV * 2;
        gl4(bp, sb + 16384 + lw * 2 * KBS, vB[0], vB[1], vB[2], vB[3]);
        gl4(bp + 8 * NQKV * 2, sb + 16384 + (lw * 2 + 1) * KBS, vB[0], vB[1], vB[2], vB[3]);
      };
      auto stage = [&](int kt, unsigned sb) { gl4(gA + krot(kt) * 128, sb + lw * 4096, vA0, vA1, vA2, vA3); stageB(kt, sb); };
      stageB(0, lds0); stage(1, lds0 + K1_STAGE);
      wait_vm<12>(); bar();
      stage(2, lds0 + 2 * K1_STAGE);
      unsigned sb = lds0, sc = lds0 + K1_STAGE;
#pragma unroll 1
      for (int kt = 1; kt < 8; ++kt) {
        wait_vm<12>(); bar();
        if (kt == 2) STAMP(10);
        if (kt == 7) STAMP(11);
        stage(kt + 2, sb);
        sb = sc; sc += K1_STAGE; if (sc == lds0 + 3 * K1_STAGE) sc = lds0;
      }
      wait_vm<12>(); bar();
      gather(cb1, 240, lds0 + L_MISC + 5120, true);
      bar();
      ldA(0, sl2(0)); ldA(1, sl2(1)); ldA(5, sl2(5));
      bar();
      ldA(4, sl2(4));
#pragma unroll 1
      for (int t = 0; t < 3 && !early; ++t) early = try_z();
      if (early) { ldB(0, sl2(0)); ldB(1, sl2(1)); ldB(4, sl2(4)); ldB(5, sl2(5)); }
      bar();
      if (!early) { early = try_z(); if (early) { ldB(0, sl2(0)); ldB(1, sl2(1)); ldB(4, sl2(4)); ldB(5, sl2(5)); } }
      bar();
    } else {
      gl4(gA + rot * 128, lds0 + lw * 4096, vA0, vA1, vA2, vA3);
      unsigned vB[3];
#pragma unroll
      for (int i = 0; i < 3; ++i) {
        const int c = i * 64 + l, row = c / 12, cc = c - row * 12;
        int pc = (cc >> 1) - (row >> 3); if (pc < 0) pc = 5;
        vB[i] = (unsigned)((row * FIN + cb1 * 96 + pc * 16 + (cc & 1) * 8) * 2) + 4096 - i * 1024;
      }
      const char* gB = (const char*)P.win + (size_t)lw * 16 * FIN * 2 - 4096;
      auto stageB = [&](int kt, unsigned sb) {
        const int k = krot(kt);
        gl3(gB + (kt < 10 ? FH * 2 : 0) + (size_t)k * 64 * FIN * 2, sb + 16384 + lw * 3072, vB[0], vB[1], vB[2]);
      };
      auto stage = [&](int kt, unsigned sb) {
        const int k = krot(kt);
        gl4(gA + k * 128, sb + lw * 4096, vA0, vA1, vA2, vA3); stageB(kt, sb);
      };
      stageB(0, lds0); stage(1, lds0 + KZ_STAGE); stage(2, lds0 + 2 * KZ_STAGE);
      wait_vm<14>(); bar();
      stage(3, lds0 + 3 * KZ_STAGE); stage(4, lds0 + 4 * KZ_STAGE);
      unsigned sb = lds0;
      auto runL = [&](int k0, int k1) {
#pragma unroll 1
        for (int kt = k0; kt < k1; ++kt) {
          wait_vm<21>(); bar();
          stage(kt + 4, sb);
          sb += KZ_STAGE; if (sb == lds0 + 5 * KZ_STAGE) sb = lds0;
        }
      };
      runL(1, 11);
      {
        const char* wp = (const char*)(P.wsp + (size_t)(panel1 * 128 + lw * 32) * S_) + (cb1 & 3) * 1024;
        const unsigned vo = (unsigned)((l >> 3) * S_ * 2 + (l & 7) * 128);
        unsigned d0, d1, d2, d3;
        asm volatile("global_load_dword %0, %4, %5\n\tglobal_load_dword %1, %4, %6\n\tglobal_load_dword %2, %4, %7\n\tglobal_load_dword %3, %4, %8"
                     : "=&v"(d0), "=&v"(d1), "=&v"(d2), "=&v"(d3)
                     : "v"(vo), "s"(wp), "s"(wp + 8 * S_ * 2), "s"(wp + 16 * S_ * 2), "s"(wp + 24 * S_ * 2) : "memory");
        runL(11, 16);
        asm volatile("" :: "v"(d0), "v"(d1), "v"(d2), "v"(d3));
      }
      wait_vm<21>(); bar(); wait_vm<14>(); bar(); wait_vm<7>(); bar(); wait_vm<0>(); bar();
      bar();
      wait_words(ws, zfl, l < 16, tag);
      early = true;
      ldA(0, sl2(0)); ldB(0, sl2(0)); ldA(1, sl2(1)); ldB(1, sl2(1));
      ldA(4, sl2(4)); ldA(5, sl2(5)); ldB(4, sl2(4)); ldB(5, sl2(5));
    }
    STAMP(2);
    if (!early) {
      wait_words(ws, zfl, l < 16, tag);
      ldB(0, sl2(0)); ldB(1, sl2(1)); ldB(4, sl2(4)); ldB(5, sl2(5));
    }
    STAMP(3);
    ldA(2, sl2(2)); ldA(3, sl2(3)); ldB(2, sl2(2)); ldB(3, sl2(3));
    wait_vm<16>(); bar();
    {
      unsigned sl = sl2(0);
      auto ldU = [&](int u) {
        ldA(2 * u, sl); ldB(2 * u, sl); ldA(2 * u + 1, sl + P2_SLOT); ldB(2 * u + 1, sl + P2_SLOT);
        sl += 2 * P2_SLOT; if (sl == lds0 + 6 * P2_SLOT) sl = lds0;
      };
      wait_vm<0>(); bar(); ldU(3);
#pragma unroll 1
      for (int u = 2; u < 14; ++u) {
        wait_vm<10>(); bar();
        if (u == 4) STAMP(8);
        if (u == 10) STAMP(9);
        ldU(u + 2);
      }
    }
    wait_vm<10>(); bar();
    wait_words(ws, WS_F1 + (((lw * 64 + l) >> 4) * 32 + 16 + (l & 15)) * 64, true, tag);
    wait_vm<0>(); bar();
    if (b == 0 && tid == 512) st8u(ws, WS_EP, tag);
    STAMP(4);
    const char* gA3 = ws + WS_OH + (size_t)panel3 * 44 * 4096 + lw * 4096;
    const char* gB3 = ws + WS_WF + (size_t)cb3 * 44 * 5120 + lw * 5120;
    int r5 = cb3 + 2 * (bi & 3); r5 = r5 >= 10 ? r5 - 10 : r5 >= 5 ? r5 - 5 : r5;
    int r6 = cb3 + 2 * (bi & 3); r6 = r6 >= 12 ? r6 - 12 : r6 >= 6 ? r6 - 6 : r6;
    auto ur = [&](int u) {
      int v;
      if (u < 5) { v = u + r5; if (v >= 5) v -= 5; } else { v = u + r6; if (v >= 11) v -= 6; }
      return __builtin_amdgcn_readfirstlane(v);
    };
    auto ldB3 = [&](int u, unsigned sl) {
      gl4c(gB3 + ur(u) * 20480, sl + 16384 + lw * 5120, l16);
      glds16c(gB3 + ur(u) * 20480 + 4096, l16, sl + 16384 + lw * 5120 + 4096);
    };
    auto ldA3 = [&](int u, unsigned sl) { gl4c(gA3 + ur(u) * 16384, sl + lw * 4096, l16); };
    bar(); bar();
    ldB3(0, lds0); ldB3(1, lds0 + P3_SLOT); ldB3(2, lds0 + 2 * P3_SLOT);
    wait_words(ws, WS_FO + ((panel3 >> 1) * 8 + (l & 7)) * 64, l < 8, tag);
    ldA3(0, lds0); ldA3(1, lds0 + P3_SLOT); ldA3(2, lds0 + 2 * P3_SLOT);
    bar();
    ldA3(3, lds0 + 3 * P3_SLOT); ldB3(3, lds0 + 3 * P3_SLOT);
    wait_vm<17>(); bar();
    wait_vm<13>(); bar(); ldA3(4, lds0); ldB3(4, lds0);
    wait_vm<18>(); bar(); ldB3(5, lds0 + P3_SLOT);
    wait_vm<14>(); bar(); ldB3(6, lds0 + 2 * P3_SLOT);
    wait_vm<10>(); bar(); ldB3(7, lds0 + 3 * P3_SLOT);
    wait_words(ws, WS_F2 + (panel3 * 32 + (l & 31)) * 64, l < 32, tag);
    STAMP(5);
    ldA3(5, lds0 + P3_SLOT); ldA3(6, lds0 + 2 * P3_SLOT); ldA3(7, lds0 + 3 * P3_SLOT);
    wait_vm<8>(); bar(); ldA3(8, lds0); ldB3(8, lds0);
    wait_vm<13>(); bar(); ldA3(9, lds0 + P3_SLOT); ldB3(9, lds0 + P3_SLOT);
    wait_vm<18>(); bar(); ldA3(10, lds0 + 2 * P3_SLOT); ldB3(10, lds0 + 2 * P3_SLOT);
    wait_vm<18>(); bar(); wait_vm<9>(); bar(); wait_vm<0>(); bar();
    bar(); bar();
    STAMP(6);
    return;
  }

  const rsrc_t rws = __builtin_amdgcn_make_buffer_rsrc(ws, 0, (int)WS_END, 0x00020000);
  bf16x8 pf[3];
  if (isq) p1q_consumer(P, rws, w, panel1, cb1, lds0, tag, pf);
  else p1z_consumer(P, rws, w, panel1, cb1, lds0, tag);
  STAMP(2);
  wf_job(P, rws, w);

  u32x4 xv[2];
  unsigned xo0, xo1, la0, la1;
  {
    const int wt = w & 3, kh = w >> 2;
    const int rsw = ((wt * 16 + fr) >> 1) & 7;
    const unsigned ao0 = (unsigned)((wt * 16 + fr) * 128 + (((4 * kh + (g >> 1)) ^ rsw) << 4) + (g & 1) * 8);
    const unsigned ao1 = (unsigned)((wt * 16 + fr) * 128 + (((4 * kh + 2 + (g >> 1)) ^ rsw) << 4) + (g & 1) * 8);
    const unsigned bo = (unsigned)(8192 + kh * 6144 + l * 16);
    const bf16x8 zeros = {0, 0, 0, 0, 0, 0, 0, 0};
    f32x4 acc[6];
#pragma unroll
    for (int n = 0; n < 6; ++n) acc[n] = (f32x4){0.f, 0.f, 0.f, 0.f};
    bf16x8 af[2], bfr[2][6];
    af[1] = zeros;
#pragma unroll
    for (int n = 0; n < 6; ++n) bfr[1][n] = zeros;
    const int zi = (bi & 15) * 8 + xcd;
    const bool reducer = !isq && zi < 72;
    const int rj = zi & 7, rf = zi >> 3;
    const int row = panel2 * 64 + wt * 16 + fr;
    u32x2 zreg[6];
    const float bs = bf2f(P.bsp[row]);
    unsigned sl = lds0 + 4 * P2_SLOT;
    auto step = [&](int cur, int kt) {
      if (cur == 0) bar();
      af[cur] = lds_2b64(sl + ao0, sl + ao1);
#pragma unroll
      for (int n = 0; n < 6; ++n) bfr[cur][n] = lds_b128(sl + bo + n * 1024);
      SB0;
#pragma unroll
      for (int n = 0; n < 6; ++n) acc[n] = MFMA(bfr[cur ^ 1][n], af[cur ^ 1], acc[n]);
      SB0;
      sl += P2_SLOT; if (sl == lds0 + 6 * P2_SLOT) sl = lds0;
    };
    auto run = [&](int k0, int k1) {
#pragma unroll 1
      for (int kt = k0; kt < k1; kt += 2) { step(0, kt); step(1, kt + 1); }
    };
    if (reducer) {
      const int et = (rf + 1) >> 1, st = rf == 0 ? 0 : ((rf + 1) & 1) + (et >= 3 ? 1 : 0);
      wait_words(ws, WS_F1 + ((l & 15) * 32 + rj) * 64, true, tag);
      f32x4 pv[8];
      bool okv[4];
#pragma unroll
      for (int t4 = 0; t4 < 4; ++t4) {
        const int i = 2 * st + (t4 & 1);
        okv[t4] = i < 5 && (t4 >= 2 || (et - i <= 1 && i - et <= 1));
        const int job = okv[t4] ? (t4 >= 2 ? 13 + i : 2 * i + et) : 0;
#pragma unroll
        for (int pp = 0; pp < 2; ++pp)
          pv[t4 * 2 + pp] = __builtin_bit_cast(f32x4, ld16(rws, WS_KVP + (unsigned)(((rj * 18 + job) * 16 + w * 2 + pp) * 1024) + l * 16));
      }
      run(0, 2);
      if (tid == 0) st8u(ws, WS_F1 + (panel1 * 32 + 16 + cb1) * 64, tag);
      const unsigned L_R = lds0 + 122880;
      f32x4 sv[4];
#pragma unroll
      for (int t4 = 0; t4 < 4; ++t4) { sv[t4] = pv[t4 * 2] + pv[t4 * 2 + 1]; if (!okv[t4]) sv[t4] = (f32x4){0.f, 0.f, 0.f, 0.f}; }
      if (w >= 4) {
#pragma unroll
        for (int t4 = 0; t4 < 4; ++t4) stsf4(L_R + ((w - 4) * 4 + t4) * 1024 + l * 16, sv[t4]);
      }
      run(2, 4);
      if (w < 4) {
#pragma unroll
        for (int t4 = 0; t4 < 4; ++t4) stsf4(L_R + (w * 4 + t4) * 1024 + l * 16, sv[t4] + ldsf4(L_R + (w * 4 + t4) * 1024 + l * 16));
      }
      run(4, 6);
      if (w < 2) {
        f32x4 v[2];
#pragma unroll
        for (int h2 = 0; h2 < 2; ++h2) {
          v[h2] = (f32x4){0.f, 0.f, 0.f, 0.f};
#pragma unroll
          for (int ww = 0; ww < 4; ++ww) v[h2] += ldsf4(L_R + (ww * 4 + w * 2 + h2) * 1024 + l * 16);
          const int he = (et * 16 + fr) / 20;
#pragma unroll
          for (int r = 0; r < 4; ++r) { const int d = (2 * st + h2) * 16 + 4 * g + r; if (d / 20 != he) v[h2][r] = 0.f; }
        }
        st16(rws, WS_KVT + (unsigned)(((rj * 2 + w) * 9 + rf) * 1024) + l * 16,
             (u32x4){cvt_pk(v[0][0], v[0][1]), cvt_pk(v[0][2], v[0][3]), cvt_pk(v[1][0], v[1][1]), cvt_pk(v[1][2], v[1][3])});
      }
      run(6, 10);
      wait_vm<0>();
      if (w < 2 && l == 0) st8u(ws, WS_FK + (rj * 32 + w * 16 + rf) * 64, tag);
      run(10, 18);
    } else {
      run(0, 6);
      wait_vm<0>();
      run(6, 8);
      if (tid == 0) st8u(ws, WS_F1 + (panel1 * 32 + (isq ? 24 : 16) + cb1) * 64, tag);
      run(8, 18);
    }
    if (isq) {
      const unsigned foff = (l & 15) < 9 ? WS_FK + (cb2 * 32 + (l & 31)) * 64 : WS_F1 + (panel1 * 32 + 16 + cb2) * 64;
      const u64 fv = ld8(ws, foff);
      run(18, 22);
      if (!__all(fv == tag)) wait_words(ws, foff, l < 32, tag);
#pragma unroll
      for (int i = 0; i < 3; ++i) {
        int ins = w + 8 * i; if (ins >= 18) ins = 17;
        ins = __builtin_amdgcn_readfirstlane(ins);
        glds16c(ws, WS_KVT + cb2 * 18432 + ins * 1024 + l * 16, __builtin_amdgcn_readfirstlane(lds0 + L_KVT + ins * 1024));
      }
      const unsigned zo = WS_Z1 + (unsigned)(((panel1 * 8 + cb2) * 4 + 2 + (wt >> 1)) * 6) * 1024 + l * 16 + (wt & 1) * 8;
#pragma unroll
      for (int n = 0; n < 6; ++n) zreg[n] = __builtin_amdgcn_raw_buffer_load_b64(rws, zo + n * 1024, 0, 16);
      run(22, 24);
      wait_vm<0>();
      run(24, 26);
      u32x2 ov[5];
#pragma unroll
      for (int et = 0; et < 5; ++et) {
        f32x4 num = {0.f, 0.f, 0.f, 0.f}, den = {0.f, 0.f, 0.f, 0.f};
        const int st0 = et >= 3 ? 1 : 0, nst = et == 0 ? 1 : 2, f0 = et == 0 ? 0 : 1 + (et - 1) * 2;
#pragma unroll
        for (int s = 0; s < nst; ++s) {
          bf16x8 ka = lds_b128(lds0 + L_KVT + (f0 + s) * 1024 + l * 16), za = lds_b128(lds0 + L_KVT + (9 + f0 + s) * 1024 + l * 16);
          num = MFMA(ka, pf[st0 + s], num); den = MFMA(za, pf[st0 + s], den);
        }
        float v[4];
#pragma unroll
        for (int j = 0; j < 4; ++j) v[j] = num[j] * __builtin_amdgcn_rcpf(den[j] + 1e-6f);
        ov[et] = (u32x2){cvt_pk(v[0], v[1]), cvt_pk(v[2], v[3])};
      }
      const unsigned ob = WS_OH + (unsigned)(((2 * panel1 + kh) * 44 + ((cb2 * 5) >> 1)) * 4 + wt) * 1024 + l * 16;
      if ((cb2 & 1) == 0) {
        st16(rws, ob, (u32x4){ov[0][0], ov[0][1], ov[1][0], ov[1][1]});
        st16(rws, ob + 4096, (u32x4){ov[2][0], ov[2][1], ov[3][0], ov[3][1]});
        st8(ws, ob + 8192, ov[4]);
      } else {
        st8(ws, ob + 8, ov[0]);
        st16(rws, ob + 4096, (u32x4){ov[1][0], ov[1][1], ov[2][0], ov[2][1]});
        st16(rws, ob + 8192, (u32x4){ov[3][0], ov[3][1], ov[4][0], ov[4][1]});
      }
      run(26, 30);
      wait_vm<0>();
      run(30, 32);
      if (tid == 0) st8u(ws, WS_FO + (panel1 * 8 + cb2) * 64, tag);
    } else {
      run(18, 32);
#pragma unroll
      for (int n = 0; n < 6; ++n) zreg[n] = *(LAS u32x2*)(uintptr_t)(lds0 + L_KVT + 6144 + ((wt >> 1) * 6 + n) * 1024 + l * 16 + (wt & 1) * 8);
    }
#pragma unroll
    for (int n = 0; n < 6; ++n) acc[n] = MFMA(bfr[1][n], af[1], acc[n]);
    STAMP(4);
    {
      const int r0 = tid / 10, h0 = tid - r0 * 10, c1 = (tid & 127) + 512, r1 = c1 / 10, h1 = c1 - r1 * 10;
      xo0 = (unsigned)((panel3 * 64 + r0) * D_ + cb3 * 80 + h0 * 8); xo1 = (unsigned)((panel3 * 64 + r1) * D_ + cb3 * 80 + h1 * 8);
      la0 = lds0 + (unsigned)((r0 * 84 + h0 * 8) * 4); la1 = lds0 + (unsigned)((r1 * 84 + h1 * 8) * 4);
      xv[0] = *(const u32x4*)(P.x + xo0); xv[1] = *(const u32x4*)(P.x + xo1);
    }
    bar();
    const unsigned L_RED = lds0 + 3 * P3_SLOT;
    if (kh == 1) {
#pragma unroll
      for (int n = 0; n < 6; ++n) stsf4(L_RED + (wt * 6 + n) * 1024 + l * 16, acc[n]);
    }
    bar();
    if (kh == 0) {
#pragma unroll
      for (int n = 0; n < 6; ++n) acc[n] += ldsf4(L_RED + (wt * 6 + n) * 1024 + l * 16);
      u32x2 hv[6];
#pragma unroll
      for (int n = 0; n < 6; ++n) {
        f32x4 gg = acc[n] + bs;
        hv[n] = (u32x2){cvt_pk(lo16(zreg[n][0]) * gg[0], hi16(zreg[n][0]) * gg[1]), cvt_pk(lo16(zreg[n][1]) * gg[2], hi16(zreg[n][1]) * gg[3])};
      }
      const unsigned hb = WS_OH + (unsigned)((panel2 * 44 + 20 + cb2 * 3) * 4 + wt) * 1024 + l * 16;
#pragma unroll
      for (int s = 0; s < 3; ++s) st16(rws, hb + s * 4096, (u32x4){hv[2 * s][0], hv[2 * s][1], hv[2 * s + 1][0], hv[2 * s + 1][1]});
      wait_vm<0>();
      if (l == 0) st8u(ws, WS_F2 + ((panel2 * 8 + cb2) * 4 + wt) * 64, tag);
    }
    bar();
    STAMP(5);
  }
  {
    const int wsr = w & 3, kh = w >> 2;
    const unsigned ao = (unsigned)(kh * 8192 + wsr * 1024 + l * 16);
    const unsigned bb = 16384u + kh * 10240 + l * 16;
    const bf16x8 zeros = {0, 0, 0, 0, 0, 0, 0, 0};
    f32x4 acc[5];
#pragma unroll
    for (int n = 0; n < 5; ++n) acc[n] = (f32x4){0.f, 0.f, 0.f, 0.f};
    bf16x8 af[2][2], bfr[2][2][5];
    unsigned sb = lds0;
    auto mm = [&](int c) {
#pragma unroll
      for (int s2 = 0; s2 < 2; ++s2)
#pragma unroll
        for (int n = 0; n < 5; ++n) acc[n] = MFMA(bfr[c][s2][n], af[c][s2], acc[n]);
      SB0;
    };
    auto rdu = [&](int c) {
      bar();
#pragma unroll
      for (int s2 = 0; s2 < 2; ++s2) {
        af[c][s2] = lds_b128(sb + ao + s2 * 4096);
#pragma unroll
        for (int n = 0; n < 5; ++n) bfr[c][s2][n] = lds_b128(sb + bb + s2 * 5120 + n * 1024);
      }
      SB0;
      sb += P3_SLOT; if (sb == lds0 + 4 * P3_SLOT) sb = lds0;
    };
    rdu(0);
#pragma unroll 1
    for (int u = 0; u < 5; ++u) { rdu(1); mm(0); rdu(0); mm(1); }
    mm(0);
    STAMP(6);
    bar();
#pragma unroll
    for (int n = 0; n < 5; ++n) stsf4(lds0 + (unsigned)(((kh * 64 + wsr * 16 + fr) * 84 + n * 16 + 4 * g) * 4), acc[n]);
    bar();
#pragma unroll
    for (int i = 0; i < 2; ++i) {
      if (i == 0 || tid < 128) {
        const unsigned a = i == 0 ? la0 : la1;
        f32x4 a0 = ldsf4(a) + ldsf4(a + 64 * 84 * 4), a1 = ldsf4(a + 16) + ldsf4(a + 64 * 84 * 4 + 16);
        u32x4 o = {cvt_pk(lo16(xv[i][0]) + a0[0], hi16(xv[i][0]) + a0[1]), cvt_pk(lo16(xv[i][1]) + a0[2], hi16(xv[i][1]) + a0[3]),
                   cvt_pk(lo16(xv[i][2]) + a1[0], hi16(xv[i][2]) + a1[1]), cvt_pk(lo16(xv[i][3]) + a1[2], hi16(xv[i][3]) + a1[3])};
        *(u32x4*)(P.out + (i == 0 ? xo0 : xo1)) = o;
      }
    }
    STAMP(7);
  }
}

extern "C" void kernel_launch(void* const* d_in, const int* in_sizes, int n_in, void* d_out, int out_size,
                              void* d_ws, size_t ws_size, hipStream_t stream) {
  Params P;
  P.x = (const u16*)d_in[0];
  P.wqkv = (const u16*)d_in[1];
  P.wo = (const u16*)d_in[2];
  P.win = (const u16*)d_in[4];
  P.wsp = (const u16*)d_in[5];
  P.bsp = (const u16*)d_in[6];
  P.wout = (const u16*)d_in[7];
  P.out = (u16*)d_out;
  P.ws = (char*)d_ws;
  (void)hipFuncSetAttribute((const void*)mega_kernel, hipFuncAttributeMaxDynamicSharedMemorySize, LDS_TOTAL);
  mega_kernel<<<256, 768, LDS_TOTAL, stream>>>(P);
}
```

```cpp
#include <hip/hip_runtime.h>
#include <hip/hip_bf16.h>
#include <stdint.h>

typedef __attribute__((ext_vector_type(8))) short bf16x8;
typedef __attribute__((ext_vector_type(4))) short bf16x4;
typedef __attribute__((ext_vector_type(4))) float f32x4;
typedef __attribute__((ext_vector_type(2))) float f32x2;
typedef __attribute__((ext_vector_type(2))) __bf16 bf2;
typedef __attribute__((ext_vector_type(4))) unsigned u32x4;
typedef __attribute__((ext_vector_type(2))) unsigned u32x2;
typedef unsigned short u16;
typedef unsigned long long u64;
#define LAS __attribute__((address_space(3)))
#define GAS __attribute__((address_space(1)))
#define DI __device__ __forceinline__
#define RLX_AGENT __ATOMIC_RELAXED, __HIP_MEMORY_SCOPE_AGENT

constexpr int S_ = 2048, D_ = 640, NQKV = 1920, FIN = 1536, FH = 768;

constexpr unsigned WS_PQ  = 0;
constexpr unsigned WS_Z1  = WS_PQ + 16 * 8 * 8 * 3 * 1024;
constexpr unsigned WS_Z2N = WS_Z1 + 16 * 8 * 24 * 1024;
constexpr unsigned WS_OH  = WS_Z2N + 8 * 64 * 6 * 1024;
constexpr unsigned WS_WF  = WS_OH + 32 * 44 * 4 * 1024;
constexpr unsigned WS_KVT = WS_WF + 8 * 44 * 5 * 1024;
constexpr unsigned WS_KVP = WS_KVT + 8 * 18 * 1024;
constexpr unsigned WS_GR  = WS_KVP + 8 * 18 * 16 * 1024;
constexpr unsigned WS_GX  = WS_GR + 16 * 8 * 256 * 8;
constexpr unsigned WS_GC  = WS_GX + 2048 * 16;
constexpr unsigned WS_F1  = WS_GC + 24 * 10 * 2048;
constexpr unsigned WS_F2  = WS_F1 + 16 * 32 * 64;
constexpr unsigned WS_FO  = WS_F2 + 1024 * 64;
constexpr unsigned WS_FK  = WS_FO + 256 * 64;
constexpr unsigned WS_EP  = WS_FK + 256 * 64;
constexpr unsigned WS_END = WS_EP + 64;

DI float bf2f(u16 v) { return __uint_as_float(((unsigned)v) << 16); }
DI unsigned cvt_pk(float lo, float hi) {
  bf2 r; r[0] = (__bf16)lo; r[1] = (__bf16)hi; return __builtin_bit_cast(unsigned, r);
}
DI float rbf(float x) { return (float)((__bf16)x); }
DI float lo16(unsigned u) { return __uint_as_float(u << 16); }
DI float hi16(unsigned u) { return __uint_as_float(u & 0xffff0000u); }

DI void glds16(const void* gbase, unsigned voff, unsigned lds_dst) {
  unsigned keep;
  asm volatile("s_mov_b32 %0, m0\n\ts_mov_b32 m0, %3\n\ts_nop 0\n\tglobal_load_lds_dwordx4 %1, %2\n\ts_mov_b32 m0, %0"
               : "=&s"(keep) : "v"(voff), "s"(gbase), "s"(lds_dst) : "memory");
}
DI void glds16c(const void* gbase, unsigned voff, unsigned lds_dst) {
  unsigned keep;
  asm volatile("s_mov_b32 %0, m0\n\ts_mov_b32 m0, %3\n\ts_nop 0\n\tglobal_load_lds_dwordx4 %1, %2 sc1\n\ts_mov_b32 m0, %0"
               : "=&s"(keep) : "v"(voff), "s"(gbase), "s"(lds_dst) : "memory");
}
#define GL_PRE "s_mov_b32 %0, m0\n\ts_mov_b32 m0, %2\n\ts_nop 0\n\t"
#define GL_POST "s_mov_b32 m0, %0"
DI void gl2(const void* gb, unsigned lds, unsigned v0, unsigned v1) {
  unsigned keep;
  asm volatile(GL_PRE "global_load_lds_dwordx4 %3, %1\n\tglobal_load_lds_dwordx4 %4, %1 offset:1024\n\t" GL_POST
               : "=&s"(keep) : "s"(gb), "s"(lds), "v"(v0), "v"(v1) : "memory");
}
DI void gl3(const void* gb, unsigned lds, unsigned v0, unsigned v1, unsigned v2) {
  unsigned keep;
  asm volatile(GL_PRE "global_load_lds_dwordx4 %3, %1\n\tglobal_load_lds_dwordx4 %4, %1 offset:1024\n\tglobal_load_lds_dwordx4 %5, %1 offset:2048\n\t" GL_POST
               : "=&s"(keep) : "s"(gb), "s"(lds), "v"(v0), "v"(v1), "v"(v2) : "memory");
}
DI void gl4(const void* gb, unsigned lds, unsigned v0, unsigned v1, unsigned v2, unsigned v3) {
  unsigned keep;
  asm volatile(GL_PRE "global_load_lds_dwordx4 %3, %1\n\tglobal_load_lds_dwordx4 %4, %1 offset:1024\n\tglobal_load_lds_dwordx4 %5, %1 offset:2048\n\tglobal_load_lds_dwordx4 %6, %1 offset:3072\n\t" GL_POST
               : "=&s"(keep) : "s"(gb), "s"(lds), "v"(v0), "v"(v1), "v"(v2), "v"(v3) : "memory");
}
DI void gl3c(const void* gb, unsigned lds, unsigned v0) {
  unsigned keep;
  asm volatile(GL_PRE "global_load_lds_dwordx4 %3, %1 sc1\n\tglobal_load_lds_dwordx4 %3, %1 offset:1024 sc1\n\tglobal_load_lds_dwordx4 %3, %1 offset:2048 sc1\n\t" GL_POST
               : "=&s"(keep) : "s"(gb), "s"(lds), "v"(v0) : "memory");
}
DI void gl4c(const void* gb, unsigned lds, unsigned v0) {
  unsigned keep;
  asm volatile(GL_PRE "global_load_lds_dwordx4 %3, %1 sc1\n\tglobal_load_lds_dwordx4 %3, %1 offset:1024 sc1\n\tglobal_load_lds_dwordx4 %3, %1 offset:2048 sc1\n\tglobal_load_lds_dwordx4 %3, %1 offset:3072 sc1\n\t" GL_POST
               : "=&s"(keep) : "s"(gb), "s"(lds), "v"(v0) : "memory");
}
template <int N> DI void wait_vm() { asm volatile("s_waitcnt vmcnt(%0)" :: "n"(N) : "memory"); }

DI bf16x8 lds_b128(unsigned addr) { return *(LAS bf16x8*)(uintptr_t)addr; }
DI bf16x8 lds_2b64(unsigned addr0, unsigned addr1) {
  bf16x4 a = *(LAS bf16x4*)(uintptr_t)addr0, b = *(LAS bf16x4*)(uintptr_t)addr1;
  return __builtin_shufflevector(a, b, 0, 1, 2, 3, 4, 5, 6, 7);
}
DI bf16x8 lds_tr(unsigned addr0, unsigned addr1) {
  bf16x4 a = __builtin_amdgcn_ds_read_tr16_b64_v4i16((LAS bf16x4*)(uintptr_t)addr0);
  bf16x4 b = __builtin_amdgcn_ds_read_tr16_b64_v4i16((LAS bf16x4*)(uintptr_t)addr1);
  return __builtin_shufflevector(a, b, 0, 1, 2, 3, 4, 5, 6, 7);
}
DI float ldsf(unsigned a) { return *(LAS float*)(uintptr_t)a; }
DI void stsf(unsigned a, float v) { *(LAS float*)(uintptr_t)a = v; }
DI f32x4 ldsf4(unsigned a) { return *(LAS f32x4*)(uintptr_t)a; }
DI void stsf4(unsigned a, f32x4 v) { *(LAS f32x4*)(uintptr_t)a = v; }
#define SB0 __builtin_amdgcn_sched_barrier(0)
#define MFMA(a, b, c) __builtin_amdgcn_mfma_f32_16x16x32_bf16((a), (b), (c), 0, 0, 0)

template <int CTRL> DI float dpp(float v) {
  return __builtin_bit_cast(float, __builtin_amdgcn_update_dpp(0, __builtin_bit_cast(int, v), CTRL, 0xf, 0xf, true));
}
DI float sum8(float v) { v += dpp<0xB1>(v); v += dpp<0x4E>(v); v += dpp<0x141>(v); return v; }
DI float sum16(float v) { v = sum8(v); v += dpp<0x140>(v); return v; }

DI float gelu_t(float x) {
  float u = x * (0.7978845608f + 0.0356774081f * x * x);
  float e = __builtin_amdgcn_exp2f(-2.885390082f * u);
  return x * __builtin_amdgcn_rcpf(1.0f + e);
}
DI f32x4 gelu4(f32x4 x) {
  const f32x4 a = x * ((x * x) * -0.1029432397f + -2.302208198f);
  f32x4 e;
#pragma unroll
  for (int j = 0; j < 4; ++j) e[j] = __builtin_amdgcn_exp2f(a[j]);
  e += 1.0f;
#pragma unroll
  for (int j = 0; j < 4; ++j) e[j] = __builtin_amdgcn_rcpf(e[j]);
  return x * e;
}
DI f32x4 phi4(f32x4 t) {
  f32x4 a, m;
#pragma unroll
  for (int j = 0; j < 4; ++j) { a[j] = fminf(t[j], 0.f); m[j] = fmaxf(t[j], 0.f); }
  a *= 1.4426950409f;
#pragma unroll
  for (int j = 0; j < 4; ++j) a[j] = __builtin_amdgcn_exp2f(a[j]);
  return (m + 1e-6f) + a;
}
DI float phi_f(float t) {
  return (fmaxf(t, 0.f) + 1e-6f) + __builtin_amdgcn_exp2f(fminf(t, 0.f) * 1.4426950409f);
}

typedef __amdgpu_buffer_rsrc_t rsrc_t;
DI void st16(rsrc_t r, unsigned off, u32x4 v) { __builtin_amdgcn_raw_buffer_store_b128(v, r, off, 0, 16); }
DI u32x4 ld16(rsrc_t r, unsigned off) { return __builtin_amdgcn_raw_buffer_load_b128(r, off, 0, 16); }
DI void st8(char* ws, unsigned off, u32x2 v) { __hip_atomic_store((GAS u64*)(ws + off), ((u64)v[1] << 32) | v[0], RLX_AGENT); }
DI void st8u(char* ws, unsigned off, u64 v) { __hip_atomic_store((GAS u64*)(ws + off), v, RLX_AGENT); }
DI void st4(char* ws, unsigned off, float v) { __hip_atomic_store((GAS unsigned*)(ws + off), __float_as_uint(v), RLX_AGENT); }
DI u64 ld8(const char* ws, unsigned off) { return __hip_atomic_load((GAS u64*)(ws + off), RLX_AGENT); }
constexpr unsigned SPIN_CAP = 1u << 20;
DI void wait_words(const char* ws, unsigned off, bool act, u64 tag) {
#pragma unroll 1
  for (unsigned sp = 0; sp < SPIN_CAP; ++sp) {
    u64 v = act ? ld8(ws, off) : tag;
    if (__all(v == tag)) break;
    __builtin_amdgcn_s_sleep(1);
  }
  __builtin_amdgcn_fence(__ATOMIC_ACQUIRE, "wavefront");
}

struct Params { const u16 *x, *wqkv, *wo, *win, *wsp, *bsp, *wout; u16* out; char* ws; };

constexpr unsigned KBS = 4224;
constexpr unsigned K1_STAGE = 16384 + 8 * KBS;
constexpr unsigned KZ_STAGE = 28672;
constexpr unsigned L_MISC = 153600;
constexpr int LDS_TOTAL = 163840;
constexpr unsigned P2_SLOT = 20480;
constexpr unsigned L_KVT = 135168;
constexpr unsigned P3_SLOT = 36864;

DI void bar() { asm volatile("s_waitcnt lgkmcnt(0)" ::: "memory"); __builtin_amdgcn_s_barrier(); asm volatile("" ::: "memory"); }

struct Side { u32x4 r0, r1; };
DI void side_issue(const Params& P, Side& S, int row, int w) {
  const int l = threadIdx.x & 63;
  const u32x4* src = (const u32x4*)(P.x + (size_t)row * D_);
  S.r0 = src[l]; S.r1 = src[64 + (l & 15)];
}
DI float rdl(float v, int lane) { return __builtin_bit_cast(float, __builtin_amdgcn_readlane(__builtin_bit_cast(int, v), lane)); }
DI void side_finish(const Params& P, rsrc_t rws, const Side& S, int row, int w, unsigned tl, unsigned th) {
  const int l = threadIdx.x & 63;
  float s = 0.f, q = 0.f, s2 = 0.f, q2 = 0.f;
#pragma unroll
  for (int j = 0; j < 4; ++j) {
    float a = lo16(S.r0[j]), b = hi16(S.r0[j]), c = lo16(S.r1[j]), d = hi16(S.r1[j]);
    s += a + b; q += a * a + b * b; s2 += c + d; q2 += c * c + d * d;
  }
  s = sum16(s); q = sum16(q); s2 = sum16(s2); q2 = sum16(q2);
  s = (rdl(s, 0) + rdl(s, 16)) + (rdl(s, 32) + rdl(s, 48)) + s2;
  q = (rdl(q, 0) + rdl(q, 16)) + (rdl(q, 32) + rdl(q, 48)) + q2;
  const float m = s * (1.f / D_);
  const float v = rbf(fmaxf(q * (1.f / D_) - m * m, 0.f));
  const float r = 1.f / rbf(sqrtf(rbf(v + 1e-5f)));
  if (l < 2) st8u(P.ws, WS_GX + row * 16 + l * 8, l == 0 ? (((u64)tl << 32) | __float_as_uint(-rbf(m) * r)) : (((u64)th << 32) | __float_as_uint(r)));
}
DI void wf_job(const Params& P, rsrc_t rws, int w) {
  const int l = threadIdx.x & 63;
  int item = blockIdx.x; if (item > 219) item = 219;
  const int st = item / 5, T = (item - st * 5) * 8 + w, cb = T / 5, nt = T - cb * 5;
  const u16* wsrc = (st < 20 ? P.wo + (size_t)st * 32 * D_ : P.wout + (size_t)(st - 20) * 32 * D_) + (size_t)(l >> 4) * 4 * D_ + T * 16 + (l & 15);
  unsigned wv[8];
#pragma unroll
  for (int e = 0; e < 8; ++e) wv[e] = wsrc[(size_t)(16 * (e >> 2) + (e & 3)) * D_];
  st16(rws, WS_WF + (unsigned)(((cb * 44 + st) * 5 + nt) * 1024) + l * 16,
       (u32x4){wv[0] | (wv[1] << 16), wv[2] | (wv[3] << 16), wv[4] | (wv[5] << 16), wv[6] | (wv[7] << 16)});
}
DI void xstat_gather(const char* ws, int panel, unsigned L_ST, int tid, unsigned tg, u64 gv) {
  if (tid < 256) {
#pragma unroll 1
    for (unsigned sp = 0; sp < SPIN_CAP; ++sp) {
      if (__all((unsigned)(gv >> 32) == tg)) break;
      __builtin_amdgcn_s_sleep(1);
      gv = ld8(ws, WS_GX + panel * 2048 + tid * 8);
    }
    stsf(L_ST + (tid & 1) * 512 + (tid >> 1) * 4, __uint_as_float((unsigned)gv));
  }
}

DI void csum_issue(const char* ws, int blk, int t, u64 (&cv)[10]) {
#pragma unroll
  for (int k = 0; k < 10; ++k) cv[k] = ld8(ws, WS_GC + (blk * 10 + k) * 2048 + t * 8);
}
DI void csum_gather(const char* ws, int blk, int t, unsigned tl, u64 (&cv)[10], unsigned dst) {
#pragma unroll 1
  for (unsigned sp = 0; sp < SPIN_CAP; ++sp) {
    bool ok = true;
#pragma unroll
    for (int k = 0; k < 10; ++k) ok &= (unsigned)(cv[k] >> 32) == tl;
    if (__all(ok)) break;
    __builtin_amdgcn_s_sleep(1);
    csum_issue(ws, blk, t, cv);
  }
  float a = 0.f;
#pragma unroll
  for (int k = 0; k < 10; ++k) a += __uint_as_float((unsigned)cv[k]);
  stsf(dst + t * 4, a);
}
DI void csum_store(char* ws, unsigned off, f32x4 d, int fr, int g, unsigned tl) {
  const float v = fr == 0 ? d[0] : fr == 1 ? d[1] : fr == 2 ? d[2] : d[3];
  if (fr < 4) st8u(ws, off + (4 * g + fr) * 8, ((u64)tl << 32) | __float_as_uint(v));
}

DI void p1q_consumer(const Params& P, rsrc_t rws, int w, int panel, int cbi, unsigned lds0, u64 tag, bf16x8 (&pf)[3]) {
  char* ws = P.ws;
  const int tid = threadIdx.x, l = tid & 63;
  const int wm = w >> 2, wc = (w + 2 * wm) & 3;
  const int g = l >> 4, fr = l & 15, q = (l & 15) >> 2, p = l & 3, g1 = g & 1;
  const unsigned aoff0 = (unsigned)((wm * 64 + fr) * 128 + ((g ^ (l & 7)) << 4));
  const unsigned ttab = (unsigned)(0x4498E765DC32BA10ull >> (16 * wc)) & 0xffffu;
  int tile[4];
  unsigned bo[4];
#pragma unroll
  for (int n = 0; n < 4; ++n) {
    tile[n] = (ttab >> (4 * n)) & 15;
    bo[n] = 16384u + g * KBS + q * 480 + p * 8 + tile[n] * 32;
  }
  const unsigned tl = (unsigned)tag, th = tl * 0x9E3779B1u ^ (unsigned)(tag >> 32);
  {
    Side S; side_issue(P, S, panel * 128 + cbi * 8 + w, w);
    side_finish(P, rws, S, panel * 128 + cbi * 8 + w, w, tl, th);
  }
  f32x4 acc[4][4];
#pragma unroll
  for (int m = 0; m < 4; ++m)
#pragma unroll
    for (int n = 0; n < 4; ++n) acc[m][n] = (f32x4){0.f, 0.f, 0.f, 0.f};
  const short one = 0x3F80;
  const bf16x8 ones = {one, one, one, one, one, one, one, one};
  const bf16x8 zeros = {0, 0, 0, 0, 0, 0, 0, 0};

  bf16x8 af[2][4], bfr[2][4];
#pragma unroll
  for (int m = 0; m < 4; ++m) { af[1][m] = zeros; bfr[1][m] = zeros; }
  auto mm = [&](int ks) {
#pragma unroll
    for (int n = 0; n < 3; ++n)
#pragma unroll
      for (int m = 0; m < 4; ++m) acc[m][n] = MFMA(bfr[ks][n], af[ks][m], acc[m][n]);
    if (wc < 3) {
#pragma unroll
      for (int m = 0; m < 4; ++m) acc[m][3] = MFMA(bfr[ks][3], af[ks][m], acc[m][3]);
    }
  };
  auto step = [&](unsigned sb, int ks) {
#pragma unroll
    for (int n = 0; n < 4; ++n) bfr[ks][n] = lds_tr(sb + ks * 4 * KBS + bo[n], sb + ks * 4 * KBS + bo[n] + 1920);
#pragma unroll
    for (int m = 0; m < 4; ++m) af[ks][m] = lds_b128(sb + ((aoff0 ^ (ks * 64)) + m * 2048));
    SB0;
    mm(ks ^ 1);
    SB0;
  };
  unsigned sb = lds0;
  auto run = [&](int n) {
#pragma unroll 1
    for (int kt = 0; kt < n; ++kt) {
      bar();
      step(sb, 0);
      step(sb, 1);
      sb += K1_STAGE; if (sb == lds0 + 3 * K1_STAGE) sb = lds0;
    }
  };
  run(1);
  if (panel < 10) {
#pragma unroll
    for (int h = 0; h < 2; ++h) {
      const bf16x8 b0 = wm == 0 ? bfr[0][h] : bfr[0][2 + h], b1 = wm == 0 ? bfr[1][h] : bfr[1][2 + h];
      const int tt = wm == 0 ? tile[h] : tile[2 + h];
      f32x4 d = MFMA(b0, ones, ((f32x4){0.f, 0.f, 0.f, 0.f}));
      d = MFMA(b1, ones, d);
      csum_store(ws, WS_GC + (cbi * 10 + panel) * 2048 + tt * 128, d, fr, g, tl);
    }
  }
  run(9);
  mm(1);
  bar();
  const unsigned L_ST = lds0 + L_MISC, L_CS = lds0 + L_MISC + 5120;
  const unsigned L_K = lds0, L_V = L_K + 20480, L_Q = lds0 + 122880;
  float mr[4], rinv[4];
#pragma unroll
  for (int m = 0; m < 4; ++m) { const int rl = wm * 64 + m * 16 + fr; mr[m] = ldsf(L_ST + rl * 4); rinv[m] = ldsf(L_ST + 512 + rl * 4); }
#pragma unroll
  for (int n = 0; n < 4; ++n) {
    if (n < 3 || wc < 3) {
      const int t = tile[n];
      const f32x4 cs = ldsf4(L_CS + (t * 16 + 4 * g) * 4);
      unsigned dst, dstep;
      if (t < 5) { dst = L_Q + (wm * 12 + (t >> 1)) * 1024 + l * 16 + (t & 1) * 8; dstep = 3072; }
      else {
        const int kb = wm * 8 + (fr >> 3), pos = (fr & 7) ^ ((fr >> 3) << 2);
        dst = (t < 10 ? L_K + (t - 5) * 256 : L_V + (t - 10) * 256) + kb * 1280 + pos * 32 + g * 8; dstep = 2560;
      }
      if (t < 10) {
#pragma unroll
        for (int m = 0; m < 4; ++m) {
          f32x4 v = acc[m][n] * rinv[m] + cs * mr[m];
          v = phi4(v);
          *(LAS u32x2*)(uintptr_t)(dst + m * dstep) = (u32x2){cvt_pk(v[0], v[1]), cvt_pk(v[2], v[3])};
        }
      } else {
#pragma unroll
        for (int m = 0; m < 4; ++m) {
          f32x4 v = acc[m][n] * rinv[m] + cs * mr[m];
          *(LAS u32x2*)(uintptr_t)(dst + m * dstep) = (u32x2){cvt_pk(v[0], v[1]), cvt_pk(v[2], v[3])};
        }
      }
    }
  }
  bar();
  {
    const unsigned troff = (unsigned)(g * 5 * 256 + ((4 * g1 + q) * 32) + p * 8);
    f32x4 d[3];
    int jb[3];
#pragma unroll
    for (int r = 0; r < 3; ++r) {
      int job = w + 8 * r; if (job >= 18) job = w;
      jb[r] = job;
      const int i = job < 13 ? (job + 1) / 3 : job - 13;
      const int j = job < 13 ? job - 2 * i : 0;
      d[r] = (f32x4){0.f, 0.f, 0.f, 0.f};
#pragma unroll
      for (int ks = 0; ks < 4; ++ks) {
        const unsigned a0 = L_K + troff + (ks * 20 + i) * 256, b0 = L_V + troff + (ks * 20 + j) * 256;
        const bf16x8 ka = lds_tr(a0, a0 ^ 128);
        bf16x8 vb = lds_tr(b0, b0 ^ 128);
        if (job >= 13) vb = ones;
        d[r] = MFMA(ka, vb, d[r]);
      }
    }
#pragma unroll
    for (int r = 0; r < 3; ++r)
      st16(rws, WS_KVP + (unsigned)(((cbi * 18 + jb[r]) * 16 + panel) * 1024) + l * 16, __builtin_bit_cast(u32x4, d[r]));
    pf[0] = lds_b128(L_Q + w * 3072 + l * 16); pf[1] = lds_b128(L_Q + w * 3072 + 1024 + l * 16);
    const u32x2 q2 = *(LAS u32x2*)(uintptr_t)(L_Q + w * 3072 + 2048 + l * 16);
    pf[2] = __builtin_bit_cast(bf16x8, ((u32x4){q2[0], q2[1], 0u, 0u}));
  }
  wait_vm<0>();
  bar();
  if (tid == 0) st8u(ws, WS_F1 + (panel * 32 + cbi) * 64, tag);
}

DI void p1z_consumer(const Params& P, rsrc_t rws, int w, int panel, int cbj, unsigned lds0, u64 tag) {
  char* ws = P.ws;
  const int tid = threadIdx.x, l = tid & 63;
  const int wm = w & 3, wn = w >> 2;
  const int g = l >> 4, fr = l & 15, q = (l & 15) >> 2, p = l & 3, g1 = g & 1;
  const unsigned aoff0 = (unsigned)((wm * 32 + fr) * 128 + ((g ^ (l & 7)) << 4));
  unsigned bo[3];
  int ca[3];
#pragma unroll
  for (int n = 0; n < 3; ++n) {
    const int c = wn * 3 + n;
    ca[n] = c;
    int pc = c + g1; if (pc == 6) pc = 0;
    bo[n] = 16384u + g * 1536 + q * 192 + pc * 32 + p * 8;
  }
  const short one = 0x3F80;
  const bf16x8 ones = {one, one, one, one, one, one, one, one};
  const bf16x8 zeros = {0, 0, 0, 0, 0, 0, 0, 0};
  f32x4 accA[2][3], accB[2][3];
#pragma unroll
  for (int m = 0; m < 2; ++m)
#pragma unroll
    for (int n = 0; n < 3; ++n) { accA[m][n] = (f32x4){0.f, 0.f, 0.f, 0.f}; accB[m][n] = (f32x4){0.f, 0.f, 0.f, 0.f}; }
  const unsigned tl = (unsigned)tag, th = tl * 0x9E3779B1u ^ (unsigned)(tag >> 32), tg = (tid & 1) ? th : tl;
  bf16x8 af[2][2][2], bfr[2][2][3];
  unsigned sb = lds0;
  auto rd = [&](int c) {
    bar();
#pragma unroll
    for (int ks = 0; ks < 2; ++ks) {
#pragma unroll
      for (int m = 0; m < 2; ++m) af[c][ks][m] = lds_b128(sb + ((aoff0 ^ (ks * 64)) + m * 2048));
#pragma unroll
      for (int n = 0; n < 3; ++n) { const unsigned o = sb + ks * 4 * 1536 + bo[n]; bfr[c][ks][n] = lds_tr(o, o + 768); }
    }
    SB0;
    sb += KZ_STAGE; if (sb == lds0 + 5 * KZ_STAGE) sb = lds0;
  };
  auto mmA = [&](int c) {
#pragma unroll
    for (int ks = 0; ks < 2; ++ks)
#pragma unroll
      for (int n = 0; n < 3; ++n)
#pragma unroll
        for (int m = 0; m < 2; ++m) accA[m][n] = MFMA(af[c][ks][m], bfr[c][ks][n], accA[m][n]);
    SB0;
  };
  auto mmB = [&](int c) {
#pragma unroll
    for (int ks = 0; ks < 2; ++ks)
#pragma unroll
      for (int n = 0; n < 3; ++n)
#pragma unroll
        for (int m = 0; m < 2; ++m) accB[m][n] = MFMA(bfr[c][ks][n], af[c][ks][m], accB[m][n]);
    SB0;
  };
  const unsigned L_ST = lds0 + L_MISC, L_CS = lds0 + L_MISC + 5120;
  const unsigned L_RS = lds0 + L_MISC + 2048;
  const unsigned L_FS = lds0 + L_MISC + 4096;
  auto cpub = [&](int blk) {
    if (panel < 10 && wm == 0) {
#pragma unroll
      for (int n = 0; n < 3; ++n) {
        f32x4 d = MFMA(bfr[0][0][n], ones, ((f32x4){0.f, 0.f, 0.f, 0.f}));
        d = MFMA(bfr[0][1][n], ones, d);
        csum_store(ws, WS_GC + (blk * 10 + panel) * 2048 + ca[n] * 128, d, fr, g, tl);
      }
    }
  };
  {
    Side S; side_issue(P, S, panel * 128 + 64 + cbj * 8 + w, w);
    side_finish(P, rws, S, panel * 128 + 64 + cbj * 8 + w, w, tl, th);
  }
  rd(0);
  cpub(8 + cbj);
#pragma unroll 1
  for (int i = 0; i < 3; ++i) { rd(1); mmA(0); rd(0); mmA(1); }
  const int tc = tid - 256;
  {
    u64 cv[10];
    if (tid < 256) cv[0] = ld8(ws, WS_GX + panel * 2048 + tid * 8);
    else if (tc < 96) csum_issue(ws, 8 + cbj, tc, cv);
    rd(1); mmA(0); rd(0); mmA(1); rd(1); mmA(0);
    if (tid < 256) xstat_gather(ws, panel, L_ST, tid, tg, cv[0]);
    else if (tc < 96) csum_gather(ws, 8 + cbj, tc, tl, cv, L_CS);
  }
  rd(0); mmA(1);
  cpub(16 + cbj);
  {
    float cn[3];
#pragma unroll
    for (int n = 0; n < 3; ++n) cn[n] = ldsf(L_CS + (ca[n] * 16 + fr) * 4);
#pragma unroll
    for (int m = 0; m < 2; ++m) {
      const int rl = wm * 32 + m * 16 + 4 * g;
      const f32x4 mr = ldsf4(L_ST + rl * 4), rinv = ldsf4(L_ST + 512 + rl * 4);
      f32x4 rs = {0.f, 0.f, 0.f, 0.f}, rq = {0.f, 0.f, 0.f, 0.f};
#pragma unroll
      for (int n = 0; n < 3; ++n) {
        f32x4 v = accA[m][n] * rinv + mr * cn[n];
        v = gelu4(v);
        rs += v; rq += v * v;
        accA[m][n] = v;
      }
#pragma unroll
      for (int j = 0; j < 4; ++j) { rs[j] = sum16(rs[j]); rq[j] = sum16(rq[j]); }
      if (fr == 0) {
        stsf4(L_RS + ((wn * 128 + rl) * 2) * 4, (f32x4){rs[0], rq[0], rs[1], rq[1]});
        stsf4(L_RS + ((wn * 128 + rl) * 2 + 4) * 4, (f32x4){rs[2], rq[2], rs[3], rq[3]});
      }
    }
  }
  rd(1);
  float part = 0.f;
  const unsigned gbase = WS_GR + (unsigned)(panel * 8) * 2048 + tid * 8;
  if (tid < 256) {
    part = ldsf(L_RS + tid * 4) + ldsf(L_RS + 1024 + tid * 4);
    st8u(ws, gbase + cbj * 2048, ((u64)tg << 32) | __float_as_uint(part));
  }
  mmB(0);
  rd(0); mmB(1);
  {
    u64 cv[10];
    if (tid < 256) {
#pragma unroll
      for (int i = 1; i < 8; ++i) cv[i] = ld8(ws, gbase + ((cbj + i) & 7) * 2048);
    }
    rd(1); mmB(0);
    if (tid < 256) {
      float tot;
#pragma unroll 1
      for (unsigned sp = 0; sp < SPIN_CAP; ++sp) {
        bool ok = true; tot = part;
#pragma unroll
        for (int i = 1; i < 8; ++i) { ok &= (unsigned)(cv[i] >> 32) == tg; tot += __uint_as_float((unsigned)cv[i]); }
        if (__all(ok)) break;
#pragma unroll
        for (int i = 1; i < 8; ++i) cv[i] = ld8(ws, gbase + ((cbj + i) & 7) * 2048);
      }
      float other = dpp<0xB1>(tot);
      if ((tid & 1) == 0) {
        float m = tot * (1.f / FH);
        float v = rbf(fmaxf(other * (1.f / FH) - m * m, 0.f));
        float r = 1.f / rbf(sqrtf(rbf(v + 1e-5f)));
        stsf(L_FS + (tid >> 1) * 4, -rbf(m) * r);
        stsf(L_FS + 512 + (tid >> 1) * 4, r);
      }
    }
  }
  rd(0);
  {
    f32x4 mr[2], ri[2];
#pragma unroll
    for (int m = 0; m < 2; ++m) { const int rl = wm * 32 + m * 16 + 4 * g; mr[m] = ldsf4(L_FS + rl * 4); ri[m] = ldsf4(L_FS + 512 + rl * 4); }
#pragma unroll
    for (int n = 0; n < 3; ++n) {
      f32x4 a = accA[0][n] * ri[0] + mr[0], b = accA[1][n] * ri[1] + mr[1];
      st16(rws, WS_Z2N + (unsigned)(((cbj * 64 + panel * 4 + wm) * 6 + ca[n]) * 1024) + l * 16,
           (u32x4){cvt_pk(a[0], a[1]), cvt_pk(a[2], a[3]), cvt_pk(b[0], b[1]), cvt_pk(b[2], b[3])});
    }
  }
  mmB(1);
  rd(1); mmB(0); rd(0); mmB(1);
  wait_vm<0>();
  rd(1);
  if (tid == 0) st8u(ws, WS_F1 + (panel * 32 + 8 + cbj) * 64, tag);
  mmB(0);
  {
    u64 cv[10];
    if (tid >= 256 && tc < 96) csum_issue(ws, 16 + cbj, tc, cv);
    rd(0); mmB(1); rd(1); mmB(0);
    if (tid >= 256 && tc < 96) csum_gather(ws, 16 + cbj, tc, tl, cv, L_CS + 512);
  }
  mmB(1);
  bar();
  {
    f32x4 cn[3];
#pragma unroll
    for (int n = 0; n < 3; ++n) { const unsigned a = L_CS + 512 + (ca[n] * 16 + 4 * g) * 4; cn[n] = ldsf4(a); }
    float mr[2], rinv[2];
#pragma unroll
    for (int m = 0; m < 2; ++m) { const int rl = wm * 32 + m * 16 + fr; mr[m] = ldsf(L_ST + rl * 4); rinv[m] = ldsf(L_ST + 512 + rl * 4); }
#pragma unroll
    for (int n = 0; n < 3; ++n) {
      u32x4 o;
#pragma unroll
      for (int m = 0; m < 2; ++m) {
        f32x4 v = accB[m][n] * rinv[m] + cn[n] * mr[m];
        v = gelu4(v);
        o[2 * m] = cvt_pk(v[0], v[1]); o[2 * m + 1] = cvt_pk(v[2], v[3]);
      }
      if (wm < 2) *(LAS u32x4*)(uintptr_t)(lds0 + L_KVT + 6144 + (wm * 6 + ca[n]) * 1024 + l * 16) = o;
      else st16(rws, WS_Z1 + (unsigned)(((panel * 8 + cbj) * 4 + wm) * 6 + ca[n]) * 1024 + l * 16, o);
    }
  }
}

__global__ __launch_bounds__(768, 3) void mega_kernel(Params P) {
  extern __shared__ __attribute__((aligned(16))) char smem[];
  const unsigned lds0 = (unsigned)(uintptr_t)(LAS char*)smem;
  char* ws = P.ws;
  const int tid = threadIdx.x, l = tid & 63;
  const int w = __builtin_amdgcn_readfirstlane(tid >> 6);
  const int g = l >> 4, fr = l & 15;
  const int b = blockIdx.x, xcd = b & 7, bi = b >> 3;
  const int panel1 = (xcd >> 1) * 4 + (bi & 3), cb1 = (xcd & 1) * 4 + ((bi >> 2) & 3);
  const bool isq = bi < 16;
  const int panel2 = 2 * panel1 + (isq ? 1 : 0), cb2 = cb1;
  const int panel3 = xcd * 4 + (bi & 3), cb3 = bi >> 2;
  const u64 tag = ld8(ws, WS_EP) + 1;
  if (w == 7 && bi < 20) {
    u64 pc; asm volatile("s_getpc_b64 %0" : "=s"(pc));
    gl2((const char*)(pc & ~127ull) + bi * 2048, lds0 + 3 * K1_STAGE, l * 16, l * 16);
  }

  if (w >= 8) {
    const int lw = w - 8;
    const unsigned vA0 = (unsigned)(((l >> 3) * D_ + (((l & 7) ^ (l >> 3)) << 3)) * 2);
    const unsigned vA1 = vA0 + 8 * D_ * 2 - 1024, vA2 = vA0 + 16 * D_ * 2 - 2048, vA3 = vA0 + 24 * D_ * 2 - 3072;
    const char* gA = (const char*)(P.x + (size_t)(panel1 * 128 + lw * 32) * D_);
    const unsigned v20 = (unsigned)(((l >> 3) * S_ + (((l & 7) ^ (l >> 4)) << 3)) * 2);
    const unsigned v21 = (unsigned)((((l >> 3) + 8) * S_ + (((l & 7) ^ ((l >> 4) | 4)) << 3)) * 2) - 1024;
    const char* gA2 = (const char*)(P.wsp + (size_t)(panel2 * 64 + lw * 16) * S_);
    const int rot2 = 0;
    auto ldA = [&](int kt, unsigned sl) { gl2(gA2 + ((kt + rot2) & 31) * 128, sl + lw * 2048, v20, v21); };
    auto sl2 = [&](int kt) { return lds0 + ((kt + 4) % 6) * P2_SLOT; };
    const unsigned tl = (unsigned)tag, th = tl * 0x9E3779B1u ^ (unsigned)(tag >> 32);
    const int lt = tid - 512;
    auto gather = [&](int blk, int ncol, unsigned dst, bool xs) {
      u64 cv[10]; u64 gv = 0;
      const int tc = lt < ncol ? lt : 0;
      if (xs) gv = ld8(ws, WS_GX + panel1 * 2048 + lt * 8);
      csum_issue(ws, blk, tc, cv);
      if (xs) xstat_gather(ws, panel1, lds0 + L_MISC, lt, (lt & 1) ? th : tl, gv);
      csum_gather(ws, blk, tc, tl, cv, dst);
    };
    const unsigned l16 = l * 16;
    const char* gB2 = ws + WS_Z2N + (size_t)cb2 * 64 * 6144 + lw * 3072;
    auto ldB = [&](int kt, unsigned sl) { gl3c(gB2 + ((kt + rot2) & 31) * 12288, sl + 8192 + lw * 3072, l16); };
    const unsigned zfl = WS_F1 + ((l & 15) * 32 + 8 + cb2) * 64;
    auto try_z = [&]() {
      const u64 v = l < 16 ? ld8(ws, zfl) : tag;
      return (bool)__builtin_amdgcn_readfirstlane((int)__all(v == tag));
    };
    bool early = false;
    const int rot = panel1 < 10 ? panel1 : 0;
    auto krot = [&](int kt) { const int k = kt >= 10 ? kt - 10 : kt; return __builtin_amdgcn_readfirstlane(k == 0 ? rot : (k - 1 < rot ? k - 1 : k)); };
    if (isq) {
      gl4(gA + rot * 128, lds0 + lw * 4096, vA0, vA1, vA2, vA3);
      unsigned vB[4];
#pragma unroll
      for (int i = 0; i < 4; ++i) {
        int c = i * 64 + l; if (c > 239) c = 239;
        const int row = c / 30, cc = c - row * 30, seg = cc / 10, ch = cc - seg * 10;
        vB[i] = (unsigned)((row * NQKV + seg * 640 + cb1 * 80 + ch * 8) * 2) + 4096 - i * 1024;
      }
      const char* gB = (const char*)P.wqkv + (size_t)lw * 16 * NQKV * 2 - 4096;
      auto stageB = [&](int kt, unsigned sb) {
        const char* bp = gB + (size_t)krot(kt) * 64 * NQKV * 2;
        gl4(bp, sb + 16384 + lw * 2 * KBS, vB[0], vB[1], vB[2], vB[3]);
        gl4(bp + 8 * NQKV * 2, sb + 16384 + (lw * 2 + 1) * KBS, vB[0], vB[1], vB[2], vB[3]);
      };
      auto stage = [&](int kt, unsigned sb) { gl4(gA + krot(kt) * 128, sb + lw * 4096, vA0, vA1, vA2, vA3); stageB(kt, sb); };
      stageB(0, lds0); stage(1, lds0 + K1_STAGE);
      wait_vm<12>(); bar();
      stage(2, lds0 + 2 * K1_STAGE);
      unsigned sb = lds0, sc = lds0 + K1_STAGE;
#pragma unroll 1
      for (int kt = 1; kt < 8; ++kt) {
        wait_vm<12>(); bar();
        stage(kt + 2, sb);
        sb = sc; sc += K1_STAGE; if (sc == lds0 + 3 * K1_STAGE) sc = lds0;
      }
      wait_vm<12>(); bar();
      gather(cb1, 240, lds0 + L_MISC + 5120, true);
      bar();
      ldA(0, sl2(0)); ldA(1, sl2(1)); ldA(5, sl2(5));
      bar();
      ldA(4, sl2(4));
#pragma unroll 1
      for (int t = 0; t < 3 && !early; ++t) early = try_z();
      if (early) { ldB(0, sl2(0)); ldB(1, sl2(1)); ldB(4, sl2(4)); ldB(5, sl2(5)); }
      bar();
      if (!early) { early = try_z(); if (early) { ldB(0, sl2(0)); ldB(1, sl2(1)); ldB(4, sl2(4)); ldB(5, sl2(5)); } }
      bar();
    } else {
      gl4(gA + rot * 128, lds0 + lw * 4096, vA0, vA1, vA2, vA3);
      unsigned vB[3];
#pragma unroll
      for (int i = 0; i < 3; ++i) {
        const int c = i * 64 + l, row = c / 12, cc = c - row * 12;
        int pc = (cc >> 1) - (row >> 3); if (pc < 0) pc = 5;
        vB[i] = (unsigned)((row * FIN + cb1 * 96 + pc * 16 + (cc & 1) * 8) * 2) + 4096 - i * 1024;
      }
      const char* gB = (const char*)P.win + (size_t)lw * 16 * FIN * 2 - 4096;
      auto stageB = [&](int kt, unsigned sb) {
        const int k = krot(kt);
        gl3(gB + (kt < 10 ? FH * 2 : 0) + (size_t)k * 64 * FIN * 2, sb + 16384 + lw * 3072, vB[0], vB[1], vB[2]);
      };
      auto stage = [&](int kt, unsigned sb) {
        const int k = krot(kt);
        gl4(gA + k * 128, sb + lw * 4096, vA0, vA1, vA2, vA3); stageB(kt, sb);
      };
      stageB(0, lds0); stage(1, lds0 + KZ_STAGE); stage(2, lds0 + 2 * KZ_STAGE);
      wait_vm<14>(); bar();
      stage(3, lds0 + 3 * KZ_STAGE); stage(4, lds0 + 4 * KZ_STAGE);
      unsigned sb = lds0;
      auto runL = [&](int k0, int k1) {
#pragma unroll 1
        for (int kt = k0; kt < k1; ++kt) {
          wait_vm<21>(); bar();
          stage(kt + 4, sb);
          sb += KZ_STAGE; if (sb == lds0 + 5 * KZ_STAGE) sb = lds0;
        }
      };
      runL(1, 11);
      {
        const char* wp = (const char*)(P.wsp + (size_t)(panel1 * 128 + lw * 32) * S_) + (cb1 & 3) * 1024;
        const unsigned vo = (unsigned)((l >> 3) * S_ * 2 + (l & 7) * 128);
        unsigned d0, d1, d2, d3;
        asm volatile("global_load_dword %0, %4, %5\n\tglobal_load_dword %1, %4, %6\n\tglobal_load_dword %2, %4, %7\n\tglobal_load_dword %3, %4, %8"
                     : "=&v"(d0), "=&v"(d1), "=&v"(d2), "=&v"(d3)
                     : "v"(vo), "s"(wp), "s"(wp + 8 * S_ * 2), "s"(wp + 16 * S_ * 2), "s"(wp + 24 * S_ * 2) : "memory");
        runL(11, 16);
        asm volatile("" :: "v"(d0), "v"(d1), "v"(d2), "v"(d3));
      }
      wait_vm<21>(); bar(); wait_vm<14>(); bar(); wait_vm<7>(); bar(); wait_vm<0>(); bar();
      bar();
      wait_words(ws, zfl, l < 16, tag);
      early = true;
      ldA(0, sl2(0)); ldB(0, sl2(0)); ldA(1, sl2(1)); ldB(1, sl2(1));
      ldA(4, sl2(4)); ldA(5, sl2(5)); ldB(4, sl2(4)); ldB(5, sl2(5));
    }
    if (!early) {
      wait_words(ws, zfl, l < 16, tag);
      ldB(0, sl2(0)); ldB(1, sl2(1)); ldB(4, sl2(4)); ldB(5, sl2(5));
    }
    ldA(2, sl2(2)); ldA(3, sl2(3)); ldB(2, sl2(2)); ldB(3, sl2(3));
    wait_vm<16>(); bar();
    {
      unsigned sl = sl2(0);
      auto ldU = [&](int u) {
        ldA(2 * u, sl); ldB(2 * u, sl); ldA(2 * u + 1, sl + P2_SLOT); ldB(2 * u + 1, sl + P2_SLOT);
        sl += 2 * P2_SLOT; if (sl == lds0 + 6 * P2_SLOT) sl = lds0;
      };
      wait_vm<0>(); bar(); ldU(3);
#pragma unroll 1
      for (int u = 2; u < 14; ++u) {
        wait_vm<10>(); bar();
        ldU(u + 2);
      }
    }
    wait_vm<10>(); bar();
    wait_words(ws, WS_F1 + (((lw * 64 + l) >> 4) * 32 + 16 + (l & 15)) * 64, true, tag);
    wait_vm<0>(); bar();
    if (b == 0 && tid == 512) st8u(ws, WS_EP, tag);
    const char* gA3 = ws + WS_OH + (size_t)panel3 * 44 * 4096 + lw * 4096;
    const char* gB3 = ws + WS_WF + (size_t)cb3 * 44 * 5120 + lw * 5120;
    int r5 = cb3 + 2 * (bi & 3); r5 = r5 >= 10 ? r5 - 10 : r5 >= 5 ? r5 - 5 : r5;
    int r6 = cb3 + 2 * (bi & 3); r6 = r6 >= 12 ? r6 - 12 : r6 >= 6 ? r6 - 6 : r6;
    auto ur = [&](int u) {
      int v;
      if (u < 5) { v = u + r5; if (v >= 5) v -= 5; } else { v = u + r6; if (v >= 11) v -= 6; }
      return __builtin_amdgcn_readfirstlane(v);
    };
    auto ldB3 = [&](int u, unsigned sl) {
      gl4c(gB3 + ur(u) * 20480, sl + 16384 + lw * 5120, l16);
      glds16c(gB3 + ur(u) * 20480 + 4096, l16, sl + 16384 + lw * 5120 + 4096);
    };
    auto ldA3 = [&](int u, unsigned sl) { gl4c(gA3 + ur(u) * 16384, sl + lw * 4096, l16); };
    bar(); bar();
    ldB3(0, lds0); ldB3(1, lds0 + P3_SLOT); ldB3(2, lds0 + 2 * P3_SLOT);
    wait_words(ws, WS_FO + ((panel3 >> 1) * 8 + (l & 7)) * 64, l < 8, tag);
    ldA3(0, lds0); ldA3(1, lds0 + P3_SLOT); ldA3(2, lds0 + 2 * P3_SLOT);
    bar();
    ldA3(3, lds0 + 3 * P3_SLOT); ldB3(3, lds0 + 3 * P3_SLOT);
    wait_vm<17>(); bar();
    wait_vm<13>(); bar(); ldA3(4, lds0); ldB3(4, lds0);
    wait_vm<18>(); bar(); ldB3(5, lds0 + P3_SLOT);
    wait_vm<14>(); bar(); ldB3(6, lds0 + 2 * P3_SLOT);
    wait_vm<10>(); bar(); ldB3(7, lds0 + 3 * P3_SLOT);
    wait_words(ws, WS_F2 + (panel3 * 32 + (l & 31)) * 64, l < 32, tag);
    ldA3(5, lds0 + P3_SLOT); ldA3(6, lds0 + 2 * P3_SLOT); ldA3(7, lds0 + 3 * P3_SLOT);
    wait_vm<8>(); bar(); ldA3(8, lds0); ldB3(8, lds0);
    wait_vm<13>(); bar(); ldA3(9, lds0 + P3_SLOT); ldB3(9, lds0 + P3_SLOT);
    wait_vm<18>(); bar(); ldA3(10, lds0 + 2 * P3_SLOT); ldB3(10, lds0 + 2 * P3_SLOT);
    wait_vm<18>(); bar(); wait_vm<9>(); bar(); wait_vm<0>(); bar();
    bar(); bar();
    return;
  }

  const rsrc_t rws = __builtin_amdgcn_make_buffer_rsrc(ws, 0, (int)WS_END, 0x00020000);
  bf16x8 pf[3];
  if (isq) p1q_consumer(P, rws, w, panel1, cb1, lds0, tag, pf);
  else p1z_consumer(P, rws, w, panel1, cb1, lds0, tag);
  wf_job(P, rws, w);

  u32x4 xv[2];
  unsigned xo0, xo1, la0, la1;
  {
    const int wt = w & 3, kh = w >> 2;
    const int rsw = ((wt * 16 + fr) >> 1) & 7;
    const unsigned ao0 = (unsigned)((wt * 16 + fr) * 128 + (((4 * kh + (g >> 1)) ^ rsw) << 4) + (g & 1) * 8);
    const unsigned ao1 = (unsigned)((wt * 16 + fr) * 128 + (((4 * kh + 2 + (g >> 1)) ^ rsw) << 4) + (g & 1) * 8);
    const unsigned bo = (unsigned)(8192 + kh * 6144 + l * 16);
    const bf16x8 zeros = {0, 0, 0, 0, 0, 0, 0, 0};
    f32x4 acc[6];
#pragma unroll
    for (int n = 0; n < 6; ++n) acc[n] = (f32x4){0.f, 0.f, 0.f, 0.f};
    bf16x8 af[2], bfr[2][6];
    af[1] = zeros;
#pragma unroll
    for (int n = 0; n < 6; ++n) bfr[1][n] = zeros;
    const int zi = (bi & 15) * 8 + xcd;
    const bool reducer = !isq && zi < 72;
    const int rj = zi & 7, rf = zi >> 3;
    const int row = panel2 * 64 + wt * 16 + fr;
    u32x2 zreg[6];
    const float bs = bf2f(P.bsp[row]);
    unsigned sl = lds0 + 4 * P2_SLOT;
    auto step = [&](int cur, int kt) {
      if (cur == 0) bar();
      af[cur] = lds_2b64(sl + ao0, sl + ao1);
#pragma unroll
      for (int n = 0; n < 6; ++n) bfr[cur][n] = lds_b128(sl + bo + n * 1024);
      SB0;
#pragma unroll
      for (int n = 0; n < 6; ++n) acc[n] = MFMA(bfr[cur ^ 1][n], af[cur ^ 1], acc[n]);
      SB0;
      sl += P2_SLOT; if (sl == lds0 + 6 * P2_SLOT) sl = lds0;
    };
    auto run = [&](int k0, int k1) {
#pragma unroll 1
      for (int kt = k0; kt < k1; kt += 2) { step(0, kt); step(1, kt + 1); }
    };
    if (reducer) {
      const int et = (rf + 1) >> 1, st = rf == 0 ? 0 : ((rf + 1) & 1) + (et >= 3 ? 1 : 0);
      wait_words(ws, WS_F1 + ((l & 15) * 32 + rj) * 64, true, tag);
      f32x4 pv[8];
      bool okv[4];
#pragma unroll
      for (int t4 = 0; t4 < 4; ++t4) {
        const int i = 2 * st + (t4 & 1);
        okv[t4] = i < 5 && (t4 >= 2 || (et - i <= 1 && i - et <= 1));
        const int job = okv[t4] ? (t4 >= 2 ? 13 + i : 2 * i + et) : 0;
#pragma unroll
        for (int pp = 0; pp < 2; ++pp)
          pv[t4 * 2 + pp] = __builtin_bit_cast(f32x4, ld16(rws, WS_KVP + (unsigned)(((rj * 18 + job) * 16 + w * 2 + pp) * 1024) + l * 16));
      }
      run(0, 2);
      if (tid == 0) st8u(ws, WS_F1 + (panel1 * 32 + 16 + cb1) * 64, tag);
      const unsigned L_R = lds0 + 122880;
      f32x4 sv[4];
#pragma unroll
      for (int t4 = 0; t4 < 4; ++t4) { sv[t4] = pv[t4 * 2] + pv[t4 * 2 + 1]; if (!okv[t4]) sv[t4] = (f32x4){0.f, 0.f, 0.f, 0.f}; }
      if (w >= 4) {
#pragma unroll
        for (int t4 = 0; t4 < 4; ++t4) stsf4(L_R + ((w - 4) * 4 + t4) * 1024 + l * 16, sv[t4]);
      }
      run(2, 4);
      if (w < 4) {
#pragma unroll
        for (int t4 = 0; t4 < 4; ++t4) stsf4(L_R + (w * 4 + t4) * 1024 + l * 16, sv[t4] + ldsf4(L_R + (w * 4 + t4) * 1024 + l * 16));
      }
      run(4, 6);
      if (w < 2) {
        f32x4 v[2];
#pragma unroll
        for (int h2 = 0; h2 < 2; ++h2) {
          v[h2] = (f32x4){0.f, 0.f, 0.f, 0.f};
#pragma unroll
          for (int ww = 0; ww < 4; ++ww) v[h2] += ldsf4(L_R + (ww * 4 + w * 2 + h2) * 1024 + l * 16);
          const int he = (et * 16 + fr) / 20;
#pragma unroll
          for (int r = 0; r < 4; ++r) { const int d = (2 * st + h2) * 16 + 4 * g + r; if (d / 20 != he) v[h2][r] = 0.f; }
        }
        st16(rws, WS_KVT + (unsigned)(((rj * 2 + w) * 9 + rf) * 1024) + l * 16,
             (u32x4){cvt_pk(v[0][0], v[0][1]), cvt_pk(v[0][2], v[0][3]), cvt_pk(v[1][0], v[1][1]), cvt_pk(v[1][2], v[1][3])});
      }
      run(6, 10);
      wait_vm<0>();
      if (w < 2 && l == 0) st8u(ws, WS_FK + (rj * 32 + w * 16 + rf) * 64, tag);
      run(10, 18);
    } else {
      run(0, 6);
      wait_vm<0>();
      run(6, 8);
      if (tid == 0) st8u(ws, WS_F1 + (panel1 * 32 + (isq ? 24 : 16) + cb1) * 64, tag);
      run(8, 18);
    }
    if (isq) {
      const unsigned foff = (l & 15) < 9 ? WS_FK + (cb2 * 32 + (l & 31)) * 64 : WS_F1 + (panel1 * 32 + 16 + cb2) * 64;
      const u64 fv = ld8(ws, foff);
      run(18, 22);
      if (!__all(fv == tag)) wait_words(ws, foff, l < 32, tag);
#pragma unroll
      for (int i = 0; i < 3; ++i) {
        int ins = w + 8 * i; if (ins >= 18) ins = 17;
        ins = __builtin_amdgcn_readfirstlane(ins);
        glds16c(ws, WS_KVT + cb2 * 18432 + ins * 1024 + l * 16, __builtin_amdgcn_readfirstlane(lds0 + L_KVT + ins * 1024));
      }
      const unsigned zo = WS_Z1 + (unsigned)(((panel1 * 8 + cb2) * 4 + 2 + (wt >> 1)) * 6) * 1024 + l * 16 + (wt & 1) * 8;
#pragma unroll
      for (int n = 0; n < 6; ++n) zreg[n] = __builtin_amdgcn_raw_buffer_load_b64(rws, zo + n * 1024, 0, 16);
      run(22, 24);
      wait_vm<0>();
      run(24, 26);
      u32x2 ov[5];
#pragma unroll
      for (int et = 0; et < 5; ++et) {
        f32x4 num = {0.f, 0.f, 0.f, 0.f}, den = {0.f, 0.f, 0.f, 0.f};
        const int st0 = et >= 3 ? 1 : 0, nst = et == 0 ? 1 : 2, f0 = et == 0 ? 0 : 1 + (et - 1) * 2;
#pragma unroll
        for (int s = 0; s < nst; ++s) {
          bf16x8 ka = lds_b128(lds0 + L_KVT + (f0 + s) * 1024 + l * 16), za = lds_b128(lds0 + L_KVT + (9 + f0 + s) * 1024 + l * 16);
          num = MFMA(ka, pf[st0 + s], num); den = MFMA(za, pf[st0 + s], den);
        }
        float v[4];
#pragma unroll
        for (int j = 0; j < 4; ++j) v[j] = num[j] * __builtin_amdgcn_rcpf(den[j] + 1e-6f);
        ov[et] = (u32x2){cvt_pk(v[0], v[1]), cvt_pk(v[2], v[3])};
      }
      const unsigned ob = WS_OH + (unsigned)(((2 * panel1 + kh) * 44 + ((cb2 * 5) >> 1)) * 4 + wt) * 1024 + l * 16;
      if ((cb2 & 1) == 0) {
        st16(rws, ob, (u32x4){ov[0][0], ov[0][1], ov[1][0], ov[1][1]});
        st16(rws, ob + 4096, (u32x4){ov[2][0], ov[2][1], ov[3][0], ov[3][1]});
        st8(ws, ob + 8192, ov[4]);
      } else {
        st8(ws, ob + 8, ov[0]);
        st16(rws, ob + 4096, (u32x4){ov[1][0], ov[1][1], ov[2][0], ov[2][1]});
        st16(rws, ob + 8192, (u32x4){ov[3][0], ov[3][1], ov[4][0], ov[4][1]});
      }
      run(26, 30);
      wait_vm<0>();
      run(30, 32);
      if (tid == 0) st8u(ws, WS_FO + (panel1 * 8 + cb2) * 64, tag);
    } else {
      run(18, 32);
#pragma unroll
      for (int n = 0; n < 6; ++n) zreg[n] = *(LAS u32x2*)(uintptr_t)(lds0 + L_KVT + 6144 + ((wt >> 1) * 6 + n) * 1024 + l * 16 + (wt & 1) * 8);
    }
#pragma unroll
    for (int n = 0; n < 6; ++n) acc[n] = MFMA(bfr[1][n], af[1], acc[n]);
    {
      const int r0 = tid / 10, h0 = tid - r0 * 10, c1 = (tid & 127) + 512, r1 = c1 / 10, h1 = c1 - r1 * 10;
      xo0 = (unsigned)((panel3 * 64 + r0) * D_ + cb3 * 80 + h0 * 8); xo1 = (unsigned)((panel3 * 64 + r1) * D_ + cb3 * 80 + h1 * 8);
      la0 = lds0 + (unsigned)((r0 * 84 + h0 * 8) * 4); la1 = lds0 + (unsigned)((r1 * 84 + h1 * 8) * 4);
      xv[0] = *(const u32x4*)(P.x + xo0); xv[1] = *(const u32x4*)(P.x + xo1);
    }
    bar();
    const unsigned L_RED = lds0 + 3 * P3_SLOT;
    if (kh == 1) {
#pragma unroll
      for (int n = 0; n < 6; ++n) stsf4(L_RED + (wt * 6 + n) * 1024 + l * 16, acc[n]);
    }
    bar();
    if (kh == 0) {
#pragma unroll
      for (int n = 0; n < 6; ++n) acc[n] += ldsf4(L_RED + (wt * 6 + n) * 1024 + l * 16);
      u32x2 hv[6];
#pragma unroll
      for (int n = 0; n < 6; ++n) {
        f32x4 gg = acc[n] + bs;
        hv[n] = (u32x2){cvt_pk(lo16(zreg[n][0]) * gg[0], hi16(zreg[n][0]) * gg[1]), cvt_pk(lo16(zreg[n][1]) * gg[2], hi16(zreg[n][1]) * gg[3])};
      }
      const unsigned hb = WS_OH + (unsigned)((panel2 * 44 + 20 + cb2 * 3) * 4 + wt) * 1024 + l * 16;
#pragma unroll
      for (int s = 0; s < 3; ++s) st16(rws, hb + s * 4096, (u32x4){hv[2 * s][0], hv[2 * s][1], hv[2 * s + 1][0], hv[2 * s + 1][1]});
      wait_vm<0>();
      if (l == 0) st8u(ws, WS_F2 + ((panel2 * 8 + cb2) * 4 + wt) * 64, tag);
    }
    bar();
  }
  {
    const int wsr = w & 3, kh = w >> 2;
    const unsigned ao = (unsigned)(kh * 8192 + wsr * 1024 + l * 16);
    const unsigned bb = 16384u + kh * 10240 + l * 16;
    const bf16x8 zeros = {0, 0, 0, 0, 0, 0, 0, 0};
    f32x4 acc[5];
#pragma unroll
    for (int n = 0; n < 5; ++n) acc[n] = (f32x4){0.f, 0.f, 0.f, 0.f};
    bf16x8 af[2][2], bfr[2][2][5];
    unsigned sb = lds0;
    auto mm = [&](int c) {
#pragma unroll
      for (int s2 = 0; s2 < 2; ++s2)
#pragma unroll
        for (int n = 0; n < 5; ++n) acc[n] = MFMA(bfr[c][s2][n], af[c][s2], acc[n]);
      SB0;
    };
    auto rdu = [&](int c) {
      bar();
#pragma unroll
      for (int s2 = 0; s2 < 2; ++s2) {
        af[c][s2] = lds_b128(sb + ao + s2 * 4096);
#pragma unroll
        for (int n = 0; n < 5; ++n) bfr[c][s2][n] = lds_b128(sb + bb + s2 * 5120 + n * 1024);
      }
      SB0;
      sb += P3_SLOT; if (sb == lds0 + 4 * P3_SLOT) sb = lds0;
    };
    rdu(0);
#pragma unroll 1
    for (int u = 0; u < 5; ++u) { rdu(1); mm(0); rdu(0); mm(1); }
    mm(0);
    bar();
#pragma unroll
    for (int n = 0; n < 5; ++n) stsf4(lds0 + (unsigned)(((kh * 64 + wsr * 16 + fr) * 84 + n * 16 + 4 * g) * 4), acc[n]);
    bar();
#pragma unroll
    for (int i = 0; i < 2; ++i) {
      if (i == 0 || tid < 128) {
        const unsigned a = i == 0 ? la0 : la1;
        f32x4 a0 = ldsf4(a) + ldsf4(a + 64 * 84 * 4), a1 = ldsf4(a + 16) + ldsf4(a + 64 * 84 * 4 + 16);
        u32x4 o = {cvt_pk(lo16(xv[i][0]) + a0[0], hi16(xv[i][0]) + a0[1]), cvt_pk(lo16(xv[i][1]) + a0[2], hi16(xv[i][1]) + a0[3]),
                   cvt_pk(lo16(xv[i][2]) + a1[0], hi16(xv[i][2]) + a1[1]), cvt_pk(lo16(xv[i][3]) + a1[2], hi16(xv[i][3]) + a1[3])};
        *(u32x4*)(P.out + (i == 0 ? xo0 : xo1)) = o;
      }
    }
  }
}

extern "C" void kernel_launch(void* const* d_in, const int* in_sizes, int n_in, void* d_out, int out_size,
                              void* d_ws, size_t ws_size, hipStream_t stream) {
  Params P;
  P.x = (const u16*)d_in[0];
  P.wqkv = (const u16*)d_in[1];
  P.wo = (const u16*)d_in[2];
  P.win = (const u16*)d_in[4];
  P.wsp = (const u16*)d_in[5];
  P.bsp = (const u16*)d_in[6];
  P.wout = (const u16*)d_in[7];
  P.out = (u16*)d_out;
  P.ws = (char*)d_ws;
  (void)hipFuncSetAttribute((const void*)mega_kernel, hipFuncAttributeMaxDynamicSharedMemorySize, LDS_TOTAL);
  mega_kernel<<<256, 768, LDS_TOTAL, stream>>>(P);
}
```
